```python
import torch

def _softmax(x, axis=-1):
    return torch.softmax(x, dim=axis)

def _gelu(x):
    return torch.nn.functional.gelu(x, approximate='tanh')

def _silu(x):
    return torch.nn.functional.silu(x)

def _sigmoid(x):
    return torch.sigmoid(x)

def _layernorm(x, axis=-1, eps=1e-05):
    m = x.mean(dim=axis, keepdim=True)
    v = x.var(dim=axis, keepdim=True, unbiased=False)
    return (x - m) / torch.sqrt(v + eps)

def _scatter_add(tgt, idx, src, axis=0):
    return tgt.clone().index_add_(axis, idx, src)

def _segment_sum(src, seg, n):
    out = torch.zeros((n,) + src.shape[1:], dtype=src.dtype)
    return out.index_add_(0, seg, src)

def _pad(x, pw):
    flat = tuple((v for pair in reversed(pw) for v in pair))
    return torch.nn.functional.pad(x, flat)
kernel_name = 'adaLN_cond__mha_block__glu_gated_geglu__prompt'

def setup_inputs():

    _w = lambda a: (lambda t: t.to(torch.bfloat16) if t.is_floating_point() else t)(torch.as_tensor(a))

    def _A_setup():
        B, S, D, H = (1, 2048, 448, 16)
        return {'x': _w((lambda *s: torch.randn(*s, dtype=torch.bfloat16) * 0.05)(B, S, D)), 'wqkv': _w((lambda *s: torch.randn(*s, dtype=torch.bfloat16) * 0.05)(D, 3 * D)), 'wo': _w((lambda *s: torch.randn(*s, dtype=torch.bfloat16) * 0.05)(D, D))}
    _A_in = _A_setup()

    def _B_setup():
        B, S, Dm, Df = (1, 2048, 448, 1792)
        return {'x': _w((lambda *s: torch.randn(*s, dtype=torch.bfloat16) * 0.05)(B, S, Dm)), 'w_gate': _w((lambda *s: torch.randn(*s, dtype=torch.bfloat16) * 0.8)(Dm, Df) * Dm ** (-0.5)), 'w_up': _w((lambda *s: torch.randn(*s, dtype=torch.bfloat16) * 0.8)(Dm, Df) * Dm ** (-0.5)), 'w_down': _w((lambda *s: torch.randn(*s, dtype=torch.bfloat16) * 0.8)(Df, Dm) * Df ** (-0.5))}
    _B_in = _B_setup()
    return {'A_x': _A_in['x'], 'A_wqkv': _A_in['wqkv'], 'A_wo': _A_in['wo'], 'B_x': _B_in['x'], 'B_w_gate': _B_in['w_gate'], 'B_w_up': _B_in['w_up'], 'B_w_down': _B_in['w_down'], 'c': _w((lambda *s: torch.randn(*s, dtype=torch.bfloat16) * 0.1)(1, 448) * 8.0), 'w_mod': _w((lambda *s: torch.randn(*s, dtype=torch.bfloat16) * 0.1)(448, 6 * 448) * 448 ** (-0.5) * 8.0)}

def reference(A_x, A_wqkv, A_wo, B_x, B_w_gate, B_w_up, B_w_down, c, w_mod):

    def _A_ref(x, wqkv, wo):
        B, S, D = x.shape
        H = 16
        Dh = D // H
        qkv = (lambda x, *d: x.permute(*d))((x @ wqkv).reshape(B, S, 3, H, Dh), 2, 0, 3, 1, 4)
        q, k, v = (qkv[0], qkv[1], qkv[2])
        scores = torch.einsum('bhqd,bhkd->bhqk', q, k) * Dh ** (-0.5)
        p = _softmax(scores, axis=-1)
        o = torch.einsum('bhqk,bhkd->bhqd', p, v)
        return (lambda x, *d: x.permute(*d))(o, 0, 2, 1, 3).reshape(B, S, D) @ wo

    def _B_ref(x, w_gate, w_up, w_down):
        g = torch.einsum('bsd,df->bsf', x, w_gate)
        u = torch.einsum('bsd,df->bsf', x, w_up)
        h = _gelu(g) * u
        return torch.einsum('bsf,fd->bsd', h, w_down)
    x = A_x
    mod = (_silu(c) @ w_mod).reshape(c.shape[0], 6, 1, -1)
    sa, ca, ga, sb, cb, gb = (mod[:, 0].clone(), mod[:, 1].clone(), mod[:, 2].clone(), mod[:, 3].clone(), mod[:, 4].clone(), mod[:, 5].clone())
    h = _layernorm(x, axis=-1) * (1 + ca) + sa
    x = x + ga * _A_ref(h, A_wqkv, A_wo)
    h = _layernorm(x, axis=-1) * (1 + cb) + sb
    x = x + gb * _B_ref(h, B_w_gate, B_w_up, B_w_down)
    return x
```

```cpp
#include <hip/hip_runtime.h>
#include <hip/hip_bf16.h>
typedef __hip_bfloat16 bf16;
constexpr int S=2048, D=448, H=16, DH=28, DF=1792;
__device__ inline float b2f(bf16 x){return __bfloat162float(x);}
__global__ void k_mod(const bf16* c, const bf16* w, float* mod){
  int j=blockIdx.x*64+threadIdx.x; float a=0;
  for(int k=0;k<D;k++){ float cv=b2f(c[k]); float s=cv/(1.f+__expf(-cv)); a+=s*b2f(w[k*6*D+j]); }
  mod[j]=a;
}
__global__ void k_ln(const bf16* xb, const float* xf, const float* shift, const float* scale, float* h){
  int r=blockIdx.x; int t=threadIdx.x; __shared__ float sm[D]; __shared__ float red[2];
  for(int i=t;i<D;i+=64) sm[i]= xb? b2f(xb[r*D+i]) : xf[r*D+i];
  __syncthreads();
  if(t==0){ float m=0; for(int i=0;i<D;i++) m+=sm[i]; m/=D; float v=0; for(int i=0;i<D;i++) v+=(sm[i]-m)*(sm[i]-m); v/=D; red[0]=m; red[1]=rsqrtf(v+1e-5f);}
  __syncthreads();
  for(int i=t;i<D;i+=64) h[r*D+i]=(sm[i]-red[0])*red[1]*(1.f+scale[i])+shift[i];
}
__global__ void k_gemm(const float* A, const bf16* W, float* C, int K, int N){
  int n=blockIdx.x*64+threadIdx.x; int r=blockIdx.y; float a=0;
  for(int k=0;k<K;k++) a+=A[r*K+k]*b2f(W[k*N+n]);
  C[r*N+n]=a;
}
__global__ void k_attn(const float* qkv, float* o){
  int q=blockIdx.x*64+threadIdx.x; int h=blockIdx.y;
  float qv[DH]; for(int d=0;d<DH;d++) qv[d]=qkv[q*3*D+h*DH+d]*0.18898223650461363f;
  float m=-1e30f,l=0; float acc[DH]; for(int d=0;d<DH;d++) acc[d]=0;
  for(int k=0;k<S;k++){ float s=0; for(int d=0;d<DH;d++) s+=qv[d]*qkv[k*3*D+D+h*DH+d];
    float mn=fmaxf(m,s); float al=__expf(m-mn); float p=__expf(s-mn); l=l*al+p;
    for(int d=0;d<DH;d++) acc[d]=acc[d]*al+p*qkv[k*3*D+2*D+h*DH+d]; m=mn; }
  for(int d=0;d<DH;d++) o[q*D+h*DH+d]=acc[d]/l;
}
__global__ void k_res1(const bf16* x, const float* g, const float* a, float* x2){ int i=blockIdx.x*256+threadIdx.x; x2[i]=b2f(x[i])+g[i%D]*a[i]; }
__global__ void k_act(const float* g, const float* u, float* h){ int i=blockIdx.x*256+threadIdx.x; float x=g[i]; float t=tanhf(0.7978845608028654f*(x+0.044715f*x*x*x)); h[i]=0.5f*x*(1.f+t)*u[i]; }
__global__ void k_res2(const float* x2, const float* g, const float* a, bf16* out){ int i=blockIdx.x*256+threadIdx.x; out[i]=__float2bfloat16(x2[i]+g[i%D]*a[i]); }
extern "C" void kernel_launch(void* const* d_in, const int* in_sizes, int n_in, void* d_out, int out_size, void* d_ws, size_t ws_size, hipStream_t stream) {
  const bf16* x=(const bf16*)d_in[0]; const bf16* wqkv=(const bf16*)d_in[1]; const bf16* wo=(const bf16*)d_in[2];
  const bf16* wg=(const bf16*)d_in[4]; const bf16* wu=(const bf16*)d_in[5]; const bf16* wd=(const bf16*)d_in[6];
  const bf16* c=(const bf16*)d_in[7]; const bf16* wm=(const bf16*)d_in[8];
  float* ws=(float*)d_ws; float* mod=ws; float* h=ws+4096; float* qkv=h+S*D; float* o=qkv+S*3*D; float* a=o+S*D; float* x2=a+S*D; float* g=x2+S*D; float* u=g+S*DF; float* hh=u+S*DF;
  k_mod<<<6*D/64,64,0,stream>>>(c,wm,mod);
  k_ln<<<S,64,0,stream>>>(x,nullptr,mod,mod+D,h);
  k_gemm<<<dim3(3*D/64,S),64,0,stream>>>(h,wqkv,qkv,D,3*D);
  k_attn<<<dim3(S/64,H),64,0,stream>>>(qkv,o);
  k_gemm<<<dim3(D/64,S),64,0,stream>>>(o,wo,a,D,D);
  k_res1<<<S*D/256,256,0,stream>>>(x,mod+2*D,a,x2);
  k_ln<<<S,64,0,stream>>>(nullptr,x2,mod+3*D,mod+4*D,h);
  k_gemm<<<dim3(DF/64,S),64,0,stream>>>(h,wg,g,D,DF);
  k_gemm<<<dim3(DF/64,S),64,0,stream>>>(h,wu,u,D,DF);
  k_act<<<S*DF/256,256,0,stream>>>(g,u,hh);
  k_gemm<<<dim3(D/64,S),64,0,stream>>>(hh,wd,a,DF,D);
  k_res2<<<S*D/256,256,0,stream>>>(x2,mod+5*D,a,(bf16*)d_out);
}
```

```cpp
#include <hip/hip_runtime.h>
#include <hip/hip_bf16.h>
#include <stdint.h>

typedef __attribute__((ext_vector_type(8))) __bf16 bf16x8;
typedef __attribute__((ext_vector_type(2))) __bf16 bf16x2;
typedef __attribute__((ext_vector_type(16))) float f32x16;
typedef __attribute__((ext_vector_type(4))) float f32x4;
typedef __attribute__((ext_vector_type(2))) float f32x2;
typedef __attribute__((ext_vector_type(4))) unsigned u32x4;
typedef __attribute__((ext_vector_type(2))) unsigned u32x2;
typedef __attribute__((ext_vector_type(4))) short s16x4;
#define DI __device__ __forceinline__
#define LAS __attribute__((address_space(3)))
#define MFMA(a, b, c) __builtin_amdgcn_mfma_f32_32x32x16_bf16(__builtin_bit_cast(bf16x8, (a)), __builtin_bit_cast(bf16x8, (b)), (c), 0, 0, 0)

constexpr int S = 2048, D = 448, H = 16, DH = 28, DF = 1792;
constexpr int NT = S / 32;
constexpr int KS = D / 16;
constexpr size_t OFF_MOD = 0;
constexpr size_t OFF_KN  = 16384;
constexpr size_t OFF_N1  = 32768;
constexpr size_t OFF_WQ  = OFF_N1 + (size_t)NT * KS * 1024;
constexpr size_t OFF_WO  = OFF_WQ + (size_t)H * KS * 3 * 1024;
constexpr size_t OFF_WGU = OFF_WO + (size_t)7 * 32 * 2 * 1024;
constexpr size_t OFF_WD  = OFF_WGU + (size_t)16 * KS * 7 * 1024;
constexpr size_t OFF_Q   = OFF_WD + (size_t)7 * 112 * 2 * 1024;
constexpr size_t OFF_KV  = OFF_Q + (size_t)H * NT * 2 * 1024;
constexpr size_t OFF_O   = OFF_KV + (size_t)H * NT * 4 * 1024;
constexpr size_t OFF_X2  = OFF_O + (size_t)NT * 32 * 1024;
constexpr size_t OFF_ST  = OFF_X2 + (size_t)NT * KS * 1024;
constexpr size_t OFF_HM  = OFF_ST + (size_t)NT * 14 * 32 * 8;

DI unsigned pk2(float lo, float hi) { f32x2 v = {lo, hi}; return __builtin_bit_cast(unsigned, __builtin_convertvector(v, bf16x2)); }
DI float bflo(unsigned u) { return __uint_as_float(u << 16); }
DI float bfhi(unsigned u) { return __uint_as_float(u & 0xffff0000u); }
DI float rbf(float x) { return bflo(pk2(x, 0.f) & 0xffffu); }
DI u32x2 trread(const char* p) { return __builtin_bit_cast(u32x2, __builtin_amdgcn_ds_read_tr16_b64_v4i16((LAS s16x4*)p)); }
DI float xor32(float v) { return __shfl_xor(v, 32); }
DI u32x4 pack8(const f32x16& a, int s) {
  u32x4 r; r[0] = pk2(a[8*s], a[8*s+1]); r[1] = pk2(a[8*s+2], a[8*s+3]); r[2] = pk2(a[8*s+4], a[8*s+5]); r[3] = pk2(a[8*s+6], a[8*s+7]); return r; }

constexpr int K0_MOD = 42, K0_WQ = 42, K0_LN = 64, K0_WO = 16, K0_WGU = 98, K0_WD = 56;
__global__ __launch_bounds__(256) void k0_prep(const uint16_t* __restrict__ x, const uint16_t* __restrict__ wqkv, const uint16_t* __restrict__ wo,
                                               const uint16_t* __restrict__ wg, const uint16_t* __restrict__ wu, const uint16_t* __restrict__ wd,
                                               const uint16_t* __restrict__ cvec, const uint16_t* __restrict__ wmod, char* __restrict__ ws) {
  __shared__ __attribute__((aligned(16))) char lds[2 * 32 * 576];
  const int t = threadIdx.x, lane = t & 63, wave = t >> 6;
  int b = blockIdx.x;
  const int hh = lane >> 5, blk = (lane >> 4) & 1, q = (lane & 15) >> 2, p = lane & 3;
  if (b < K0_MOD) {
    const int cc = t & 7, rg = t >> 3;
    u32x4 w[14]; float sc[14];
#pragma unroll
    for (int i = 0; i < 14; ++i) w[i] = *(const u32x4*)(wmod + (size_t)(rg + 32 * i) * (6 * D) + b * 64 + cc * 8);
#pragma unroll
    for (int i = 0; i < 14; ++i) { float c = bflo(cvec[rg + 32 * i]); sc[i] = rbf(c / (1.f + __expf(-c))); }
    float acc[8] = {0, 0, 0, 0, 0, 0, 0, 0};
#pragma unroll
    for (int i = 0; i < 14; ++i)
#pragma unroll
      for (int j = 0; j < 4; ++j) { acc[2*j] += sc[i] * bflo(w[i][j]); acc[2*j+1] += sc[i] * bfhi(w[i][j]); }
#pragma unroll
    for (int j = 0; j < 8; ++j) { acc[j] += __shfl_xor(acc[j], 8); acc[j] += __shfl_xor(acc[j], 16); acc[j] += __shfl_xor(acc[j], 32); }
    float* red = (float*)lds;
    if (lane < 8) {
#pragma unroll
      for (int j = 0; j < 8; ++j) red[wave * 64 + lane * 8 + j] = acc[j];
    }
    __syncthreads();
    if (t < 64) {
      float v = rbf(red[t] + red[64 + t] + red[128 + t] + red[192 + t]);
      const int col = b * 64 + t, seg = col / D;
      if (seg == 1 || seg == 4) v = rbf(1.f + v);
      ((float*)(ws + OFF_MOD))[col] = v;
    }
    return;
  }
  b -= K0_MOD;
  if (b < K0_WQ) {
    const int kb = b / 3, part = b % 3;
    u32x4 v[7];
#pragma unroll
    for (int i = 0; i < 7; ++i) { const int id = t + 256 * i, r = id / 56, ch = id % 56; v[i] = *(const u32x4*)(wqkv + (size_t)(kb * 32 + r) * (3 * D) + part * D + ch * 8); }
#pragma unroll
    for (int i = 0; i < 7; ++i) { const int id = t + 256 * i, r = id / 56, ch = id % 56; *(u32x4*)(lds + r * 960 + ch * 16) = v[i]; }
    __syncthreads();
#pragma unroll
    for (int i = 0; i < 8; ++i) {
      const int h = wave * 4 + (i >> 1), s = i & 1;
      const char* a = lds + (16 * s + 8 * hh + q) * 960 + (h * DH + 16 * blk + 4 * p) * 2;
      u32x2 lo = trread(a), hi = trread(a + 4 * 960);
      u32x4 o = {lo[0], lo[1], hi[0], hi[1]};
      if ((lane & 31) >= DH) o = (u32x4){0, 0, 0, 0};
      *(u32x4*)(ws + OFF_WQ + ((size_t)(h * KS + kb * 2 + s) * 3 + part) * 1024 + lane * 16) = o;
    }
    return;
  }
  b -= K0_WQ;
  if (b < K0_LN) {
    const int row = b * 32 + wave * 8 + (lane >> 3), c8 = lane & 7;
    u32x4 v[7];
#pragma unroll
    for (int i = 0; i < 7; ++i) v[i] = *(const u32x4*)(x + (size_t)row * D + (c8 + 8 * i) * 8);
    float sum = 0;
#pragma unroll
    for (int i = 0; i < 7; ++i)
#pragma unroll
      for (int j = 0; j < 4; ++j) sum += bflo(v[i][j]) + bfhi(v[i][j]);
    sum += __shfl_xor(sum, 1); sum += __shfl_xor(sum, 2); sum += __shfl_xor(sum, 4);
    const float mean = sum * (1.f / D);
    float sq = 0;
#pragma unroll
    for (int i = 0; i < 7; ++i)
#pragma unroll
      for (int j = 0; j < 4; ++j) { float a = bflo(v[i][j]) - mean, c = bfhi(v[i][j]) - mean; sq += a * a + c * c; }
    sq += __shfl_xor(sq, 1); sq += __shfl_xor(sq, 2); sq += __shfl_xor(sq, 4);
    const float rstd = __builtin_amdgcn_rsqf(sq * (1.f / D) + 1e-5f);
#pragma unroll
    for (int i = 0; i < 7; ++i) {
      u32x4 o;
#pragma unroll
      for (int j = 0; j < 4; ++j) o[j] = pk2((bflo(v[i][j]) - mean) * rstd, (bfhi(v[i][j]) - mean) * rstd);
      const int ch = c8 + 8 * i;
      *(u32x4*)(ws + OFF_N1 + ((size_t)(b * KS + (ch >> 1)) * 64 + (ch & 1) * 32 + (row & 31)) * 16) = o;
    }
    return;
  }
  b -= K0_LN;
  if (b < K0_WO + K0_WD) {
    const bool iso = b < K0_WO;
    const int u = iso ? b : b - K0_WO;
    const uint16_t* src = iso ? wo + (size_t)u * DH * D : wd + (size_t)u * 32 * D;
    const int nch = iso ? DH * 56 : 32 * 56;
    u32x4 v[7];
#pragma unroll
    for (int i = 0; i < 7; ++i) { const int id = t + 256 * i; v[i] = *(const u32x4*)(src + (size_t)(id < nch ? id : 0) * 8); }
#pragma unroll
    for (int i = 0; i < 7; ++i) { const int id = t + 256 * i, r = id / 56, ch = id % 56; *(u32x4*)(lds + r * 960 + ch * 16) = v[i]; }
    __syncthreads();
    char* dst = ws + (iso ? OFF_WO : OFF_WD);
    const int nks = iso ? 32 : 112;
#pragma unroll
    for (int i = 0; i < 7; ++i) {
      const int f = wave * 7 + i, c = f >> 1, s = f & 1;
      const char* a = lds + (16 * s + 4 * hh + q) * 960 + (c * 32 + 16 * blk + 4 * p) * 2;
      u32x2 lo = trread(a), hi = trread(a + 8 * 960);
      if (iso && s == 1 && hh == 1) hi = (u32x2){0, 0};
      u32x4 o = {lo[0], lo[1], hi[0], hi[1]};
      *(u32x4*)(dst + ((size_t)((c >> 1) * nks + u * 2 + s) * 2 + (c & 1)) * 1024 + lane * 16) = o;
    }
    return;
  }
  b -= K0_WO + K0_WD;
  {
    const int kb = b / 7, cg = b % 7;
    u32x4 v[8];
#pragma unroll
    for (int i = 0; i < 8; ++i) { const int id = t + 256 * (i & 3), r = id >> 5, ch = id & 31; v[i] = *(const u32x4*)((i < 4 ? wg : wu) + (size_t)(kb * 32 + r) * DF + cg * 256 + ch * 8); }
#pragma unroll
    for (int i = 0; i < 8; ++i) { const int id = t + 256 * (i & 3), r = id >> 5, ch = id & 31; *(u32x4*)(lds + (i >> 2) * (32 * 576) + r * 576 + ch * 16) = v[i]; }
    __syncthreads();
#pragma unroll
    for (int i = 0; i < 8; ++i) {
      const int tl = wave * 4 + (i >> 1), s = i & 1, T = cg * 16 + tl;
      const char* a = lds + blk * (32 * 576) + (16 * s + 4 * hh + q) * 576 + (tl * 16 + 4 * p) * 2;
      u32x2 lo = trread(a), hi = trread(a + 8 * 576);
      u32x4 o = {lo[0], lo[1], hi[0], hi[1]};
      *(u32x4*)(ws + OFF_WGU + ((size_t)((T / 7) * KS + kb * 2 + s) * 7 + (T % 7)) * 1024 + lane * 16) = o;
    }
  }
}

__global__ __launch_bounds__(256, 1) void k1_qkv(char* __restrict__ ws) {
  extern __shared__ __attribute__((aligned(16))) char lds[];
  const int t = threadIdx.x, lane = t & 63, wave = __builtin_amdgcn_readfirstlane(t >> 6), hh = lane >> 5;
  const int xcd = blockIdx.x & 7, j = blockIdx.x >> 3, h = 2 * xcd + (j & 1), rb = j >> 1, tile = rb * 4 + wave;
  const char* wsrc = ws + OFF_WQ + (size_t)h * KS * 3 * 1024 + lane * 16;
  const char* nsrc = ws + OFF_N1 + (size_t)tile * KS * 1024 + lane * 16;
  u32x4 w[21], n[KS];
  float* modl = (float*)(lds + 84 * 1024);
  f32x4 mv;
  if (t < 224) mv = ((const f32x4*)(ws + OFF_MOD))[t];
#pragma unroll
  for (int i = 0; i < 21; ++i) w[i] = *(const u32x4*)(wsrc + (i * 4 + wave) * 1024);
#pragma unroll
  for (int i = 0; i < KS; ++i) n[i] = *(const u32x4*)(nsrc + i * 1024);
  if (t < 224) ((f32x4*)modl)[t] = mv;
#pragma unroll
  for (int i = 0; i < 21; ++i) *(u32x4*)(lds + (i * 4 + wave) * 1024 + lane * 16) = w[i];
  __syncthreads();
  f32x16 aq = {}, ak = {}, av = {};
#pragma unroll
  for (int s = 0; s < KS; ++s) {
    const f32x4* sh = (const f32x4*)(modl + s * 16 + hh * 8);
    const f32x4* sc = (const f32x4*)(modl + D + s * 16 + hh * 8);
    const f32x4 sh0 = sh[0], sh1 = sh[1], sc0 = sc[0], sc1 = sc[1];
    u32x4 hf;
    hf[0] = pk2(bflo(n[s][0]) * sc0[0] + sh0[0], bfhi(n[s][0]) * sc0[1] + sh0[1]);
    hf[1] = pk2(bflo(n[s][1]) * sc0[2] + sh0[2], bfhi(n[s][1]) * sc0[3] + sh0[3]);
    hf[2] = pk2(bflo(n[s][2]) * sc1[0] + sh1[0], bfhi(n[s][2]) * sc1[1] + sh1[1]);
    hf[3] = pk2(bflo(n[s][3]) * sc1[2] + sh1[2], bfhi(n[s][3]) * sc1[3] + sh1[3]);
    const u32x4 wq = *(const u32x4*)(lds + (s * 3 + 0) * 1024 + lane * 16);
    const u32x4 wk = *(const u32x4*)(lds + (s * 3 + 1) * 1024 + lane * 16);
    const u32x4 wv = *(const u32x4*)(lds + (s * 3 + 2) * 1024 + lane * 16);
    aq = MFMA(wq, hf, aq); ak = MFMA(wk, hf, ak); av = MFMA(hf, wv, av);
  }
  constexpr float QS = 0.18898223650461363f * 1.4426950408889634f;
#pragma unroll
  for (int r = 0; r < 16; ++r) aq[r] *= QS;
  u32x4 q0 = pack8(aq, 0), q1 = pack8(aq, 1), k0 = pack8(ak, 0), k1 = pack8(ak, 1);
  float kn = 0;
#pragma unroll
  for (int i = 0; i < 4; ++i) { float a = bflo(k0[i]), b = bfhi(k0[i]), c = bflo(k1[i]), d = bfhi(k1[i]); kn += a * a + b * b + c * c + d * d; }
  kn += xor32(kn);
#pragma unroll
  for (int m = 1; m < 32; m <<= 1) kn = fmaxf(kn, __shfl_xor(kn, m));
  if (hh) k1[2] = 0x00003f80u;
  if ((lane & 31) == DH) {
#pragma unroll
    for (int r = 0; r < 16; ++r) av[r] = 1.f;
  }
  char* qd = ws + OFF_Q + ((size_t)h * NT + tile) * 2048 + lane * 16;
  char* kd = ws + OFF_KV + ((size_t)h * NT + tile) * 4096 + lane * 16;
  *(u32x4*)qd = q0; *(u32x4*)(qd + 1024) = q1;
  *(u32x4*)kd = k0; *(u32x4*)(kd + 1024) = k1; *(u32x4*)(kd + 2048) = pack8(av, 0); *(u32x4*)(kd + 3072) = pack8(av, 1);
  if (lane == 0) ((float*)(ws + OFF_KN))[h * NT + tile] = kn;
}

DI f32x16 combine4(char* lds, const f32x16 (&a)[4], int wave, int lane) {
#pragma unroll
  for (int i = 0; i < 4; ++i)
#pragma unroll
    for (int g = 0; g < 4; ++g) *(f32x4*)(lds + (((i * 4 + wave) * 4 + g) * 64 + lane) * 16) = (f32x4){a[i][4*g], a[i][4*g+1], a[i][4*g+2], a[i][4*g+3]};
  __syncthreads();
  f32x16 r;
#pragma unroll
  for (int g = 0; g < 4; ++g) {
    f32x4 v = *(const f32x4*)(lds + (((wave * 4 + 0) * 4 + g) * 64 + lane) * 16);
#pragma unroll
    for (int w = 1; w < 4; ++w) v += *(const f32x4*)(lds + (((wave * 4 + w) * 4 + g) * 64 + lane) * 16);
    r[4*g] = v[0]; r[4*g+1] = v[1]; r[4*g+2] = v[2]; r[4*g+3] = v[3];
  }
  return r;
}

__global__ __launch_bounds__(256, 1) void k2_attn(char* __restrict__ ws) {
  extern __shared__ __attribute__((aligned(16))) char lds[];
  const int t = threadIdx.x, lane = t & 63, wave = __builtin_amdgcn_readfirstlane(t >> 6), hh = lane >> 5;
  const int xcd = blockIdx.x & 7, j = blockIdx.x >> 3, h = 2 * xcd + (j & 1), qb = j >> 1;
  const char* qsrc = ws + OFF_Q + ((size_t)h * NT + qb * 4) * 2048 + lane * 16;
  const char* kv = ws + OFF_KV + ((size_t)h * NT + wave * 16) * 4096 + lane * 16;
  u32x4 q[4][2];
#pragma unroll
  for (int i = 0; i < 4; ++i) { q[i][0] = *(const u32x4*)(qsrc + i * 2048); q[i][1] = *(const u32x4*)(qsrc + i * 2048 + 1024); }
  float kn = ((const float*)(ws + OFF_KN))[h * NT + lane];
  u32x4 f[3][4];
#pragma unroll
  for (int i = 0; i < 2; ++i)
#pragma unroll
    for (int u = 0; u < 4; ++u) f[i][u] = *(const u32x4*)(kv + i * 4096 + u * 1024);
#pragma unroll
  for (int m = 1; m < 64; m <<= 1) kn = fmaxf(kn, __shfl_xor(kn, m));
  float ub[4]; bool safe = true;
#pragma unroll
  for (int i = 0; i < 4; ++i) {
    float qn = 0;
#pragma unroll
    for (int u = 0; u < 2; ++u)
#pragma unroll
      for (int e = 0; e < 4; ++e) { float a = bflo(q[i][u][e]), b = bfhi(q[i][u][e]); qn += a * a + b * b; }
    qn += xor32(qn);
    ub[i] = __builtin_amdgcn_sqrtf(qn * kn) * 1.01f;
    safe = safe && (ub[i] <= 48.f);
  }
  f32x16 o[4] = {{}, {}, {}, {}};
  if (__all(safe)) {
#pragma unroll
    for (int i = 0; i < 4; ++i) if (hh) q[i][1][2] = pk2(-ub[i], 0.f);
#pragma unroll 1
    for (int k3 = 0; k3 < 15; k3 += 3) {
#pragma unroll
      for (int kk = 0; kk < 3; ++kk) {
        const int kt = k3 + kk;
        if (kt + 2 < 16) {
#pragma unroll
          for (int u = 0; u < 4; ++u) f[(kk + 2) % 3][u] = *(const u32x4*)(kv + (kt + 2) * 4096 + u * 1024);
        }
#pragma unroll
        for (int i = 0; i < 4; ++i) {
          f32x16 s = MFMA(f[kk][0], q[i][0], (f32x16){}); s = MFMA(f[kk][1], q[i][1], s);
#pragma unroll
          for (int r = 0; r < 16; ++r) s[r] = __builtin_amdgcn_exp2f(s[r]);
          o[i] = MFMA(f[kk][2], pack8(s, 0), o[i]); o[i] = MFMA(f[kk][3], pack8(s, 1), o[i]);
        }
      }
    }
#pragma unroll
    for (int i = 0; i < 4; ++i) {
      f32x16 s = MFMA(f[0][0], q[i][0], (f32x16){}); s = MFMA(f[0][1], q[i][1], s);
#pragma unroll
      for (int r = 0; r < 16; ++r) s[r] = __builtin_amdgcn_exp2f(s[r]);
      o[i] = MFMA(f[0][2], pack8(s, 0), o[i]); o[i] = MFMA(f[0][3], pack8(s, 1), o[i]);
    }
  } else {
    float m[4] = {-3.0e38f, -3.0e38f, -3.0e38f, -3.0e38f};
#pragma unroll 1
    for (int kt = 0; kt < 16; ++kt) {
      u32x4 g[4];
#pragma unroll
      for (int u = 0; u < 4; ++u) g[u] = *(const u32x4*)(kv + kt * 4096 + u * 1024);
#pragma unroll
      for (int i = 0; i < 4; ++i) {
        f32x16 s = MFMA(g[0], q[i][0], (f32x16){}); s = MFMA(g[1], q[i][1], s);
        float mx = s[0];
#pragma unroll
        for (int r = 1; r < 16; ++r) mx = fmaxf(mx, s[r]);
        mx = fmaxf(mx, xor32(mx));
        const float mn = fmaxf(m[i], mx), al = __builtin_amdgcn_exp2f(m[i] - mn);
        m[i] = mn;
#pragma unroll
        for (int r = 0; r < 16; ++r) { s[r] = __builtin_amdgcn_exp2f(s[r] - mn); o[i][r] *= al; }
        o[i] = MFMA(g[2], pack8(s, 0), o[i]); o[i] = MFMA(g[3], pack8(s, 1), o[i]);
      }
    }
    float* ml = (float*)(lds + 65536);
#pragma unroll
    for (int i = 0; i < 4; ++i) if (!hh) ml[(i * 4 + wave) * 32 + lane] = m[i];
    __syncthreads();
#pragma unroll
    for (int i = 0; i < 4; ++i) {
      const int c = lane & 31;
      const float mg = fmaxf(fmaxf(ml[(i * 4 + 0) * 32 + c], ml[(i * 4 + 1) * 32 + c]), fmaxf(ml[(i * 4 + 2) * 32 + c], ml[(i * 4 + 3) * 32 + c]));
      const float al = __builtin_amdgcn_exp2f(m[i] - mg);
#pragma unroll
      for (int r = 0; r < 16; ++r) o[i][r] *= al;
    }
  }
  f32x16 r = combine4(lds, o, wave, lane);
  const float l = __shfl(r[12], lane | 32);
  const float il = 1.f / l;
#pragma unroll
  for (int e = 0; e < 16; ++e) r[e] *= il;
  char* od = ws + OFF_O + ((size_t)(qb * 4 + wave) * 32 + 2 * h) * 1024 + lane * 16;
  *(u32x4*)od = pack8(r, 0); *(u32x4*)(od + 1024) = pack8(r, 1);
}

__global__ __launch_bounds__(256, 1) void k3_wo(const uint16_t* __restrict__ x, char* __restrict__ ws) {
  extern __shared__ __attribute__((aligned(16))) char lds[];
  const int t = threadIdx.x, lane = t & 63, wave = __builtin_amdgcn_readfirstlane(t >> 6), hh = lane >> 5;
  const int xcd = blockIdx.x & 7, j = blockIdx.x >> 3, tp = xcd * 4 + (j & 3), cp = j >> 2;
  const char* asrc = ws + OFF_WO + ((size_t)cp * 32 + wave * 8) * 2048 + lane * 16;
  const char* bsrc = ws + OFF_O + ((size_t)tp * 2 * 32 + wave * 8) * 1024 + lane * 16;
  u32x4 a[8][2], b[8][2];
#pragma unroll
  for (int s = 0; s < 8; ++s) { a[s][0] = *(const u32x4*)(asrc + s * 2048); a[s][1] = *(const u32x4*)(asrc + s * 2048 + 1024);
                                b[s][0] = *(const u32x4*)(bsrc + s * 1024); b[s][1] = *(const u32x4*)(bsrc + 32 * 1024 + s * 1024); }
  const int tile = tp * 2 + (wave >> 1), ct = cp * 2 + (wave & 1);
  u32x2 xr[4]; f32x4 ga[4];
#pragma unroll
  for (int g = 0; g < 4; ++g) { xr[g] = *(const u32x2*)(x + (size_t)(tile * 32 + (lane & 31)) * D + ct * 32 + 8 * g + 4 * hh);
                                ga[g] = *(const f32x4*)(ws + OFF_MOD + (2 * D + ct * 32 + 8 * g + 4 * hh) * 4); }
  f32x16 acc[4] = {{}, {}, {}, {}};
#pragma unroll
  for (int s = 0; s < 8; ++s) {
    acc[0] = MFMA(a[s][0], b[s][0], acc[0]); acc[1] = MFMA(a[s][1], b[s][0], acc[1]);
    acc[2] = MFMA(a[s][0], b[s][1], acc[2]); acc[3] = MFMA(a[s][1], b[s][1], acc[3]);
  }
  f32x16 r = combine4(lds, acc, wave, lane);
#pragma unroll
  for (int g = 0; g < 4; ++g) {
    r[4*g]   = bflo(xr[g][0]) + ga[g][0] * r[4*g];   r[4*g+1] = bfhi(xr[g][0]) + ga[g][1] * r[4*g+1];
    r[4*g+2] = bflo(xr[g][1]) + ga[g][2] * r[4*g+2]; r[4*g+3] = bfhi(xr[g][1]) + ga[g][3] * r[4*g+3];
  }
  const u32x4 p0 = pack8(r, 0), p1 = pack8(r, 1);
  char* xd = ws + OFF_X2 + ((size_t)tile * KS + ct * 2) * 1024 + lane * 16;
  *(u32x4*)xd = p0; *(u32x4*)(xd + 1024) = p1;
  float sum = 0;
#pragma unroll
  for (int i = 0; i < 4; ++i) sum += bflo(p0[i]) + bfhi(p0[i]) + bflo(p1[i]) + bfhi(p1[i]);
  sum += xor32(sum);
  const float mean = sum * (1.f / 32);
  float m2 = 0;
#pragma unroll
  for (int i = 0; i < 4; ++i) { float u0 = bflo(p0[i]) - mean, u1 = bfhi(p0[i]) - mean, u2 = bflo(p1[i]) - mean, u3 = bfhi(p1[i]) - mean; m2 += u0 * u0 + u1 * u1 + u2 * u2 + u3 * u3; }
  m2 += xor32(m2);
  if (!hh) *(f32x2*)(ws + OFF_ST + ((size_t)(tile * 14 + ct) * 32 + lane) * 8) = (f32x2){mean, m2};
}

__global__ __launch_bounds__(256, 1) void k4_gu(char* __restrict__ ws) {
  extern __shared__ __attribute__((aligned(16))) char lds[];
  const int t = threadIdx.x, lane = t & 63, wave = __builtin_amdgcn_readfirstlane(t >> 6), hh = lane >> 5;
  const int xcd = blockIdx.x & 7, j = blockIdx.x >> 3, rb = 2 * xcd + (j & 1), fg = j >> 1, tile = rb * 4 + wave;
  const char* wsrc = ws + OFF_WGU + (size_t)fg * KS * 7 * 1024 + wave * 7 * 1024 + lane * 16;
  const char* xsrc = ws + OFF_X2 + (size_t)tile * KS * 1024 + lane * 16;
  float* modl = (float*)(lds + 2 * 28 * 1024);
  f32x4 mv;
  if (t < 224) mv = ((const f32x4*)(ws + OFF_MOD + 3 * D * 4))[t];
  f32x2 st[7];
#pragma unroll
  for (int i = 0; i < 7; ++i) st[i] = *(const f32x2*)(ws + OFF_ST + ((size_t)(tile * 14 + hh * 7 + i) * 32 + (lane & 31)) * 8);
  u32x4 w[7][7], n[KS];
#pragma unroll
  for (int c = 0; c < 7; ++c) {
#pragma unroll
    for (int i = 0; i < 7; ++i) w[c][i] = *(const u32x4*)(wsrc + c * 28 * 1024 + i * 1024);
#pragma unroll
    for (int i = 0; i < 4; ++i) n[c * 4 + i] = *(const u32x4*)(xsrc + (c * 4 + i) * 1024);
  }
  if (t < 224) ((f32x4*)modl)[t] = mv;
  float mean = 0;
#pragma unroll
  for (int i = 0; i < 7; ++i) mean += st[i][0];
  mean += xor32(mean); mean *= (1.f / 14);
  float m2 = 0;
#pragma unroll
  for (int i = 0; i < 7; ++i) { const float d = st[i][0] - mean; m2 += st[i][1] + 32.f * d * d; }
  m2 += xor32(m2);
  const float rstd = __builtin_amdgcn_rsqf(m2 * (1.f / D) + 1e-5f), nm = -mean * rstd;
  f32x16 acc[7] = {{}, {}, {}, {}, {}, {}, {}};
#pragma unroll
  for (int i = 0; i < 7; ++i) *(u32x4*)(lds + (wave * 7 + i) * 1024 + lane * 16) = w[0][i];
  __syncthreads();
#pragma unroll
  for (int c = 0; c < 7; ++c) {
    const char* buf = lds + (c & 1) * 28 * 1024 + lane * 16;
#pragma unroll
    for (int u = 0; u < 4; ++u) {
      const int s = c * 4 + u;
      const f32x4* sh = (const f32x4*)(modl + s * 16 + hh * 4);
      const f32x4* sc = (const f32x4*)(modl + D + s * 16 + hh * 4);
      const f32x4 sh0 = sh[0], sh1 = sh[2], sc0 = sc[0], sc1 = sc[2];
      u32x4 hf;
      hf[0] = pk2((bflo(n[s][0]) * rstd + nm) * sc0[0] + sh0[0], (bfhi(n[s][0]) * rstd + nm) * sc0[1] + sh0[1]);
      hf[1] = pk2((bflo(n[s][1]) * rstd + nm) * sc0[2] + sh0[2], (bfhi(n[s][1]) * rstd + nm) * sc0[3] + sh0[3]);
      hf[2] = pk2((bflo(n[s][2]) * rstd + nm) * sc1[0] + sh1[0], (bfhi(n[s][2]) * rstd + nm) * sc1[1] + sh1[1]);
      hf[3] = pk2((bflo(n[s][3]) * rstd + nm) * sc1[2] + sh1[2], (bfhi(n[s][3]) * rstd + nm) * sc1[3] + sh1[3]);
#pragma unroll
      for (int i = 0; i < 7; ++i) acc[i] = MFMA(*(const u32x4*)(buf + (u * 7 + i) * 1024), hf, acc[i]);
      if (u == 1 && c < 6) {
#pragma unroll
        for (int i = 0; i < 7; ++i) *(u32x4*)(lds + ((c + 1) & 1) * 28 * 1024 + (wave * 7 + i) * 1024 + lane * 16) = w[c + 1][i];
      }
    }
    if (c < 6) __syncthreads();
  }
  char* hd = ws + OFF_HM + ((size_t)tile * 112 + fg * 7) * 1024 + lane * 16;
  constexpr float G0 = -2.f * 0.7978845608028654f * 1.4426950408889634f, G1 = G0 * 0.044715f;
#pragma unroll
  for (int i = 0; i < 7; ++i) {
    float hv[8];
#pragma unroll
    for (int r = 0; r < 8; ++r) {
      const float g = acc[i][r], u = acc[i][r + 8];
      const float e = __builtin_amdgcn_exp2f(g * (G0 + G1 * g * g));
      hv[r] = g * u * __builtin_amdgcn_rcpf(1.f + e);
    }
    *(u32x4*)(hd + i * 1024) = (u32x4){pk2(hv[0], hv[1]), pk2(hv[2], hv[3]), pk2(hv[4], hv[5]), pk2(hv[6], hv[7])};
  }
}

__global__ __launch_bounds__(256, 1) void k5_down(char* __restrict__ ws, uint16_t* __restrict__ out) {
  extern __shared__ __attribute__((aligned(16))) char lds[];
  const int t = threadIdx.x, lane = t & 63, wave = __builtin_amdgcn_readfirstlane(t >> 6), hh = lane >> 5;
  const int xcd = blockIdx.x & 7, j = blockIdx.x >> 3, tp = xcd * 4 + (j & 3), cp = j >> 2;
  const char* asrc = ws + OFF_WD + ((size_t)cp * 112 + wave * 28) * 2048 + lane * 16;
  const char* bsrc = ws + OFF_HM + ((size_t)tp * 2 * 112 + wave * 28) * 1024 + lane * 16;
  constexpr int W = 14;
  u32x4 a[W][2], b[W][2];
#pragma unroll
  for (int s = 0; s < W; ++s) { a[s][0] = *(const u32x4*)(asrc + s * 2048); a[s][1] = *(const u32x4*)(asrc + s * 2048 + 1024);
                                b[s][0] = *(const u32x4*)(bsrc + s * 1024); b[s][1] = *(const u32x4*)(bsrc + 112 * 1024 + s * 1024); }
  const int tile = tp * 2 + (wave >> 1), ct = cp * 2 + (wave & 1);
  f32x16 acc[4] = {{}, {}, {}, {}};
#pragma unroll
  for (int s = 0; s < 28; ++s) {
    const int i = s % W;
    acc[0] = MFMA(a[i][0], b[i][0], acc[0]); acc[1] = MFMA(a[i][1], b[i][0], acc[1]);
    acc[2] = MFMA(a[i][0], b[i][1], acc[2]); acc[3] = MFMA(a[i][1], b[i][1], acc[3]);
    if (s + W < 28) { const int s2 = s + W;
      a[i][0] = *(const u32x4*)(asrc + s2 * 2048); a[i][1] = *(const u32x4*)(asrc + s2 * 2048 + 1024);
      b[i][0] = *(const u32x4*)(bsrc + s2 * 1024); b[i][1] = *(const u32x4*)(bsrc + 112 * 1024 + s2 * 1024); }
  }
  const char* xs = ws + OFF_X2 + ((size_t)tile * KS + ct * 2) * 1024 + lane * 16;
  const u32x4 x0 = *(const u32x4*)xs, x1 = *(const u32x4*)(xs + 1024);
  f32x4 gb[4];
#pragma unroll
  for (int g = 0; g < 4; ++g) gb[g] = *(const f32x4*)(ws + OFF_MOD + (5 * D + ct * 32 + 8 * g + 4 * hh) * 4);
  f32x16 r = combine4(lds, acc, wave, lane);
  uint16_t* od = out + (size_t)(tile * 32 + (lane & 31)) * D + ct * 32 + 4 * hh;
#pragma unroll
  for (int g = 0; g < 4; ++g) {
    const unsigned u0 = g < 2 ? x0[2 * g] : x1[2 * g - 4], u1 = g < 2 ? x0[2 * g + 1] : x1[2 * g - 3];
    *(u32x2*)(od + 8 * g) = (u32x2){pk2(bflo(u0) + gb[g][0] * r[4*g], bfhi(u0) + gb[g][1] * r[4*g+1]), pk2(bflo(u1) + gb[g][2] * r[4*g+2], bfhi(u1) + gb[g][3] * r[4*g+3])};
  }
}

extern "C" void kernel_launch(void* const* d_in, const int* in_sizes, int n_in, void* d_out, int out_size, void* d_ws, size_t ws_size, hipStream_t stream) {
  const uint16_t* x = (const uint16_t*)d_in[0]; const uint16_t* wqkv = (const uint16_t*)d_in[1]; const uint16_t* wo = (const uint16_t*)d_in[2];
  const uint16_t* wg = (const uint16_t*)d_in[4]; const uint16_t* wu = (const uint16_t*)d_in[5]; const uint16_t* wd = (const uint16_t*)d_in[6];
  const uint16_t* c = (const uint16_t*)d_in[7]; const uint16_t* wm = (const uint16_t*)d_in[8];
  char* ws = (char*)d_ws;
  (void)hipFuncSetAttribute((const void*)k1_qkv, hipFuncAttributeMaxDynamicSharedMemorySize, 84 * 1024 + 3584);
  (void)hipFuncSetAttribute((const void*)k2_attn, hipFuncAttributeMaxDynamicSharedMemorySize, 65536 + 2048);
  k0_prep<<<K0_MOD + K0_WQ + K0_LN + K0_WO + K0_WD + K0_WGU, 256, 0, stream>>>(x, wqkv, wo, wg, wu, wd, c, wm, ws);
  k1_qkv<<<256, 256, 84 * 1024 + 3584, stream>>>(ws);
  k2_attn<<<256, 256, 65536 + 2048, stream>>>(ws);
  k3_wo<<<224, 256, 65536, stream>>>(x, ws);
  k4_gu<<<256, 256, 2 * 28 * 1024 + 3584, stream>>>(ws);
  k5_down<<<224, 256, 65536, stream>>>(ws, (uint16_t*)d_out);
}
```

```cpp
#include <hip/hip_runtime.h>
#include <stdint.h>

__global__ __launch_bounds__(256, 1) void adaln_block(const void* x, const void* wqkv, const void* wo, const void* wg, const void* wu, const void* wd,
                                                      const void* c, const void* wmod, void* ws, void* out) {
  const void* ka = (const void*)__builtin_amdgcn_kernarg_segment_ptr();
  unsigned bid = blockIdx.x, tid = threadIdx.x;
  asm volatile(
    "Lstart%=:\n"
    "s_load_dwordx16 s[24:39], s[0:1], 0x0\n"
    "s_load_dwordx4 s[40:43], s[0:1], 0x40\n"
    "s_getpc_b64 s[12:13]\n"
    "Lpc%=:\n"
    "v_lshrrev_b32 v2, 6, v0\n"
    "v_and_b32 v1, 63, v0\n"
    "v_readfirstlane_b32 s18, v2\n"
    "v_lshlrev_b32 v0, 4, v1\n"
    "s_sub_u32 s12, s12, Lpc%=-Lstart%=\n"
    "s_subb_u32 s13, s13, 0\n"
    "s_and_b32 s13, s13, 0xffff\n"
    "s_mov_b32 s14, Lend%=-Lstart%=\n"
    "s_mov_b32 s15, 0x20000\n"
    "s_lshr_b32 s20, s2, 3\n"
    "s_lshl_b32 s44, s20, 2\n"
    "s_add_u32 s44, s44, s18\n"
    "s_and_b32 s44, s44, 63\n"
    "s_lshl_b32 s44, s44, 10\n"
    "buffer_load_dwordx4 a[0:3], v0, s[12:15], s44 offen\n"
    "v_lshrrev_b32 v6, 3, v1\n"
    "v_and_b32 v7, 7, v1\n"
    "v_cmp_le_u32 vcc, 28, v1\n"
    "v_subrev_u32 v8, 28, v1\n"
    "s_nop 0\n"
    "v_cndmask_b32 v8, v1, v8, vcc\n"
    "v_cndmask_b32 v9, 0, 1, vcc\n"
    "s_mul_i32 s46, s2, 171\n"
    "s_lshr_b32 s46, s46, 10\n"
    "s_mul_i32 s47, s46, 6\n"
    "s_sub_u32 s47, s2, s47\n"
    "s_mul_i32 s44, s46, 43008\n"
    "s_mul_i32 s45, s47, 448\n"
    "s_add_u32 s49, s44, s45\n"
    "s_mul_i32 s44, s2, 7168\n"
    "s_movk_i32 s45, 128\n"
    "s_movk_i32 s48, 896\n"
    "s_mov_b32 s10, 1835008\n"
    "s_cmp_eq_u32 s18, 1\n"
    "s_cselect_b32 s44, s49, s44\n"
    "s_cmovk_i32 s45, 5376\n"
    "s_cmovk_i32 s48, 2688\n"
    "s_cselect_b32 s10, 1204224, s10\n"
    "s_cselect_b64 s[50:51], -1, 0\n"
    "s_cselect_b32 s52, 0xffffff, -1\n"
    "v_cndmask_b32 v10, v6, v9, s[50:51]\n"
    "v_cndmask_b32 v11, v7, v8, s[50:51]\n"
    "s_lshr_b32 s53, s2, 2\n"
    "s_and_b32 s54, s2, 3\n"
    "s_mul_i32 s49, s54, 602112\n"
    "s_lshl_b32 s55, s53, 7\n"
    "s_add_u32 s49, s49, s55\n"
    "s_cmp_eq_u32 s18, 2\n"
    "s_cselect_b32 s44, s49, s44\n"
    "s_cselect_b32 s45, 43008, s45\n"
    "s_cmovk_i32 s48, 5376\n"
    "s_cselect_b32 s10, 2408448, s10\n"
    "s_cselect_b32 s46, s53, s46\n"
    "s_cselect_b32 s47, s54, s47\n"
    "v_mul_u32_u24 v10, s48, v10\n"
    "v_lshl_add_u32 v10, v11, 4, v10\n"
    "s_and_b32 s19, s2, 7\n"
    "s_mov_b32 s11, 0x20000\n"
    "s_cmp_ge_u32 s2, 168\n"
    "s_cselect_b32 s49, 1, 3\n"
    "s_cmp_ge_u32 s18, s49\n"
    "s_cselect_b32 s49, 1, 0\n"
    "s_waitcnt lgkmcnt(0)\n"
    "s_mov_b64 s[8:9], s[24:25]\n"
    "s_cmp_eq_u32 s18, 1\n"
    "s_cselect_b64 s[8:9], s[26:27], s[8:9]\n"
    "s_cmp_eq_u32 s18, 2\n"
    "s_cselect_b64 s[8:9], s[38:39], s[8:9]\n"
    "s_and_b32 s9, s9, 0xffff\n"
    "s_cmp_eq_u32 s49, 1\n"
    "s_cselect_b32 s10, 0, s10\n"
    "s_mov_b32 exec_hi, s52\n"
    "buffer_load_dwordx4 v[12:15], v10, s[8:11], s44 offen\n"
    "s_add_u32 s44, s44, s45\n"
    "buffer_load_dwordx4 v[16:19], v10, s[8:11], s44 offen\n"
    "s_add_u32 s44, s44, s45\n"
    "buffer_load_dwordx4 v[20:23], v10, s[8:11], s44 offen\n"
    "s_add_u32 s44, s44, s45\n"
    "buffer_load_dwordx4 v[24:27], v10, s[8:11], s44 offen\n"
    "s_add_u32 s44, s44, s45\n"
    "buffer_load_dwordx4 v[28:31], v10, s[8:11], s44 offen\n"
    "s_add_u32 s44, s44, s45\n"
    "buffer_load_dwordx4 v[32:35], v10, s[8:11], s44 offen\n"
    "s_add_u32 s44, s44, s45\n"
    "buffer_load_dwordx4 v[36:39], v10, s[8:11], s44 offen\n"
    "s_add_u32 s44, s44, s45\n"
    "s_mov_b32 s4, s40\n"
    "s_and_b32 s5, s41, 0xffff\n"
    "s_mov_b32 s6, 24723456\n"
    "s_mov_b32 s7, 0x20000\n"
    "buffer_load_dwordx2 v[4:5], off, s[4:7], 0 sc1\n"
    "s_mov_b32 s78, 0\n"
    "s_mov_b32 s79, -1\n"
    "s_mov_b64 s[64:65], 1\n"
    "s_mov_b32 s76, 143360\n"
    "s_cmp_eq_u32 s49, 1\n"
    "s_cbranch_scc1 Lidle_4%=\n"
    "s_cmp_eq_u32 s18, 0\n"
    "s_cbranch_scc1 Lln_2%=\n"
    "buffer_load_dwordx4 v[40:43], v10, s[8:11], s44 offen\n"
    "s_add_u32 s44, s44, s45\n"
    "s_cmp_eq_u32 s18, 1\n"
    "s_cbranch_scc1 Lwq_1%=\n"
    "s_mov_b32 s12, s36\n"
    "s_and_b32 s13, s37, 0xffff\n"
    "s_mov_b32 s14, 896\n"
    "s_mov_b32 s15, 0x20000\n"
    "v_lshlrev_b32 v8, 1, v1\n"
    "s_mul_i32 s48, s47, 224\n"
    "buffer_load_ushort v2, v8, s[12:15], s48 offen\n"
    "buffer_load_ushort v3, v8, s[12:15], s48 offen offset:128\n"
    "buffer_load_dwordx4 v[44:47], v10, s[8:11], s44 offen\n"
    "s_add_u32 s44, s44, s45\n"
    "buffer_load_dwordx4 v[48:51], v10, s[8:11], s44 offen\n"
    "s_add_u32 s44, s44, s45\n"
    "buffer_load_dwordx4 v[52:55], v10, s[8:11], s44 offen\n"
    "s_add_u32 s44, s44, s45\n"
    "buffer_load_dwordx4 v[56:59], v10, s[8:11], s44 offen\n"
    "s_add_u32 s44, s44, s45\n"
    "buffer_load_dwordx4 v[60:63], v10, s[8:11], s44 offen\n"
    "s_add_u32 s44, s44, s45\n"
    "buffer_load_dwordx4 v[64:67], v10, s[8:11], s44 offen\n"
    "v_lshlrev_b32 v8, 6, v7\n"
    "v_lshl_add_u32 v8, v6, 2, v8\n"
    "v_add_u32 v8, s76, v8\n"
    "v_lshlrev_b32 v9, 6, v6\n"
    "v_add_u32 v9, s76, v9\n"
    "s_waitcnt vmcnt(7)\n"
    "v_lshlrev_b32 v10, 16, v2\n"
    "s_waitcnt vmcnt(6)\n"
    "v_lshlrev_b32 v11, 16, v3\n"
    "v_mul_f32 v68, 0xbfb8aa3b, v10\n"
    "v_mul_f32 v69, 0xbfb8aa3b, v11\n"
    "v_exp_f32 v68, v68\n"
    "v_exp_f32 v69, v69\n"
    "v_add_f32 v68, 1.0, v68\n"
    "v_add_f32 v69, 1.0, v69\n"
    "v_rcp_f32 v68, v68\n"
    "v_rcp_f32 v69, v69\n"
    "v_mul_f32 v10, v10, v68\n"
    "v_mul_f32 v11, v11, v69\n"
    "v_cvt_pk_bf16_f32 v10, v10, 0\n"
    "v_cvt_pk_bf16_f32 v11, v11, 0\n"
    "v_lshlrev_b32 v68, 16, v10\n"
    "v_lshlrev_b32 v69, 16, v11\n"
    "ds_write_b32 v8, v10\n"
    "ds_write_b32 v8, v11 offset:32\n"
    "ds_write_b32 v8, v68 offset:512\n"
    "ds_write_b32 v8, v69 offset:544\n"
    "s_waitcnt lgkmcnt(0)\n"
    "ds_read_b128 v[72:75], v9 offset:0\n"
    "ds_read_b128 v[76:79], v9 offset:16\n"
    "ds_read_b128 v[80:83], v9 offset:32\n"
    "ds_read_b128 v[84:87], v9 offset:48\n"
    "ds_read_b128 v[88:91], v9 offset:512\n"
    "ds_read_b128 v[92:95], v9 offset:528\n"
    "ds_read_b128 v[96:99], v9 offset:544\n"
    "ds_read_b128 v[100:103], v9 offset:560\n"
    "s_waitcnt lgkmcnt(7)\n"
    "v_dot2_f32_bf16 v104, v12, v72, 0\n"
    "s_waitcnt lgkmcnt(3)\n"
    "v_dot2_f32_bf16 v105, v12, v88, 0\n"
    "v_dot2_f32_bf16 v106, v13, v72, 0\n"
    "v_dot2_f32_bf16 v107, v13, v88, 0\n"
    "v_dot2_f32_bf16 v108, v14, v72, 0\n"
    "v_dot2_f32_bf16 v109, v14, v88, 0\n"
    "v_dot2_f32_bf16 v110, v15, v72, 0\n"
    "v_dot2_f32_bf16 v111, v15, v88, 0\n"
    "v_dot2_f32_bf16 v104, v16, v73, v104\n"
    "v_dot2_f32_bf16 v105, v16, v89, v105\n"
    "v_dot2_f32_bf16 v106, v17, v73, v106\n"
    "v_dot2_f32_bf16 v107, v17, v89, v107\n"
    "v_dot2_f32_bf16 v108, v18, v73, v108\n"
    "v_dot2_f32_bf16 v109, v18, v89, v109\n"
    "v_dot2_f32_bf16 v110, v19, v73, v110\n"
    "v_dot2_f32_bf16 v111, v19, v89, v111\n"
    "v_dot2_f32_bf16 v104, v20, v74, v104\n"
    "v_dot2_f32_bf16 v105, v20, v90, v105\n"
    "v_dot2_f32_bf16 v106, v21, v74, v106\n"
    "v_dot2_f32_bf16 v107, v21, v90, v107\n"
    "v_dot2_f32_bf16 v108, v22, v74, v108\n"
    "v_dot2_f32_bf16 v109, v22, v90, v109\n"
    "v_dot2_f32_bf16 v110, v23, v74, v110\n"
    "v_dot2_f32_bf16 v111, v23, v90, v111\n"
    "v_dot2_f32_bf16 v104, v24, v75, v104\n"
    "v_dot2_f32_bf16 v105, v24, v91, v105\n"
    "v_dot2_f32_bf16 v106, v25, v75, v106\n"
    "v_dot2_f32_bf16 v107, v25, v91, v107\n"
    "v_dot2_f32_bf16 v108, v26, v75, v108\n"
    "v_dot2_f32_bf16 v109, v26, v91, v109\n"
    "v_dot2_f32_bf16 v110, v27, v75, v110\n"
    "v_dot2_f32_bf16 v111, v27, v91, v111\n"
    "v_dot2_f32_bf16 v104, v28, v76, v104\n"
    "s_waitcnt lgkmcnt(2)\n"
    "v_dot2_f32_bf16 v105, v28, v92, v105\n"
    "v_dot2_f32_bf16 v106, v29, v76, v106\n"
    "v_dot2_f32_bf16 v107, v29, v92, v107\n"
    "v_dot2_f32_bf16 v108, v30, v76, v108\n"
    "v_dot2_f32_bf16 v109, v30, v92, v109\n"
    "v_dot2_f32_bf16 v110, v31, v76, v110\n"
    "v_dot2_f32_bf16 v111, v31, v92, v111\n"
    "v_dot2_f32_bf16 v104, v32, v77, v104\n"
    "v_dot2_f32_bf16 v105, v32, v93, v105\n"
    "v_dot2_f32_bf16 v106, v33, v77, v106\n"
    "v_dot2_f32_bf16 v107, v33, v93, v107\n"
    "v_dot2_f32_bf16 v108, v34, v77, v108\n"
    "v_dot2_f32_bf16 v109, v34, v93, v109\n"
    "v_dot2_f32_bf16 v110, v35, v77, v110\n"
    "v_dot2_f32_bf16 v111, v35, v93, v111\n"
    "v_dot2_f32_bf16 v104, v36, v78, v104\n"
    "v_dot2_f32_bf16 v105, v36, v94, v105\n"
    "v_dot2_f32_bf16 v106, v37, v78, v106\n"
    "v_dot2_f32_bf16 v107, v37, v94, v107\n"
    "v_dot2_f32_bf16 v108, v38, v78, v108\n"
    "v_dot2_f32_bf16 v109, v38, v94, v109\n"
    "v_dot2_f32_bf16 v110, v39, v78, v110\n"
    "v_dot2_f32_bf16 v111, v39, v94, v111\n"
    "v_dot2_f32_bf16 v104, v40, v79, v104\n"
    "v_dot2_f32_bf16 v105, v40, v95, v105\n"
    "v_dot2_f32_bf16 v106, v41, v79, v106\n"
    "v_dot2_f32_bf16 v107, v41, v95, v107\n"
    "v_dot2_f32_bf16 v108, v42, v79, v108\n"
    "v_dot2_f32_bf16 v109, v42, v95, v109\n"
    "v_dot2_f32_bf16 v110, v43, v79, v110\n"
    "v_dot2_f32_bf16 v111, v43, v95, v111\n"
    "s_waitcnt vmcnt(5)\n"
    "v_dot2_f32_bf16 v104, v44, v80, v104\n"
    "s_waitcnt lgkmcnt(1)\n"
    "v_dot2_f32_bf16 v105, v44, v96, v105\n"
    "v_dot2_f32_bf16 v106, v45, v80, v106\n"
    "v_dot2_f32_bf16 v107, v45, v96, v107\n"
    "v_dot2_f32_bf16 v108, v46, v80, v108\n"
    "v_dot2_f32_bf16 v109, v46, v96, v109\n"
    "v_dot2_f32_bf16 v110, v47, v80, v110\n"
    "v_dot2_f32_bf16 v111, v47, v96, v111\n"
    "s_waitcnt vmcnt(4)\n"
    "v_dot2_f32_bf16 v104, v48, v81, v104\n"
    "v_dot2_f32_bf16 v105, v48, v97, v105\n"
    "v_dot2_f32_bf16 v106, v49, v81, v106\n"
    "v_dot2_f32_bf16 v107, v49, v97, v107\n"
    "v_dot2_f32_bf16 v108, v50, v81, v108\n"
    "v_dot2_f32_bf16 v109, v50, v97, v109\n"
    "v_dot2_f32_bf16 v110, v51, v81, v110\n"
    "v_dot2_f32_bf16 v111, v51, v97, v111\n"
    "s_waitcnt vmcnt(3)\n"
    "v_dot2_f32_bf16 v104, v52, v82, v104\n"
    "v_dot2_f32_bf16 v105, v52, v98, v105\n"
    "v_dot2_f32_bf16 v106, v53, v82, v106\n"
    "v_dot2_f32_bf16 v107, v53, v98, v107\n"
    "v_dot2_f32_bf16 v108, v54, v82, v108\n"
    "v_dot2_f32_bf16 v109, v54, v98, v109\n"
    "v_dot2_f32_bf16 v110, v55, v82, v110\n"
    "v_dot2_f32_bf16 v111, v55, v98, v111\n"
    "s_waitcnt vmcnt(2)\n"
    "v_dot2_f32_bf16 v104, v56, v83, v104\n"
    "v_dot2_f32_bf16 v105, v56, v99, v105\n"
    "v_dot2_f32_bf16 v106, v57, v83, v106\n"
    "v_dot2_f32_bf16 v107, v57, v99, v107\n"
    "v_dot2_f32_bf16 v108, v58, v83, v108\n"
    "v_dot2_f32_bf16 v109, v58, v99, v109\n"
    "v_dot2_f32_bf16 v110, v59, v83, v110\n"
    "v_dot2_f32_bf16 v111, v59, v99, v111\n"
    "s_waitcnt vmcnt(1)\n"
    "v_dot2_f32_bf16 v104, v60, v84, v104\n"
    "s_waitcnt lgkmcnt(0)\n"
    "v_dot2_f32_bf16 v105, v60, v100, v105\n"
    "v_dot2_f32_bf16 v106, v61, v84, v106\n"
    "v_dot2_f32_bf16 v107, v61, v100, v107\n"
    "v_dot2_f32_bf16 v108, v62, v84, v108\n"
    "v_dot2_f32_bf16 v109, v62, v100, v109\n"
    "v_dot2_f32_bf16 v110, v63, v84, v110\n"
    "v_dot2_f32_bf16 v111, v63, v100, v111\n"
    "s_waitcnt vmcnt(0)\n"
    "v_dot2_f32_bf16 v104, v64, v85, v104\n"
    "v_dot2_f32_bf16 v105, v64, v101, v105\n"
    "v_dot2_f32_bf16 v106, v65, v85, v106\n"
    "v_dot2_f32_bf16 v107, v65, v101, v107\n"
    "v_dot2_f32_bf16 v108, v66, v85, v108\n"
    "v_dot2_f32_bf16 v109, v66, v101, v109\n"
    "v_dot2_f32_bf16 v110, v67, v85, v110\n"
    "v_dot2_f32_bf16 v111, v67, v101, v111\n"
    "v_permlane16_swap_b32 v104, v105\n"
    "v_permlane16_swap_b32 v106, v107\n"
    "v_permlane16_swap_b32 v108, v109\n"
    "v_permlane16_swap_b32 v110, v111\n"
    "v_add_f32 v104, v104, v105\n"
    "v_add_f32 v106, v106, v107\n"
    "v_add_f32 v108, v108, v109\n"
    "v_add_f32 v110, v110, v111\n"
    "v_permlane32_swap_b32 v104, v106\n"
    "s_nop 0\n"
    "v_permlane32_swap_b32 v108, v110\n"
    "v_add_f32 v104, v104, v106\n"
    "v_add_f32 v108, v108, v110\n"
    "s_nop 0\n"
    "v_add_f32_dpp v104, v104, v104 row_ror:8 row_mask:0xf bank_mask:0xf\n"
    "v_add_f32_dpp v108, v108, v108 row_ror:8 row_mask:0xf bank_mask:0xf\n"
    "v_lshrrev_b32 v8, 4, v1\n"
    "v_lshlrev_b32 v8, 2, v8\n"
    "v_lshl_add_u32 v8, v7, 5, v8\n"
    "s_lshl_b32 s44, s2, 8\n"
    "s_add_u32 s44, s44, 126976\n"
    "buffer_store_dword v104, v8, s[4:7], s44 offen sc1\n"
    "buffer_store_dword v108, v8, s[4:7], s44 offen offset:16 sc1\n"
    "s_lshl_b32 s44, s2, 3\n"
    "s_add_u32 s44, s44, 6144\n"
    "s_mov_b32 s45, 0\n"
    "s_cmp_ge_u32 s46, 14\n"
    "s_cbranch_scc1 Lpub_3%=\n"
    "s_and_b32 s44, s46, 3\n"
    "s_lshl_b32 s44, s44, 4\n"
    "s_and_b32 s48, s46, 12\n"
    "s_add_u32 s44, s44, s48\n"
    "s_add_u32 s44, s44, s47\n"
    "s_lshl_b32 s44, s44, 3\n"
    "s_add_u32 s44, s44, 83968\n"
    "s_bfm_b64 s[64:65], 16, 0\n"
    "s_branch Lpub_3%=\n"
    "Lwq_1%=:\n"
    "s_mov_b32 exec_hi, -1\n"
    "v_lshlrev_b32 v8, 4, v8\n"
    "v_mul_u32_u24 v11, 576, v9\n"
    "v_add_u32 v11, v11, v8\n"
    "v_lshrrev_b32 v8, 5, v1\n"
    "v_lshlrev_b32 v8, 3, v8\n"
    "v_bfe_u32 v9, v1, 2, 2\n"
    "v_add_u32 v8, v8, v9\n"
    "v_mul_u32_u24 v8, 576, v8\n"
    "v_bfe_u32 v9, v1, 4, 1\n"
    "v_lshl_add_u32 v8, v9, 5, v8\n"
    "v_and_b32 v9, 3, v1\n"
    "v_lshl_add_u32 v8, v9, 3, v8\n"
    "s_and_b32 s48, s47, 1\n"
    "s_lshr_b32 s49, s47, 1\n"
    "s_mul_i32 s45, s48, 224\n"
    "s_add_u32 s45, s45, s46\n"
    "s_mul_i32 s45, s45, 3\n"
    "s_add_u32 s45, s45, s49\n"
    "s_lshl_b32 s45, s45, 10\n"
    "s_add_u32 s45, s45, 2359296\n"
    "s_mul_i32 s50, s46, 3\n"
    "s_add_u32 s50, s50, s49\n"
    "s_mul_i32 s53, s50, 86\n"
    "s_lshr_b32 s53, s53, 8\n"
    "s_mul_i32 s54, s53, 3\n"
    "s_sub_u32 s54, s50, s54\n"
    "s_sub_u32 s51, s50, 12\n"
    "s_mul_i32 s55, s51, 43\n"
    "s_lshr_b32 s55, s55, 8\n"
    "s_mul_i32 s56, s55, 6\n"
    "s_sub_u32 s56, s51, s56\n"
    "s_add_u32 s56, s56, 3\n"
    "s_cmp_lt_u32 s50, 12\n"
    "s_cselect_b32 s53, s53, s55\n"
    "s_cselect_b32 s54, s54, s56\n"
    "s_sub_u32 s51, s50, 36\n"
    "s_mul_i32 s55, s51, 43\n"
    "s_lshr_b32 s55, s55, 9\n"
    "s_mul_i32 s56, s55, 12\n"
    "s_sub_u32 s56, s51, s56\n"
    "s_add_u32 s56, s56, 9\n"
    "s_cmp_lt_u32 s50, 36\n"
    "s_cselect_b32 s53, s53, s55\n"
    "s_cselect_b32 s54, s54, s56\n"
    "s_lshl_b32 s44, s48, 2\n"
    "s_add_u32 s44, s44, s53\n"
    "s_lshl_b32 s44, s44, 5\n"
    "s_add_u32 s44, s44, s54\n"
    "s_lshl_b32 s44, s44, 3\n"
    "s_add_u32 s44, s44, 81920\n"
    "s_bfm_b64 s[64:65], 16, 0\n"
    "s_mov_b32 exec_hi, 0xffffff\n"
    "s_waitcnt vmcnt(8)\n"
    "ds_write_b128 v11, v[12:15] offset:0\n"
    "s_waitcnt vmcnt(7)\n"
    "ds_write_b128 v11, v[16:19] offset:1152\n"
    "s_waitcnt vmcnt(6)\n"
    "ds_write_b128 v11, v[20:23] offset:2304\n"
    "s_waitcnt vmcnt(5)\n"
    "ds_write_b128 v11, v[24:27] offset:3456\n"
    "s_waitcnt vmcnt(4)\n"
    "ds_write_b128 v11, v[28:31] offset:4608\n"
    "s_waitcnt vmcnt(3)\n"
    "ds_write_b128 v11, v[32:35] offset:5760\n"
    "s_waitcnt vmcnt(2)\n"
    "ds_write_b128 v11, v[36:39] offset:6912\n"
    "s_waitcnt vmcnt(0)\n"
    "ds_write_b128 v11, v[40:43] offset:8064\n"
    "s_mov_b32 exec_hi, -1\n"
    "s_waitcnt lgkmcnt(0)\n"
    "ds_read_b64_tr_b16 v[44:45], v8 offset:0\n"
    "ds_read_b64_tr_b16 v[46:47], v8 offset:2304\n"
    "ds_read_b64_tr_b16 v[48:49], v8 offset:56\n"
    "ds_read_b64_tr_b16 v[50:51], v8 offset:2360\n"
    "ds_read_b64_tr_b16 v[52:53], v8 offset:112\n"
    "ds_read_b64_tr_b16 v[54:55], v8 offset:2416\n"
    "ds_read_b64_tr_b16 v[56:57], v8 offset:168\n"
    "ds_read_b64_tr_b16 v[58:59], v8 offset:2472\n"
    "ds_read_b64_tr_b16 v[60:61], v8 offset:224\n"
    "ds_read_b64_tr_b16 v[62:63], v8 offset:2528\n"
    "ds_read_b64_tr_b16 v[64:65], v8 offset:280\n"
    "ds_read_b64_tr_b16 v[66:67], v8 offset:2584\n"
    "ds_read_b64_tr_b16 v[68:69], v8 offset:336\n"
    "ds_read_b64_tr_b16 v[70:71], v8 offset:2640\n"
    "ds_read_b64_tr_b16 v[72:73], v8 offset:392\n"
    "ds_read_b64_tr_b16 v[74:75], v8 offset:2696\n"
    "s_waitcnt lgkmcnt(14)\n"
    "buffer_store_dwordx4 v[44:47], v0, s[4:7], s45 offen sc1\n"
    "s_add_u32 s45, s45, 86016\n"
    "s_waitcnt lgkmcnt(12)\n"
    "buffer_store_dwordx4 v[48:51], v0, s[4:7], s45 offen sc1\n"
    "s_add_u32 s45, s45, 86016\n"
    "s_waitcnt lgkmcnt(10)\n"
    "buffer_store_dwordx4 v[52:55], v0, s[4:7], s45 offen sc1\n"
    "s_add_u32 s45, s45, 86016\n"
    "s_waitcnt lgkmcnt(8)\n"
    "buffer_store_dwordx4 v[56:59], v0, s[4:7], s45 offen sc1\n"
    "s_add_u32 s45, s45, 86016\n"
    "s_waitcnt lgkmcnt(6)\n"
    "buffer_store_dwordx4 v[60:63], v0, s[4:7], s45 offen sc1\n"
    "s_add_u32 s45, s45, 86016\n"
    "s_waitcnt lgkmcnt(4)\n"
    "buffer_store_dwordx4 v[64:67], v0, s[4:7], s45 offen sc1\n"
    "s_add_u32 s45, s45, 86016\n"
    "s_waitcnt lgkmcnt(2)\n"
    "buffer_store_dwordx4 v[68:71], v0, s[4:7], s45 offen sc1\n"
    "s_add_u32 s45, s45, 86016\n"
    "s_waitcnt lgkmcnt(0)\n"
    "buffer_store_dwordx4 v[72:75], v0, s[4:7], s45 offen sc1\n"
    "s_mov_b32 s45, 0\n"
    "s_branch Lpub_3%=\n"
    "Lln_2%=:\n"
    "v_lshrrev_b32 v9, 1, v7\n"
    "v_lshlrev_b32 v9, 10, v9\n"
    "v_and_b32 v10, 1, v1\n"
    "v_lshl_add_u32 v9, v10, 9, v9\n"
    "v_lshl_add_u32 v9, v6, 4, v9\n"
    "s_lshr_b32 s45, s2, 2\n"
    "s_mul_i32 s45, s45, 28672\n"
    "s_and_b32 s46, s2, 3\n"
    "s_lshl_b32 s46, s46, 7\n"
    "s_add_u32 s45, s45, s46\n"
    "s_add_u32 s45, s45, 524288\n"
    "s_mov_b32 s46, 0x3f803f80\n"
    "s_waitcnt vmcnt(7)\n"
    "v_dot2_f32_bf16 v40, v12, s46, 0\n"
    "v_dot2_f32_bf16 v41, v12, v12, 0\n"
    "v_dot2_f32_bf16 v40, v13, s46, v40\n"
    "v_dot2_f32_bf16 v41, v13, v13, v41\n"
    "v_dot2_f32_bf16 v40, v14, s46, v40\n"
    "v_dot2_f32_bf16 v41, v14, v14, v41\n"
    "v_dot2_f32_bf16 v40, v15, s46, v40\n"
    "v_dot2_f32_bf16 v41, v15, v15, v41\n"
    "s_waitcnt vmcnt(6)\n"
    "v_dot2_f32_bf16 v40, v16, s46, v40\n"
    "v_dot2_f32_bf16 v41, v16, v16, v41\n"
    "v_dot2_f32_bf16 v40, v17, s46, v40\n"
    "v_dot2_f32_bf16 v41, v17, v17, v41\n"
    "v_dot2_f32_bf16 v40, v18, s46, v40\n"
    "v_dot2_f32_bf16 v41, v18, v18, v41\n"
    "v_dot2_f32_bf16 v40, v19, s46, v40\n"
    "v_dot2_f32_bf16 v41, v19, v19, v41\n"
    "s_waitcnt vmcnt(5)\n"
    "v_dot2_f32_bf16 v40, v20, s46, v40\n"
    "v_dot2_f32_bf16 v41, v20, v20, v41\n"
    "v_dot2_f32_bf16 v40, v21, s46, v40\n"
    "v_dot2_f32_bf16 v41, v21, v21, v41\n"
    "v_dot2_f32_bf16 v40, v22, s46, v40\n"
    "v_dot2_f32_bf16 v41, v22, v22, v41\n"
    "v_dot2_f32_bf16 v40, v23, s46, v40\n"
    "v_dot2_f32_bf16 v41, v23, v23, v41\n"
    "s_waitcnt vmcnt(4)\n"
    "v_dot2_f32_bf16 v40, v24, s46, v40\n"
    "v_dot2_f32_bf16 v41, v24, v24, v41\n"
    "v_dot2_f32_bf16 v40, v25, s46, v40\n"
    "v_dot2_f32_bf16 v41, v25, v25, v41\n"
    "v_dot2_f32_bf16 v40, v26, s46, v40\n"
    "v_dot2_f32_bf16 v41, v26, v26, v41\n"
    "v_dot2_f32_bf16 v40, v27, s46, v40\n"
    "v_dot2_f32_bf16 v41, v27, v27, v41\n"
    "s_waitcnt vmcnt(3)\n"
    "v_dot2_f32_bf16 v40, v28, s46, v40\n"
    "v_dot2_f32_bf16 v41, v28, v28, v41\n"
    "v_dot2_f32_bf16 v40, v29, s46, v40\n"
    "v_dot2_f32_bf16 v41, v29, v29, v41\n"
    "v_dot2_f32_bf16 v40, v30, s46, v40\n"
    "v_dot2_f32_bf16 v41, v30, v30, v41\n"
    "v_dot2_f32_bf16 v40, v31, s46, v40\n"
    "v_dot2_f32_bf16 v41, v31, v31, v41\n"
    "s_waitcnt vmcnt(2)\n"
    "v_dot2_f32_bf16 v40, v32, s46, v40\n"
    "v_dot2_f32_bf16 v41, v32, v32, v41\n"
    "v_dot2_f32_bf16 v40, v33, s46, v40\n"
    "v_dot2_f32_bf16 v41, v33, v33, v41\n"
    "v_dot2_f32_bf16 v40, v34, s46, v40\n"
    "v_dot2_f32_bf16 v41, v34, v34, v41\n"
    "v_dot2_f32_bf16 v40, v35, s46, v40\n"
    "v_dot2_f32_bf16 v41, v35, v35, v41\n"
    "s_waitcnt vmcnt(1)\n"
    "v_dot2_f32_bf16 v40, v36, s46, v40\n"
    "v_dot2_f32_bf16 v41, v36, v36, v41\n"
    "v_dot2_f32_bf16 v40, v37, s46, v40\n"
    "v_dot2_f32_bf16 v41, v37, v37, v41\n"
    "v_dot2_f32_bf16 v40, v38, s46, v40\n"
    "v_dot2_f32_bf16 v41, v38, v38, v41\n"
    "v_dot2_f32_bf16 v40, v39, s46, v40\n"
    "v_dot2_f32_bf16 v41, v39, v39, v41\n"
    "s_nop 1\n"
    "v_add_f32_dpp v40, v40, v40 quad_perm:[1,0,3,2] row_mask:0xf bank_mask:0xf\n"
    "v_add_f32_dpp v41, v41, v41 quad_perm:[1,0,3,2] row_mask:0xf bank_mask:0xf\n"
    "s_nop 0\n"
    "v_add_f32_dpp v40, v40, v40 quad_perm:[2,3,0,1] row_mask:0xf bank_mask:0xf\n"
    "v_add_f32_dpp v41, v41, v41 quad_perm:[2,3,0,1] row_mask:0xf bank_mask:0xf\n"
    "s_nop 0\n"
    "v_add_f32_dpp v40, v40, v40 row_half_mirror row_mask:0xf bank_mask:0xf\n"
    "v_add_f32_dpp v41, v41, v41 row_half_mirror row_mask:0xf bank_mask:0xf\n"
    "v_mul_f32 v40, 0x3b124925, v40\n"
    "v_mul_f32 v41, 0x3b124925, v41\n"
    "v_fma_f32 v41, -v40, v40, v41\n"
    "v_max_f32 v41, 0, v41\n"
    "v_add_f32 v41, 0x3727c5ac, v41\n"
    "v_rsq_f32 v41, v41\n"
    "s_nop 0\n"
    "v_mul_f32 v40, v40, v41\n"
    "v_sub_f32 v40, 0, v40\n"
    "v_lshlrev_b32 v42, 16, v12\n"
    "v_and_b32 v43, 0xffff0000, v12\n"
    "v_fma_mixlo_f16 v12, v42, v41, v40\n"
    "v_fma_mixhi_f16 v12, v43, v41, v40\n"
    "v_lshlrev_b32 v44, 16, v13\n"
    "v_and_b32 v45, 0xffff0000, v13\n"
    "v_fma_mixlo_f16 v13, v44, v41, v40\n"
    "v_fma_mixhi_f16 v13, v45, v41, v40\n"
    "v_lshlrev_b32 v42, 16, v14\n"
    "v_and_b32 v43, 0xffff0000, v14\n"
    "v_fma_mixlo_f16 v14, v42, v41, v40\n"
    "v_fma_mixhi_f16 v14, v43, v41, v40\n"
    "v_lshlrev_b32 v44, 16, v15\n"
    "v_and_b32 v45, 0xffff0000, v15\n"
    "v_fma_mixlo_f16 v15, v44, v41, v40\n"
    "v_fma_mixhi_f16 v15, v45, v41, v40\n"
    "buffer_store_dwordx4 v[12:15], v9, s[4:7], s45 offen sc1\n"
    "s_add_u32 s45, s45, 4096\n"
    "v_lshlrev_b32 v42, 16, v16\n"
    "v_and_b32 v43, 0xffff0000, v16\n"
    "v_fma_mixlo_f16 v16, v42, v41, v40\n"
    "v_fma_mixhi_f16 v16, v43, v41, v40\n"
    "v_lshlrev_b32 v44, 16, v17\n"
    "v_and_b32 v45, 0xffff0000, v17\n"
    "v_fma_mixlo_f16 v17, v44, v41, v40\n"
    "v_fma_mixhi_f16 v17, v45, v41, v40\n"
    "v_lshlrev_b32 v42, 16, v18\n"
    "v_and_b32 v43, 0xffff0000, v18\n"
    "v_fma_mixlo_f16 v18, v42, v41, v40\n"
    "v_fma_mixhi_f16 v18, v43, v41, v40\n"
    "v_lshlrev_b32 v44, 16, v19\n"
    "v_and_b32 v45, 0xffff0000, v19\n"
    "v_fma_mixlo_f16 v19, v44, v41, v40\n"
    "v_fma_mixhi_f16 v19, v45, v41, v40\n"
    "buffer_store_dwordx4 v[16:19], v9, s[4:7], s45 offen sc1\n"
    "s_add_u32 s45, s45, 4096\n"
    "v_lshlrev_b32 v42, 16, v20\n"
    "v_and_b32 v43, 0xffff0000, v20\n"
    "v_fma_mixlo_f16 v20, v42, v41, v40\n"
    "v_fma_mixhi_f16 v20, v43, v41, v40\n"
    "v_lshlrev_b32 v44, 16, v21\n"
    "v_and_b32 v45, 0xffff0000, v21\n"
    "v_fma_mixlo_f16 v21, v44, v41, v40\n"
    "v_fma_mixhi_f16 v21, v45, v41, v40\n"
    "v_lshlrev_b32 v42, 16, v22\n"
    "v_and_b32 v43, 0xffff0000, v22\n"
    "v_fma_mixlo_f16 v22, v42, v41, v40\n"
    "v_fma_mixhi_f16 v22, v43, v41, v40\n"
    "v_lshlrev_b32 v44, 16, v23\n"
    "v_and_b32 v45, 0xffff0000, v23\n"
    "v_fma_mixlo_f16 v23, v44, v41, v40\n"
    "v_fma_mixhi_f16 v23, v45, v41, v40\n"
    "buffer_store_dwordx4 v[20:23], v9, s[4:7], s45 offen sc1\n"
    "s_add_u32 s45, s45, 4096\n"
    "v_lshlrev_b32 v42, 16, v24\n"
    "v_and_b32 v43, 0xffff0000, v24\n"
    "v_fma_mixlo_f16 v24, v42, v41, v40\n"
    "v_fma_mixhi_f16 v24, v43, v41, v40\n"
    "v_lshlrev_b32 v44, 16, v25\n"
    "v_and_b32 v45, 0xffff0000, v25\n"
    "v_fma_mixlo_f16 v25, v44, v41, v40\n"
    "v_fma_mixhi_f16 v25, v45, v41, v40\n"
    "v_lshlrev_b32 v42, 16, v26\n"
    "v_and_b32 v43, 0xffff0000, v26\n"
    "v_fma_mixlo_f16 v26, v42, v41, v40\n"
    "v_fma_mixhi_f16 v26, v43, v41, v40\n"
    "v_lshlrev_b32 v44, 16, v27\n"
    "v_and_b32 v45, 0xffff0000, v27\n"
    "v_fma_mixlo_f16 v27, v44, v41, v40\n"
    "v_fma_mixhi_f16 v27, v45, v41, v40\n"
    "buffer_store_dwordx4 v[24:27], v9, s[4:7], s45 offen sc1\n"
    "s_add_u32 s45, s45, 4096\n"
    "v_lshlrev_b32 v42, 16, v28\n"
    "v_and_b32 v43, 0xffff0000, v28\n"
    "v_fma_mixlo_f16 v28, v42, v41, v40\n"
    "v_fma_mixhi_f16 v28, v43, v41, v40\n"
    "v_lshlrev_b32 v44, 16, v29\n"
    "v_and_b32 v45, 0xffff0000, v29\n"
    "v_fma_mixlo_f16 v29, v44, v41, v40\n"
    "v_fma_mixhi_f16 v29, v45, v41, v40\n"
    "v_lshlrev_b32 v42, 16, v30\n"
    "v_and_b32 v43, 0xffff0000, v30\n"
    "v_fma_mixlo_f16 v30, v42, v41, v40\n"
    "v_fma_mixhi_f16 v30, v43, v41, v40\n"
    "v_lshlrev_b32 v44, 16, v31\n"
    "v_and_b32 v45, 0xffff0000, v31\n"
    "v_fma_mixlo_f16 v31, v44, v41, v40\n"
    "v_fma_mixhi_f16 v31, v45, v41, v40\n"
    "buffer_store_dwordx4 v[28:31], v9, s[4:7], s45 offen sc1\n"
    "s_add_u32 s45, s45, 4096\n"
    "v_lshlrev_b32 v42, 16, v32\n"
    "v_and_b32 v43, 0xffff0000, v32\n"
    "v_fma_mixlo_f16 v32, v42, v41, v40\n"
    "v_fma_mixhi_f16 v32, v43, v41, v40\n"
    "v_lshlrev_b32 v44, 16, v33\n"
    "v_and_b32 v45, 0xffff0000, v33\n"
    "v_fma_mixlo_f16 v33, v44, v41, v40\n"
    "v_fma_mixhi_f16 v33, v45, v41, v40\n"
    "v_lshlrev_b32 v42, 16, v34\n"
    "v_and_b32 v43, 0xffff0000, v34\n"
    "v_fma_mixlo_f16 v34, v42, v41, v40\n"
    "v_fma_mixhi_f16 v34, v43, v41, v40\n"
    "v_lshlrev_b32 v44, 16, v35\n"
    "v_and_b32 v45, 0xffff0000, v35\n"
    "v_fma_mixlo_f16 v35, v44, v41, v40\n"
    "v_fma_mixhi_f16 v35, v45, v41, v40\n"
    "buffer_store_dwordx4 v[32:35], v9, s[4:7], s45 offen sc1\n"
    "s_add_u32 s45, s45, 4096\n"
    "v_lshlrev_b32 v42, 16, v36\n"
    "v_and_b32 v43, 0xffff0000, v36\n"
    "v_fma_mixlo_f16 v36, v42, v41, v40\n"
    "v_fma_mixhi_f16 v36, v43, v41, v40\n"
    "v_lshlrev_b32 v44, 16, v37\n"
    "v_and_b32 v45, 0xffff0000, v37\n"
    "v_fma_mixlo_f16 v37, v44, v41, v40\n"
    "v_fma_mixhi_f16 v37, v45, v41, v40\n"
    "v_lshlrev_b32 v42, 16, v38\n"
    "v_and_b32 v43, 0xffff0000, v38\n"
    "v_fma_mixlo_f16 v38, v42, v41, v40\n"
    "v_fma_mixhi_f16 v38, v43, v41, v40\n"
    "v_lshlrev_b32 v44, 16, v39\n"
    "v_and_b32 v45, 0xffff0000, v39\n"
    "v_fma_mixlo_f16 v39, v44, v41, v40\n"
    "v_fma_mixhi_f16 v39, v45, v41, v40\n"
    "buffer_store_dwordx4 v[36:39], v9, s[4:7], s45 offen sc1\n"
    "s_lshl_b32 s44, s2, 3\n"
    "s_add_u32 s44, s44, 4096\n"
    "s_mov_b32 s45, 0\n"
    "s_branch Lpub_3%=\n"
    "Lidle_4%=:\n"
    "s_mov_b32 exec_hi, -1\n"
    "s_waitcnt vmcnt(0)\n"
    "s_mov_b32 s44, 0\n"
    "s_mov_b32 s45, 0\n"
    "Lpub_3%=:\n"
    "s_and_b32 s21, s19, 3\n"
    "s_lshl_b32 s21, s21, 2\n"
    "s_and_b32 s50, s20, 3\n"
    "s_add_u32 s21, s21, s50\n"
    "s_lshr_b32 s22, s19, 2\n"
    "s_lshl_b32 s22, s22, 3\n"
    "s_lshr_b32 s50, s20, 2\n"
    "s_add_u32 s22, s22, s50\n"
    "s_lshl_b32 s23, s22, 2\n"
    "s_add_u32 s23, s23, s18\n"
    "s_lshl_b32 s71, s19, 2\n"
    "s_and_b32 s50, s20, 3\n"
    "s_add_u32 s71, s71, s50\n"
    "s_lshr_b32 s72, s20, 2\n"
    "s_min_u32 s72, s72, 6\n"
    "s_lshr_b32 s73, s19, 1\n"
    "s_lshl_b32 s73, s73, 2\n"
    "s_and_b32 s50, s20, 3\n"
    "s_add_u32 s73, s73, s50\n"
    "s_and_b32 s74, s19, 1\n"
    "s_lshl_b32 s74, s74, 3\n"
    "s_lshr_b32 s50, s20, 2\n"
    "s_add_u32 s74, s74, s50\n"
    "s_lshl_b32 s75, s73, 2\n"
    "s_add_u32 s75, s75, s18\n"
    "s_cmp_ge_u32 s20, 28\n"
    "s_cselect_b64 s[68:69], -1, 0\n"
    "s_lshr_b32 s50, s20, 1\n"
    "s_mul_i32 s50, s50, 2816\n"
    "s_add_u32 s50, s50, 81920\n"
    "s_lshr_b32 s51, s21, 3\n"
    "s_lshl_b32 s51, s51, 2\n"
    "s_add_u32 s51, s51, s18\n"
    "s_lshl_b32 s51, s51, 8\n"
    "s_add_u32 s51, s51, s50\n"
    "s_lshl_b32 s52, s18, 7\n"
    "s_add_u32 s50, s50, s52\n"
    "s_add_u32 s50, s50, 1848\n"
    "s_lshl_b32 s52, s23, 5\n"
    "s_add_u32 s52, s52, 3928\n"
    "v_lshlrev_b32 v9, 3, v1\n"
    "v_add_u32 v8, s51, v9\n"
    "v_add_u32 v7, s52, v9\n"
    "v_cmp_le_u32 vcc, 21, v1\n"
    "s_nop 1\n"
    "v_cndmask_b32 v8, v8, v7, vcc\n"
    "v_add_u32 v7, s50, v9\n"
    "v_cmp_le_u32 vcc, 25, v1\n"
    "s_nop 1\n"
    "v_cndmask_b32 v8, v8, v7, vcc\n"
    "s_cmp_lt_u32 s18, 2\n"
    "s_cselect_b32 s49, 0x1ff, 31\n"
    "v_bfe_u32 v9, v1, 2, 2\n"
    "v_lshlrev_b32 v10, 2, v9\n"
    "v_add_u32 v10, s18, v10\n"
    "v_mov_b32 v11, s18\n"
    "v_cmp_lt_u32 vcc, 13, v10\n"
    "s_nop 1\n"
    "v_cndmask_b32 v10, v10, v11, vcc\n"
    "v_cmp_eq_u32 s[60:61], 1, v9\n"
    "v_cmp_le_u32 vcc, 2, v9\n"
    "v_mov_b32 v11, 14\n"
    "v_cndmask_b32 v11, v11, 28, s[60:61]\n"
    "v_cndmask_b32 v11, v11, 35, vcc\n"
    "v_add_u32 v11, s72, v11\n"
    "v_add_u32 v10, 21, v10\n"
    "v_cndmask_b32 v11, v11, v10, s[68:69]\n"
    "v_and_b32 v9, 3, v1\n"
    "v_lshl_add_u32 v11, v11, 2, v9\n"
    "v_lshlrev_b32 v11, 3, v11\n"
    "v_add_u32 v240, 6144, v11\n"
    "v_lshrrev_b32 v9, 4, v1\n"
    "v_lshlrev_b32 v10, 2, v9\n"
    "v_add_u32 v10, s18, v10\n"
    "v_mov_b32 v11, s18\n"
    "v_cmp_lt_u32 vcc, 13, v10\n"
    "s_nop 1\n"
    "v_cndmask_b32 v10, v10, v11, vcc\n"
    "v_cmp_eq_u32 s[60:61], 1, v9\n"
    "v_cmp_le_u32 vcc, 2, v9\n"
    "v_mov_b32 v11, 14\n"
    "v_cndmask_b32 v11, v11, 28, s[60:61]\n"
    "v_cndmask_b32 v11, v11, 35, vcc\n"
    "v_add_u32 v11, s72, v11\n"
    "v_and_b32 v14, 15, v1\n"
    "v_lshlrev_b32 v14, 4, v14\n"
    "v_lshl_add_u32 v13, v10, 8, v14\n"
    "v_lshl_add_u32 v12, v10, 10, v14\n"
    "v_cmp_lt_u32 s[62:63], 6, v10\n"
    "v_min_u32 v9, 2, v9\n"
    "v_lshl_add_u32 v245, v9, 8, v14\n"
    "v_add_u32 v245, 155136, v245\n"
    "v_add_u32 v15, 155968, v13\n"
    "v_cndmask_b32 v245, v245, v15, s[68:69]\n"
    "v_add_u32 v13, 151552, v13\n"
    "v_lshl_add_u32 v244, v11, 10, v14\n"
    "v_add_u32 v15, 21504, v12\n"
    "v_cndmask_b32 v244, v244, v15, s[68:69]\n"
    "s_cmp_ge_u32 s20, 28\n"
    "s_cselect_b64 s[60:61], s[62:63], s[60:61]\n"
    "s_mul_i32 s46, s21, 86016\n"
    "s_add_u32 s46, s46, 2359296\n"
    "s_mul_i32 s47, s23, 28672\n"
    "s_add_u32 s47, s47, 524288\n"
    "s_mov_b32 s48, 126976\n"
    "v_mul_u32_u24 v6, 2816, v1\n"
    "s_waitcnt vmcnt(0)\n"
    "v_readfirstlane_b32 s16, v4\n"
    "v_readfirstlane_b32 s17, v5\n"
    "s_add_u32 s16, s16, 1\n"
    "s_addc_u32 s17, s17, 0\n"
    "v_mov_b32 v254, s16\n"
    "v_mov_b32 v255, s17\n"
    "s_mov_b64 exec, s[64:65]\n"
    "s_cmp_eq_u32 s44, 0\n"
    "s_cbranch_scc1 Lnopub_5%=\n"
    "buffer_atomic_swap_x2 v[254:255], v6, s[4:7], s44 offen sc1\n"
    "s_cmp_eq_u32 s45, 0\n"
    "s_cbranch_scc1 Lnopub_5%=\n"
    "buffer_atomic_swap_x2 v[254:255], v6, s[4:7], s45 offen sc1\n"
    "s_nop 1\n"
    "Lnopub_5%=:\n"
    "s_mov_b64 exec, -1\n"
    "v_add_u32 v2, 4096, v0\n"
    "v_add_u32 v3, 8192, v0\n"
    "v_add_u32 v4, 12288, v0\n"
    "v_lshrrev_b32 v5, 5, v1\n"
    "v_lshlrev_b32 v5, 5, v5\n"
    "v_add_u32 v5, 151552, v5\n"
    "v_add_u32 v6, 0x10000, v0\n"
    "s_mov_b32 s8, s24\n"
    "s_and_b32 s9, s25, 0xffff\n"
    "s_mov_b32 s10, 1835008\n"
    "s_mov_b32 s11, 0x20000\n"
    "s_mov_b32 s36, s28\n"
    "s_and_b32 s37, s29, 0xffff\n"
    "s_mov_b32 s38, 401408\n"
    "s_mov_b32 s39, 0x20000\n"
    "s_lshl_b32 s50, s2, 2\n"
    "s_add_u32 s50, s50, s18\n"
    "s_lshl_b32 s41, s18, 13\n"
    "s_add_u32 s40, s41, 86016\n"
    "s_cmp_ge_u32 s50, 392\n"
    "s_cbranch_scc1 Ltd_6%=\n"
    "s_mov_b32 s96, s32\n"
    "s_and_b32 s97, s33, 0xffff\n"
    "s_mov_b32 s98, 1605632\n"
    "s_mov_b32 s99, 0x20000\n"
    "s_mov_b32 s32, s30\n"
    "s_and_b32 s33, s31, 0xffff\n"
    "s_mov_b32 s34, 1605632\n"
    "s_mov_b32 s35, 0x20000\n"
    "s_mul_i32 s51, s50, 2341\n"
    "s_lshr_b32 s51, s51, 16\n"
    "s_mul_i32 s59, s51, 28\n"
    "s_sub_u32 s59, s50, s59\n"
    "s_mul_i32 s0, s51, 114688\n"
    "s_lshl_b32 s1, s59, 7\n"
    "s_add_u32 s0, s0, s1\n"
    "s_mov_b32 s1, s0\n"
    "s_mul_i32 s24, s59, 116\n"
    "s_lshl_b32 s45, s51, 1\n"
    "s_add_u32 s24, s24, s45\n"
    "s_lshl_b32 s24, s24, 10\n"
    "s_add_u32 s24, s24, 4194304\n"
    "s_mul_i32 s3, s59, 14\n"
    "s_add_u32 s3, s3, s51\n"
    "s_lshl_b32 s3, s3, 3\n"
    "s_add_u32 s3, s3, 8192\n"
    "s_mov_b32 s31, 1\n"
    "s_movk_i32 s25, 29696\n"
    "s_branch Lte_7%=\n"
    "Ltd_6%=:\n"
    "s_mov_b32 s32, s34\n"
    "s_and_b32 s33, s35, 0xffff\n"
    "s_mov_b32 s34, 1605632\n"
    "s_mov_b32 s35, 0x20000\n"
    "s_sub_u32 s50, s50, 392\n"
    "s_mul_i32 s51, s50, 2341\n"
    "s_lshr_b32 s51, s51, 16\n"
    "s_mul_i32 s59, s51, 28\n"
    "s_sub_u32 s59, s50, s59\n"
    "s_mul_i32 s0, s59, 57344\n"
    "s_lshl_b32 s1, s51, 7\n"
    "s_add_u32 s0, s0, s1\n"
    "s_add_u32 s1, s0, 28672\n"
    "s_lshl_b32 s24, s50, 13\n"
    "s_add_u32 s24, s24, 7520256\n"
    "s_lshl_b32 s3, s50, 3\n"
    "s_add_u32 s3, s3, 11328\n"
    "s_cmp_lt_u32 s50, 196\n"
    "s_cselect_b32 s34, s34, 0\n"
    "s_cselect_b32 s24, s24, 24723456\n"
    "s_cselect_b32 s3, s3, 24723456\n"
    "s_mov_b64 s[96:97], s[32:33]\n"
    "s_mov_b64 s[98:99], s[34:35]\n"
    "s_mov_b32 s31, 0\n"
    "s_movk_i32 s25, 2048\n"
    "Lte_7%=:\n"
    "s_mul_i32 s26, s18, 100352\n"
    "s_lshl_b32 s27, s72, 7\n"
    "s_add_u32 s26, s26, s27\n"
    "s_add_u32 s27, s26, 25088\n"
    "s_add_u32 s28, s26, 50176\n"
    "s_add_u32 s29, s27, 50176\n"
    "s_barrier\n"
    "s_mul_i32 s52, s18, 3072\n"
    "s_add_u32 s52, s52, 0\n"
    "s_add_u32 s55, s52, s46\n"
    "s_mul_i32 s53, s18, 6144\n"
    "s_add_u32 s53, s53, 12288\n"
    "s_add_u32 s56, s53, s46\n"
    "s_mul_i32 s54, s18, 12288\n"
    "s_add_u32 s54, s54, 36864\n"
    "s_add_u32 s57, s54, s46\n"
    "s_add_u32 s58, s47, 16384\n"
    "s_mov_b32 s70, 0\n"
    "s_mov_b32 exec_hi, s49\n"
    "Lpoll_8%=:\n"
    "v_mov_b32 v16, 0\n"
    "v_mov_b32 v17, 0\n"
    "buffer_atomic_or_x2 v[16:17], v8, s[4:7], 0 offen sc0 sc1\n"
    "s_waitcnt vmcnt(0)\n"
    "v_cmp_ne_u64 vcc, s[16:17], v[16:17]\n"
    "s_nop 0\n"
    "s_and_b64 exec, exec, vcc\n"
    "s_cbranch_scc0 Lpollok_9%=\n"
    "s_sleep 2\n"
    "s_add_u32 s70, s70, 1\n"
    "s_cmp_lt_u32 s70, 0x40000\n"
    "s_cbranch_scc1 Lpoll_8%=\n"
    "Lpollok_9%=:\n"
    "s_mov_b64 exec, -1\n"
    "buffer_load_dwordx4 v[64:67], v12, s[4:7], s48 offen offset:0 sc1\n"
    "buffer_load_dwordx4 v[68:71], v12, s[4:7], s48 offen offset:256 sc1\n"
    "buffer_load_dwordx4 v[72:75], v12, s[4:7], s48 offen offset:512 sc1\n"
    "buffer_load_dwordx4 v[76:79], v12, s[4:7], s48 offen offset:768 sc1\n"
    "s_add_u32 m0, s52, 0\n"
    "s_nop 0\n"
    "buffer_load_dwordx4 v0, s[4:7], s55 offen offset:0 sc1 lds\n"
    "buffer_load_dwordx4 v0, s[4:7], s55 offen offset:1024 sc1 lds\n"
    "buffer_load_dwordx4 v0, s[4:7], s55 offen offset:2048 sc1 lds\n"
    "buffer_load_dwordx4 v[128:131], v0, s[4:7], s47 offen offset:0 sc1\n"
    "buffer_load_dwordx4 v[132:135], v0, s[4:7], s47 offen offset:1024 sc1\n"
    "buffer_load_dwordx4 v[136:139], v0, s[4:7], s47 offen offset:2048 sc1\n"
    "buffer_load_dwordx4 v[140:143], v0, s[4:7], s47 offen offset:3072 sc1\n"
    "s_add_u32 m0, s53, 0\n"
    "s_nop 0\n"
    "buffer_load_dwordx4 v0, s[4:7], s56 offen offset:0 sc1 lds\n"
    "buffer_load_dwordx4 v0, s[4:7], s56 offen offset:1024 sc1 lds\n"
    "buffer_load_dwordx4 v0, s[4:7], s56 offen offset:2048 sc1 lds\n"
    "buffer_load_dwordx4 v0, s[4:7], s56 offen offset:3072 sc1 lds\n"
    "s_add_u32 m0, s53, 4096\n"
    "s_nop 0\n"
    "buffer_load_dwordx4 v2, s[4:7], s56 offen offset:0 sc1 lds\n"
    "buffer_load_dwordx4 v2, s[4:7], s56 offen offset:1024 sc1 lds\n"
    "s_waitcnt vmcnt(15)\n"
    "v_add_f32 v64, v64, v68\n"
    "v_add_f32 v65, v65, v69\n"
    "v_add_f32 v66, v66, v70\n"
    "v_add_f32 v67, v67, v71\n"
    "s_waitcnt vmcnt(13)\n"
    "v_add_f32 v72, v72, v76\n"
    "v_add_f32 v73, v73, v77\n"
    "v_add_f32 v74, v74, v78\n"
    "v_add_f32 v75, v75, v79\n"
    "v_add_f32 v64, v64, v72\n"
    "v_add_f32 v65, v65, v73\n"
    "v_add_f32 v66, v66, v74\n"
    "v_add_f32 v67, v67, v75\n"
    "v_cvt_pk_bf16_f32 v68, v64, v65\n"
    "v_cvt_pk_bf16_f32 v69, v66, v67\n"
    "v_lshlrev_b32 v64, 16, v68\n"
    "v_and_b32 v65, 0xffff0000, v68\n"
    "v_lshlrev_b32 v66, 16, v69\n"
    "v_and_b32 v67, 0xffff0000, v69\n"
    "v_add_f32 v72, 1.0, v64\n"
    "v_add_f32 v73, 1.0, v65\n"
    "v_add_f32 v74, 1.0, v66\n"
    "v_add_f32 v75, 1.0, v67\n"
    "v_cvt_pk_bf16_f32 v68, v72, v73\n"
    "v_cvt_pk_bf16_f32 v69, v74, v75\n"
    "v_lshlrev_b32 v72, 16, v68\n"
    "v_and_b32 v73, 0xffff0000, v68\n"
    "v_lshlrev_b32 v74, 16, v69\n"
    "v_and_b32 v75, 0xffff0000, v69\n"
    "v_cndmask_b32 v64, v64, v72, s[62:63]\n"
    "v_cndmask_b32 v65, v65, v73, s[62:63]\n"
    "v_cndmask_b32 v66, v66, v74, s[62:63]\n"
    "v_cndmask_b32 v67, v67, v75, s[62:63]\n"
    "ds_write_b128 v13, v[64:67]\n"
    "s_waitcnt vmcnt(10)\n"
    "s_waitcnt lgkmcnt(0)\n"
    "s_barrier\n"
    "ds_read_b128 v[16:19], v5 offset:0\n"
    "ds_read_b128 v[20:23], v5 offset:16\n"
    "ds_read_b128 v[24:27], v5 offset:1792\n"
    "ds_read_b128 v[28:31], v5 offset:1808\n"
    "ds_read_b128 v[48:51], v0 offset:0\n"
    "ds_read_b128 v[52:55], v0 offset:1024\n"
    "ds_read_b128 v[56:59], v0 offset:2048\n"
    "ds_read_b128 v[32:35], v5 offset:64\n"
    "ds_read_b128 v[36:39], v5 offset:80\n"
    "ds_read_b128 v[40:43], v5 offset:1856\n"
    "ds_read_b128 v[44:47], v5 offset:1872\n"
    "buffer_load_dwordx4 v[144:147], v2, s[4:7], s47 offen offset:0 sc1\n"
    "buffer_load_dwordx4 v[148:151], v2, s[4:7], s47 offen offset:1024 sc1\n"
    "buffer_load_dwordx4 v[152:155], v2, s[4:7], s47 offen offset:2048 sc1\n"
    "s_waitcnt vmcnt(12) lgkmcnt(8)\n"
    "v_fma_mix_f32 v8, v128, v24, v16 op_sel:[0,0,0] op_sel_hi:[1,0,0]\n"
    "v_fma_mix_f32 v9, v128, v25, v17 op_sel:[1,0,0] op_sel_hi:[1,0,0]\n"
    "v_fma_mix_f32 v10, v129, v26, v18 op_sel:[0,0,0] op_sel_hi:[1,0,0]\n"
    "v_fma_mix_f32 v11, v129, v27, v19 op_sel:[1,0,0] op_sel_hi:[1,0,0]\n"
    "s_waitcnt lgkmcnt(7)\n"
    "v_fma_mix_f32 v12, v130, v28, v20 op_sel:[0,0,0] op_sel_hi:[1,0,0]\n"
    "v_fma_mix_f32 v13, v130, v29, v21 op_sel:[1,0,0] op_sel_hi:[1,0,0]\n"
    "v_fma_mix_f32 v14, v131, v30, v22 op_sel:[0,0,0] op_sel_hi:[1,0,0]\n"
    "v_fma_mix_f32 v15, v131, v31, v23 op_sel:[1,0,0] op_sel_hi:[1,0,0]\n"
    "v_cvt_pk_bf16_f32 v60, v8, v9\n"
    "v_cvt_pk_bf16_f32 v61, v10, v11\n"
    "v_cvt_pk_bf16_f32 v62, v12, v13\n"
    "v_cvt_pk_bf16_f32 v63, v14, v15\n"
    "s_waitcnt lgkmcnt(6)\n"
    "s_nop 0\n"
    "v_mfma_f32_32x32x16_bf16 v[64:79], v[48:51], v[60:63], 0\n"
    "ds_read_b128 v[112:115], v0 offset:3072\n"
    "ds_read_b128 v[116:119], v0 offset:4096\n"
    "ds_read_b128 v[120:123], v0 offset:5120\n"
    "s_waitcnt vmcnt(11) lgkmcnt(4)\n"
    "v_fma_mix_f32 v8, v132, v40, v32 op_sel:[0,0,0] op_sel_hi:[1,0,0]\n"
    "v_fma_mix_f32 v9, v132, v41, v33 op_sel:[1,0,0] op_sel_hi:[1,0,0]\n"
    "v_fma_mix_f32 v10, v133, v42, v34 op_sel:[0,0,0] op_sel_hi:[1,0,0]\n"
    "v_fma_mix_f32 v11, v133, v43, v35 op_sel:[1,0,0] op_sel_hi:[1,0,0]\n"
    "v_mfma_f32_32x32x16_bf16 v[80:95], v[52:55], v[60:63], 0\n"
    "ds_read_b128 v[16:19], v5 offset:128\n"
    "ds_read_b128 v[20:23], v5 offset:144\n"
    "buffer_load_dwordx4 v[156:159], v2, s[4:7], s47 offen offset:3072 sc1\n"
    "s_waitcnt lgkmcnt(5)\n"
    "v_fma_mix_f32 v12, v134, v44, v36 op_sel:[0,0,0] op_sel_hi:[1,0,0]\n"
    "v_fma_mix_f32 v13, v134, v45, v37 op_sel:[1,0,0] op_sel_hi:[1,0,0]\n"
    "v_fma_mix_f32 v14, v135, v46, v38 op_sel:[0,0,0] op_sel_hi:[1,0,0]\n"
    "v_fma_mix_f32 v15, v135, v47, v39 op_sel:[1,0,0] op_sel_hi:[1,0,0]\n"
    "v_mfma_f32_32x32x16_bf16 v[96:111], v[60:63], v[56:59], 0\n"
    "ds_read_b128 v[24:27], v5 offset:1920\n"
    "ds_read_b128 v[28:31], v5 offset:1936\n"
    "buffer_load_dwordx4 v[160:163], v3, s[4:7], s47 offen offset:0 sc1\n"
    "s_waitcnt lgkmcnt(4)\n"
    "v_cvt_pk_bf16_f32 v124, v8, v9\n"
    "v_cvt_pk_bf16_f32 v125, v10, v11\n"
    "v_cvt_pk_bf16_f32 v126, v12, v13\n"
    "v_cvt_pk_bf16_f32 v127, v14, v15\n"
    "s_nop 1\n"
    "v_mfma_f32_32x32x16_bf16 v[64:79], v[112:115], v[124:127], v[64:79]\n"
    "ds_read_b128 v[48:51], v0 offset:6144\n"
    "ds_read_b128 v[52:55], v0 offset:7168\n"
    "ds_read_b128 v[56:59], v0 offset:8192\n"
    "s_waitcnt vmcnt(12) lgkmcnt(4)\n"
    "v_fma_mix_f32 v8, v136, v24, v16 op_sel:[0,0,0] op_sel_hi:[1,0,0]\n"
    "v_fma_mix_f32 v9, v136, v25, v17 op_sel:[1,0,0] op_sel_hi:[1,0,0]\n"
    "v_fma_mix_f32 v10, v137, v26, v18 op_sel:[0,0,0] op_sel_hi:[1,0,0]\n"
    "v_fma_mix_f32 v11, v137, v27, v19 op_sel:[1,0,0] op_sel_hi:[1,0,0]\n"
    "v_mfma_f32_32x32x16_bf16 v[80:95], v[116:119], v[124:127], v[80:95]\n"
    "ds_read_b128 v[32:35], v5 offset:192\n"
    "ds_read_b128 v[36:39], v5 offset:208\n"
    "buffer_load_dwordx4 v[164:167], v3, s[4:7], s47 offen offset:1024 sc1\n"
    "s_waitcnt lgkmcnt(5)\n"
    "v_fma_mix_f32 v12, v138, v28, v20 op_sel:[0,0,0] op_sel_hi:[1,0,0]\n"
    "v_fma_mix_f32 v13, v138, v29, v21 op_sel:[1,0,0] op_sel_hi:[1,0,0]\n"
    "v_fma_mix_f32 v14, v139, v30, v22 op_sel:[0,0,0] op_sel_hi:[1,0,0]\n"
    "v_fma_mix_f32 v15, v139, v31, v23 op_sel:[1,0,0] op_sel_hi:[1,0,0]\n"
    "v_mfma_f32_32x32x16_bf16 v[96:111], v[124:127], v[120:123], v[96:111]\n"
    "ds_read_b128 v[40:43], v5 offset:1984\n"
    "ds_read_b128 v[44:47], v5 offset:2000\n"
    "buffer_load_dwordx4 v[168:171], v3, s[4:7], s47 offen offset:2048 sc1\n"
    "s_waitcnt lgkmcnt(4)\n"
    "v_cvt_pk_bf16_f32 v60, v8, v9\n"
    "v_cvt_pk_bf16_f32 v61, v10, v11\n"
    "v_cvt_pk_bf16_f32 v62, v12, v13\n"
    "v_cvt_pk_bf16_f32 v63, v14, v15\n"
    "s_nop 1\n"
    "v_mfma_f32_32x32x16_bf16 v[64:79], v[48:51], v[60:63], v[64:79]\n"
    "ds_read_b128 v[112:115], v0 offset:9216\n"
    "ds_read_b128 v[116:119], v0 offset:10240\n"
    "ds_read_b128 v[120:123], v0 offset:11264\n"
    "s_waitcnt vmcnt(13) lgkmcnt(4)\n"
    "v_fma_mix_f32 v8, v140, v40, v32 op_sel:[0,0,0] op_sel_hi:[1,0,0]\n"
    "v_fma_mix_f32 v9, v140, v41, v33 op_sel:[1,0,0] op_sel_hi:[1,0,0]\n"
    "v_fma_mix_f32 v10, v141, v42, v34 op_sel:[0,0,0] op_sel_hi:[1,0,0]\n"
    "v_fma_mix_f32 v11, v141, v43, v35 op_sel:[1,0,0] op_sel_hi:[1,0,0]\n"
    "v_mfma_f32_32x32x16_bf16 v[80:95], v[52:55], v[60:63], v[80:95]\n"
    "ds_read_b128 v[16:19], v5 offset:256\n"
    "ds_read_b128 v[20:23], v5 offset:272\n"
    "buffer_load_dwordx4 v[172:175], v3, s[4:7], s47 offen offset:3072 sc1\n"
    "s_waitcnt lgkmcnt(5)\n"
    "v_fma_mix_f32 v12, v142, v44, v36 op_sel:[0,0,0] op_sel_hi:[1,0,0]\n"
    "v_fma_mix_f32 v13, v142, v45, v37 op_sel:[1,0,0] op_sel_hi:[1,0,0]\n"
    "v_fma_mix_f32 v14, v143, v46, v38 op_sel:[0,0,0] op_sel_hi:[1,0,0]\n"
    "v_fma_mix_f32 v15, v143, v47, v39 op_sel:[1,0,0] op_sel_hi:[1,0,0]\n"
    "v_mfma_f32_32x32x16_bf16 v[96:111], v[60:63], v[56:59], v[96:111]\n"
    "ds_read_b128 v[24:27], v5 offset:2048\n"
    "ds_read_b128 v[28:31], v5 offset:2064\n"
    "s_add_u32 m0, s54, 0\n"
    "s_nop 0\n"
    "buffer_load_dwordx4 v0, s[4:7], s57 offen offset:0 sc1 lds\n"
    "s_waitcnt lgkmcnt(4)\n"
    "v_cvt_pk_bf16_f32 v124, v8, v9\n"
    "v_cvt_pk_bf16_f32 v125, v10, v11\n"
    "v_cvt_pk_bf16_f32 v126, v12, v13\n"
    "v_cvt_pk_bf16_f32 v127, v14, v15\n"
    "s_waitcnt vmcnt(9)\n"
    "s_waitcnt lgkmcnt(0)\n"
    "s_barrier\n"
    "v_mfma_f32_32x32x16_bf16 v[64:79], v[112:115], v[124:127], v[64:79]\n"
    "ds_read_b128 v[48:51], v0 offset:12288\n"
    "ds_read_b128 v[52:55], v0 offset:13312\n"
    "ds_read_b128 v[56:59], v0 offset:14336\n"
    "s_waitcnt vmcnt(8)\n"
    "v_fma_mix_f32 v8, v144, v24, v16 op_sel:[0,0,0] op_sel_hi:[1,0,0]\n"
    "v_fma_mix_f32 v9, v144, v25, v17 op_sel:[1,0,0] op_sel_hi:[1,0,0]\n"
    "v_fma_mix_f32 v10, v145, v26, v18 op_sel:[0,0,0] op_sel_hi:[1,0,0]\n"
    "v_fma_mix_f32 v11, v145, v27, v19 op_sel:[1,0,0] op_sel_hi:[1,0,0]\n"
    "v_mfma_f32_32x32x16_bf16 v[80:95], v[116:119], v[124:127], v[80:95]\n"
    "ds_read_b128 v[32:35], v5 offset:320\n"
    "ds_read_b128 v[36:39], v5 offset:336\n"
    "buffer_load_dwordx4 v0, s[4:7], s57 offen offset:1024 sc1 lds\n"
    "v_fma_mix_f32 v12, v146, v28, v20 op_sel:[0,0,0] op_sel_hi:[1,0,0]\n"
    "v_fma_mix_f32 v13, v146, v29, v21 op_sel:[1,0,0] op_sel_hi:[1,0,0]\n"
    "v_fma_mix_f32 v14, v147, v30, v22 op_sel:[0,0,0] op_sel_hi:[1,0,0]\n"
    "v_fma_mix_f32 v15, v147, v31, v23 op_sel:[1,0,0] op_sel_hi:[1,0,0]\n"
    "v_mfma_f32_32x32x16_bf16 v[96:111], v[124:127], v[120:123], v[96:111]\n"
    "ds_read_b128 v[40:43], v5 offset:2112\n"
    "ds_read_b128 v[44:47], v5 offset:2128\n"
    "buffer_load_dwordx4 v0, s[4:7], s57 offen offset:2048 sc1 lds\n"
    "s_waitcnt lgkmcnt(4)\n"
    "v_cvt_pk_bf16_f32 v60, v8, v9\n"
    "v_cvt_pk_bf16_f32 v61, v10, v11\n"
    "v_cvt_pk_bf16_f32 v62, v12, v13\n"
    "v_cvt_pk_bf16_f32 v63, v14, v15\n"
    "s_nop 1\n"
    "v_mfma_f32_32x32x16_bf16 v[64:79], v[48:51], v[60:63], v[64:79]\n"
    "ds_read_b128 v[112:115], v0 offset:15360\n"
    "ds_read_b128 v[116:119], v0 offset:16384\n"
    "ds_read_b128 v[120:123], v0 offset:17408\n"
    "s_waitcnt vmcnt(9) lgkmcnt(4)\n"
    "v_fma_mix_f32 v8, v148, v40, v32 op_sel:[0,0,0] op_sel_hi:[1,0,0]\n"
    "v_fma_mix_f32 v9, v148, v41, v33 op_sel:[1,0,0] op_sel_hi:[1,0,0]\n"
    "v_fma_mix_f32 v10, v149, v42, v34 op_sel:[0,0,0] op_sel_hi:[1,0,0]\n"
    "v_fma_mix_f32 v11, v149, v43, v35 op_sel:[1,0,0] op_sel_hi:[1,0,0]\n"
    "v_mfma_f32_32x32x16_bf16 v[80:95], v[52:55], v[60:63], v[80:95]\n"
    "ds_read_b128 v[16:19], v5 offset:384\n"
    "ds_read_b128 v[20:23], v5 offset:400\n"
    "buffer_load_dwordx4 v0, s[4:7], s57 offen offset:3072 sc1 lds\n"
    "s_waitcnt lgkmcnt(5)\n"
    "v_fma_mix_f32 v12, v150, v44, v36 op_sel:[0,0,0] op_sel_hi:[1,0,0]\n"
    "v_fma_mix_f32 v13, v150, v45, v37 op_sel:[1,0,0] op_sel_hi:[1,0,0]\n"
    "v_fma_mix_f32 v14, v151, v46, v38 op_sel:[0,0,0] op_sel_hi:[1,0,0]\n"
    "v_fma_mix_f32 v15, v151, v47, v39 op_sel:[1,0,0] op_sel_hi:[1,0,0]\n"
    "v_mfma_f32_32x32x16_bf16 v[96:111], v[60:63], v[56:59], v[96:111]\n"
    "ds_read_b128 v[24:27], v5 offset:2176\n"
    "ds_read_b128 v[28:31], v5 offset:2192\n"
    "s_add_u32 m0, s54, 4096\n"
    "s_nop 0\n"
    "buffer_load_dwordx4 v2, s[4:7], s57 offen offset:0 sc1 lds\n"
    "s_waitcnt lgkmcnt(4)\n"
    "v_cvt_pk_bf16_f32 v124, v8, v9\n"
    "v_cvt_pk_bf16_f32 v125, v10, v11\n"
    "v_cvt_pk_bf16_f32 v126, v12, v13\n"
    "v_cvt_pk_bf16_f32 v127, v14, v15\n"
    "s_nop 1\n"
    "v_mfma_f32_32x32x16_bf16 v[64:79], v[112:115], v[124:127], v[64:79]\n"
    "ds_read_b128 v[48:51], v0 offset:18432\n"
    "ds_read_b128 v[52:55], v0 offset:19456\n"
    "ds_read_b128 v[56:59], v0 offset:20480\n"
    "s_waitcnt vmcnt(10) lgkmcnt(4)\n"
    "v_fma_mix_f32 v8, v152, v24, v16 op_sel:[0,0,0] op_sel_hi:[1,0,0]\n"
    "v_fma_mix_f32 v9, v152, v25, v17 op_sel:[1,0,0] op_sel_hi:[1,0,0]\n"
    "v_fma_mix_f32 v10, v153, v26, v18 op_sel:[0,0,0] op_sel_hi:[1,0,0]\n"
    "v_fma_mix_f32 v11, v153, v27, v19 op_sel:[1,0,0] op_sel_hi:[1,0,0]\n"
    "v_mfma_f32_32x32x16_bf16 v[80:95], v[116:119], v[124:127], v[80:95]\n"
    "ds_read_b128 v[32:35], v5 offset:448\n"
    "ds_read_b128 v[36:39], v5 offset:464\n"
    "buffer_load_dwordx4 v2, s[4:7], s57 offen offset:1024 sc1 lds\n"
    "s_waitcnt lgkmcnt(5)\n"
    "v_fma_mix_f32 v12, v154, v28, v20 op_sel:[0,0,0] op_sel_hi:[1,0,0]\n"
    "v_fma_mix_f32 v13, v154, v29, v21 op_sel:[1,0,0] op_sel_hi:[1,0,0]\n"
    "v_fma_mix_f32 v14, v155, v30, v22 op_sel:[0,0,0] op_sel_hi:[1,0,0]\n"
    "v_fma_mix_f32 v15, v155, v31, v23 op_sel:[1,0,0] op_sel_hi:[1,0,0]\n"
    "v_mfma_f32_32x32x16_bf16 v[96:111], v[124:127], v[120:123], v[96:111]\n"
    "ds_read_b128 v[40:43], v5 offset:2240\n"
    "ds_read_b128 v[44:47], v5 offset:2256\n"
    "buffer_load_dwordx4 v2, s[4:7], s57 offen offset:2048 sc1 lds\n"
    "s_waitcnt lgkmcnt(4)\n"
    "v_cvt_pk_bf16_f32 v60, v8, v9\n"
    "v_cvt_pk_bf16_f32 v61, v10, v11\n"
    "v_cvt_pk_bf16_f32 v62, v12, v13\n"
    "v_cvt_pk_bf16_f32 v63, v14, v15\n"
    "s_nop 1\n"
    "v_mfma_f32_32x32x16_bf16 v[64:79], v[48:51], v[60:63], v[64:79]\n"
    "ds_read_b128 v[112:115], v0 offset:21504\n"
    "ds_read_b128 v[116:119], v0 offset:22528\n"
    "ds_read_b128 v[120:123], v0 offset:23552\n"
    "s_waitcnt vmcnt(11) lgkmcnt(4)\n"
    "v_fma_mix_f32 v8, v156, v40, v32 op_sel:[0,0,0] op_sel_hi:[1,0,0]\n"
    "v_fma_mix_f32 v9, v156, v41, v33 op_sel:[1,0,0] op_sel_hi:[1,0,0]\n"
    "v_fma_mix_f32 v10, v157, v42, v34 op_sel:[0,0,0] op_sel_hi:[1,0,0]\n"
    "v_fma_mix_f32 v11, v157, v43, v35 op_sel:[1,0,0] op_sel_hi:[1,0,0]\n"
    "v_mfma_f32_32x32x16_bf16 v[80:95], v[52:55], v[60:63], v[80:95]\n"
    "ds_read_b128 v[16:19], v5 offset:512\n"
    "ds_read_b128 v[20:23], v5 offset:528\n"
    "buffer_load_dwordx4 v2, s[4:7], s57 offen offset:3072 sc1 lds\n"
    "s_waitcnt lgkmcnt(5)\n"
    "v_fma_mix_f32 v12, v158, v44, v36 op_sel:[0,0,0] op_sel_hi:[1,0,0]\n"
    "v_fma_mix_f32 v13, v158, v45, v37 op_sel:[1,0,0] op_sel_hi:[1,0,0]\n"
    "v_fma_mix_f32 v14, v159, v46, v38 op_sel:[0,0,0] op_sel_hi:[1,0,0]\n"
    "v_fma_mix_f32 v15, v159, v47, v39 op_sel:[1,0,0] op_sel_hi:[1,0,0]\n"
    "v_mfma_f32_32x32x16_bf16 v[96:111], v[60:63], v[56:59], v[96:111]\n"
    "ds_read_b128 v[24:27], v5 offset:2304\n"
    "ds_read_b128 v[28:31], v5 offset:2320\n"
    "s_add_u32 m0, s54, 8192\n"
    "s_nop 0\n"
    "buffer_load_dwordx4 v3, s[4:7], s57 offen offset:0 sc1 lds\n"
    "s_waitcnt lgkmcnt(4)\n"
    "v_cvt_pk_bf16_f32 v124, v8, v9\n"
    "v_cvt_pk_bf16_f32 v125, v10, v11\n"
    "v_cvt_pk_bf16_f32 v126, v12, v13\n"
    "v_cvt_pk_bf16_f32 v127, v14, v15\n"
    "s_nop 1\n"
    "v_mfma_f32_32x32x16_bf16 v[64:79], v[112:115], v[124:127], v[64:79]\n"
    "ds_read_b128 v[48:51], v0 offset:24576\n"
    "ds_read_b128 v[52:55], v0 offset:25600\n"
    "ds_read_b128 v[56:59], v0 offset:26624\n"
    "s_waitcnt vmcnt(12) lgkmcnt(4)\n"
    "v_fma_mix_f32 v8, v160, v24, v16 op_sel:[0,0,0] op_sel_hi:[1,0,0]\n"
    "v_fma_mix_f32 v9, v160, v25, v17 op_sel:[1,0,0] op_sel_hi:[1,0,0]\n"
    "v_fma_mix_f32 v10, v161, v26, v18 op_sel:[0,0,0] op_sel_hi:[1,0,0]\n"
    "v_fma_mix_f32 v11, v161, v27, v19 op_sel:[1,0,0] op_sel_hi:[1,0,0]\n"
    "v_mfma_f32_32x32x16_bf16 v[80:95], v[116:119], v[124:127], v[80:95]\n"
    "ds_read_b128 v[32:35], v5 offset:576\n"
    "ds_read_b128 v[36:39], v5 offset:592\n"
    "buffer_load_dwordx4 v3, s[4:7], s57 offen offset:1024 sc1 lds\n"
    "s_waitcnt lgkmcnt(5)\n"
    "v_fma_mix_f32 v12, v162, v28, v20 op_sel:[0,0,0] op_sel_hi:[1,0,0]\n"
    "v_fma_mix_f32 v13, v162, v29, v21 op_sel:[1,0,0] op_sel_hi:[1,0,0]\n"
    "v_fma_mix_f32 v14, v163, v30, v22 op_sel:[0,0,0] op_sel_hi:[1,0,0]\n"
    "v_fma_mix_f32 v15, v163, v31, v23 op_sel:[1,0,0] op_sel_hi:[1,0,0]\n"
    "v_mfma_f32_32x32x16_bf16 v[96:111], v[124:127], v[120:123], v[96:111]\n"
    "ds_read_b128 v[40:43], v5 offset:2368\n"
    "ds_read_b128 v[44:47], v5 offset:2384\n"
    "buffer_load_dwordx4 v3, s[4:7], s57 offen offset:2048 sc1 lds\n"
    "s_waitcnt lgkmcnt(4)\n"
    "v_cvt_pk_bf16_f32 v60, v8, v9\n"
    "v_cvt_pk_bf16_f32 v61, v10, v11\n"
    "v_cvt_pk_bf16_f32 v62, v12, v13\n"
    "v_cvt_pk_bf16_f32 v63, v14, v15\n"
    "s_nop 1\n"
    "v_mfma_f32_32x32x16_bf16 v[64:79], v[48:51], v[60:63], v[64:79]\n"
    "ds_read_b128 v[112:115], v0 offset:27648\n"
    "ds_read_b128 v[116:119], v0 offset:28672\n"
    "ds_read_b128 v[120:123], v0 offset:29696\n"
    "s_waitcnt vmcnt(13) lgkmcnt(4)\n"
    "v_fma_mix_f32 v8, v164, v40, v32 op_sel:[0,0,0] op_sel_hi:[1,0,0]\n"
    "v_fma_mix_f32 v9, v164, v41, v33 op_sel:[1,0,0] op_sel_hi:[1,0,0]\n"
    "v_fma_mix_f32 v10, v165, v42, v34 op_sel:[0,0,0] op_sel_hi:[1,0,0]\n"
    "v_fma_mix_f32 v11, v165, v43, v35 op_sel:[1,0,0] op_sel_hi:[1,0,0]\n"
    "v_mfma_f32_32x32x16_bf16 v[80:95], v[52:55], v[60:63], v[80:95]\n"
    "ds_read_b128 v[16:19], v5 offset:640\n"
    "ds_read_b128 v[20:23], v5 offset:656\n"
    "buffer_load_dwordx4 v3, s[4:7], s57 offen offset:3072 sc1 lds\n"
    "s_waitcnt lgkmcnt(5)\n"
    "v_fma_mix_f32 v12, v166, v44, v36 op_sel:[0,0,0] op_sel_hi:[1,0,0]\n"
    "v_fma_mix_f32 v13, v166, v45, v37 op_sel:[1,0,0] op_sel_hi:[1,0,0]\n"
    "v_fma_mix_f32 v14, v167, v46, v38 op_sel:[0,0,0] op_sel_hi:[1,0,0]\n"
    "v_fma_mix_f32 v15, v167, v47, v39 op_sel:[1,0,0] op_sel_hi:[1,0,0]\n"
    "v_mfma_f32_32x32x16_bf16 v[96:111], v[60:63], v[56:59], v[96:111]\n"
    "ds_read_b128 v[24:27], v5 offset:2432\n"
    "ds_read_b128 v[28:31], v5 offset:2448\n"
    "buffer_load_dwordx4 v[176:179], v4, s[4:7], s47 offen offset:0 sc1\n"
    "s_waitcnt lgkmcnt(4)\n"
    "v_cvt_pk_bf16_f32 v124, v8, v9\n"
    "v_cvt_pk_bf16_f32 v125, v10, v11\n"
    "v_cvt_pk_bf16_f32 v126, v12, v13\n"
    "v_cvt_pk_bf16_f32 v127, v14, v15\n"
    "s_nop 1\n"
    "v_mfma_f32_32x32x16_bf16 v[64:79], v[112:115], v[124:127], v[64:79]\n"
    "ds_read_b128 v[48:51], v0 offset:30720\n"
    "ds_read_b128 v[52:55], v0 offset:31744\n"
    "ds_read_b128 v[56:59], v0 offset:32768\n"
    "s_waitcnt vmcnt(14) lgkmcnt(4)\n"
    "v_fma_mix_f32 v8, v168, v24, v16 op_sel:[0,0,0] op_sel_hi:[1,0,0]\n"
    "v_fma_mix_f32 v9, v168, v25, v17 op_sel:[1,0,0] op_sel_hi:[1,0,0]\n"
    "v_fma_mix_f32 v10, v169, v26, v18 op_sel:[0,0,0] op_sel_hi:[1,0,0]\n"
    "v_fma_mix_f32 v11, v169, v27, v19 op_sel:[1,0,0] op_sel_hi:[1,0,0]\n"
    "v_mfma_f32_32x32x16_bf16 v[80:95], v[116:119], v[124:127], v[80:95]\n"
    "ds_read_b128 v[32:35], v5 offset:704\n"
    "ds_read_b128 v[36:39], v5 offset:720\n"
    "buffer_load_dwordx4 v[180:183], v4, s[4:7], s47 offen offset:1024 sc1\n"
    "s_waitcnt lgkmcnt(5)\n"
    "v_fma_mix_f32 v12, v170, v28, v20 op_sel:[0,0,0] op_sel_hi:[1,0,0]\n"
    "v_fma_mix_f32 v13, v170, v29, v21 op_sel:[1,0,0] op_sel_hi:[1,0,0]\n"
    "v_fma_mix_f32 v14, v171, v30, v22 op_sel:[0,0,0] op_sel_hi:[1,0,0]\n"
    "v_fma_mix_f32 v15, v171, v31, v23 op_sel:[1,0,0] op_sel_hi:[1,0,0]\n"
    "v_mfma_f32_32x32x16_bf16 v[96:111], v[124:127], v[120:123], v[96:111]\n"
    "ds_read_b128 v[40:43], v5 offset:2496\n"
    "ds_read_b128 v[44:47], v5 offset:2512\n"
    "buffer_load_dwordx4 v[184:187], v4, s[4:7], s47 offen offset:2048 sc1\n"
    "s_waitcnt lgkmcnt(4)\n"
    "v_cvt_pk_bf16_f32 v60, v8, v9\n"
    "v_cvt_pk_bf16_f32 v61, v10, v11\n"
    "v_cvt_pk_bf16_f32 v62, v12, v13\n"
    "v_cvt_pk_bf16_f32 v63, v14, v15\n"
    "s_nop 1\n"
    "v_mfma_f32_32x32x16_bf16 v[64:79], v[48:51], v[60:63], v[64:79]\n"
    "ds_read_b128 v[112:115], v0 offset:33792\n"
    "ds_read_b128 v[116:119], v0 offset:34816\n"
    "ds_read_b128 v[120:123], v0 offset:35840\n"
    "s_waitcnt vmcnt(15) lgkmcnt(4)\n"
    "v_fma_mix_f32 v8, v172, v40, v32 op_sel:[0,0,0] op_sel_hi:[1,0,0]\n"
    "v_fma_mix_f32 v9, v172, v41, v33 op_sel:[1,0,0] op_sel_hi:[1,0,0]\n"
    "v_fma_mix_f32 v10, v173, v42, v34 op_sel:[0,0,0] op_sel_hi:[1,0,0]\n"
    "v_fma_mix_f32 v11, v173, v43, v35 op_sel:[1,0,0] op_sel_hi:[1,0,0]\n"
    "v_mfma_f32_32x32x16_bf16 v[80:95], v[52:55], v[60:63], v[80:95]\n"
    "ds_read_b128 v[16:19], v5 offset:768\n"
    "ds_read_b128 v[20:23], v5 offset:784\n"
    "buffer_load_dwordx4 v[188:191], v4, s[4:7], s47 offen offset:3072 sc1\n"
    "s_waitcnt lgkmcnt(5)\n"
    "v_fma_mix_f32 v12, v174, v44, v36 op_sel:[0,0,0] op_sel_hi:[1,0,0]\n"
    "v_fma_mix_f32 v13, v174, v45, v37 op_sel:[1,0,0] op_sel_hi:[1,0,0]\n"
    "v_fma_mix_f32 v14, v175, v46, v38 op_sel:[0,0,0] op_sel_hi:[1,0,0]\n"
    "v_fma_mix_f32 v15, v175, v47, v39 op_sel:[1,0,0] op_sel_hi:[1,0,0]\n"
    "v_mfma_f32_32x32x16_bf16 v[96:111], v[60:63], v[56:59], v[96:111]\n"
    "ds_read_b128 v[24:27], v5 offset:2560\n"
    "ds_read_b128 v[28:31], v5 offset:2576\n"
    "buffer_load_dwordx4 v[192:195], v0, s[4:7], s58 offen offset:0 sc1\n"
    "s_waitcnt lgkmcnt(4)\n"
    "v_cvt_pk_bf16_f32 v124, v8, v9\n"
    "v_cvt_pk_bf16_f32 v125, v10, v11\n"
    "v_cvt_pk_bf16_f32 v126, v12, v13\n"
    "v_cvt_pk_bf16_f32 v127, v14, v15\n"
    "s_waitcnt vmcnt(5)\n"
    "s_waitcnt lgkmcnt(0)\n"
    "s_barrier\n"
    "v_mfma_f32_32x32x16_bf16 v[64:79], v[112:115], v[124:127], v[64:79]\n"
    "ds_read_b128 v[48:51], v0 offset:36864\n"
    "ds_read_b128 v[52:55], v0 offset:37888\n"
    "ds_read_b128 v[56:59], v0 offset:38912\n"
    "s_waitcnt vmcnt(4)\n"
    "v_fma_mix_f32 v8, v176, v24, v16 op_sel:[0,0,0] op_sel_hi:[1,0,0]\n"
    "v_fma_mix_f32 v9, v176, v25, v17 op_sel:[1,0,0] op_sel_hi:[1,0,0]\n"
    "v_fma_mix_f32 v10, v177, v26, v18 op_sel:[0,0,0] op_sel_hi:[1,0,0]\n"
    "v_fma_mix_f32 v11, v177, v27, v19 op_sel:[1,0,0] op_sel_hi:[1,0,0]\n"
    "v_mfma_f32_32x32x16_bf16 v[80:95], v[116:119], v[124:127], v[80:95]\n"
    "ds_read_b128 v[32:35], v5 offset:832\n"
    "ds_read_b128 v[36:39], v5 offset:848\n"
    "buffer_load_dwordx4 v[196:199], v0, s[4:7], s58 offen offset:1024 sc1\n"
    "v_fma_mix_f32 v12, v178, v28, v20 op_sel:[0,0,0] op_sel_hi:[1,0,0]\n"
    "v_fma_mix_f32 v13, v178, v29, v21 op_sel:[1,0,0] op_sel_hi:[1,0,0]\n"
    "v_fma_mix_f32 v14, v179, v30, v22 op_sel:[0,0,0] op_sel_hi:[1,0,0]\n"
    "v_fma_mix_f32 v15, v179, v31, v23 op_sel:[1,0,0] op_sel_hi:[1,0,0]\n"
    "v_mfma_f32_32x32x16_bf16 v[96:111], v[124:127], v[120:123], v[96:111]\n"
    "ds_read_b128 v[40:43], v5 offset:2624\n"
    "ds_read_b128 v[44:47], v5 offset:2640\n"
    "buffer_load_dwordx4 v[200:203], v0, s[4:7], s58 offen offset:2048 sc1\n"
    "s_waitcnt lgkmcnt(4)\n"
    "v_cvt_pk_bf16_f32 v60, v8, v9\n"
    "v_cvt_pk_bf16_f32 v61, v10, v11\n"
    "v_cvt_pk_bf16_f32 v62, v12, v13\n"
    "v_cvt_pk_bf16_f32 v63, v14, v15\n"
    "s_nop 1\n"
    "v_mfma_f32_32x32x16_bf16 v[64:79], v[48:51], v[60:63], v[64:79]\n"
    "ds_read_b128 v[112:115], v0 offset:39936\n"
    "ds_read_b128 v[116:119], v0 offset:40960\n"
    "ds_read_b128 v[120:123], v0 offset:41984\n"
    "s_waitcnt vmcnt(5) lgkmcnt(4)\n"
    "v_fma_mix_f32 v8, v180, v40, v32 op_sel:[0,0,0] op_sel_hi:[1,0,0]\n"
    "v_fma_mix_f32 v9, v180, v41, v33 op_sel:[1,0,0] op_sel_hi:[1,0,0]\n"
    "v_fma_mix_f32 v10, v181, v42, v34 op_sel:[0,0,0] op_sel_hi:[1,0,0]\n"
    "v_fma_mix_f32 v11, v181, v43, v35 op_sel:[1,0,0] op_sel_hi:[1,0,0]\n"
    "v_mfma_f32_32x32x16_bf16 v[80:95], v[52:55], v[60:63], v[80:95]\n"
    "ds_read_b128 v[16:19], v5 offset:896\n"
    "ds_read_b128 v[20:23], v5 offset:912\n"
    "buffer_load_dwordx4 v[204:207], v0, s[4:7], s58 offen offset:3072 sc1\n"
    "s_waitcnt lgkmcnt(5)\n"
    "v_fma_mix_f32 v12, v182, v44, v36 op_sel:[0,0,0] op_sel_hi:[1,0,0]\n"
    "v_fma_mix_f32 v13, v182, v45, v37 op_sel:[1,0,0] op_sel_hi:[1,0,0]\n"
    "v_fma_mix_f32 v14, v183, v46, v38 op_sel:[0,0,0] op_sel_hi:[1,0,0]\n"
    "v_fma_mix_f32 v15, v183, v47, v39 op_sel:[1,0,0] op_sel_hi:[1,0,0]\n"
    "v_mfma_f32_32x32x16_bf16 v[96:111], v[60:63], v[56:59], v[96:111]\n"
    "ds_read_b128 v[24:27], v5 offset:2688\n"
    "ds_read_b128 v[28:31], v5 offset:2704\n"
    "buffer_load_dwordx2 v[242:243], v240, s[4:7], 0 offen sc1\n"
    "s_waitcnt lgkmcnt(4)\n"
    "v_cvt_pk_bf16_f32 v124, v8, v9\n"
    "v_cvt_pk_bf16_f32 v125, v10, v11\n"
    "v_cvt_pk_bf16_f32 v126, v12, v13\n"
    "v_cvt_pk_bf16_f32 v127, v14, v15\n"
    "s_nop 1\n"
    "v_mfma_f32_32x32x16_bf16 v[64:79], v[112:115], v[124:127], v[64:79]\n"
    "ds_read_b128 v[48:51], v0 offset:43008\n"
    "ds_read_b128 v[52:55], v0 offset:44032\n"
    "ds_read_b128 v[56:59], v0 offset:45056\n"
    "s_waitcnt vmcnt(6) lgkmcnt(4)\n"
    "v_fma_mix_f32 v8, v184, v24, v16 op_sel:[0,0,0] op_sel_hi:[1,0,0]\n"
    "v_fma_mix_f32 v9, v184, v25, v17 op_sel:[1,0,0] op_sel_hi:[1,0,0]\n"
    "v_fma_mix_f32 v10, v185, v26, v18 op_sel:[0,0,0] op_sel_hi:[1,0,0]\n"
    "v_fma_mix_f32 v11, v185, v27, v19 op_sel:[1,0,0] op_sel_hi:[1,0,0]\n"
    "v_mfma_f32_32x32x16_bf16 v[80:95], v[116:119], v[124:127], v[80:95]\n"
    "ds_read_b128 v[32:35], v5 offset:960\n"
    "ds_read_b128 v[36:39], v5 offset:976\n"
    "buffer_load_dwordx4 v[208:211], v2, s[4:7], s58 offen offset:0 sc1\n"
    "s_waitcnt lgkmcnt(5)\n"
    "v_fma_mix_f32 v12, v186, v28, v20 op_sel:[0,0,0] op_sel_hi:[1,0,0]\n"
    "v_fma_mix_f32 v13, v186, v29, v21 op_sel:[1,0,0] op_sel_hi:[1,0,0]\n"
    "v_fma_mix_f32 v14, v187, v30, v22 op_sel:[0,0,0] op_sel_hi:[1,0,0]\n"
    "v_fma_mix_f32 v15, v187, v31, v23 op_sel:[1,0,0] op_sel_hi:[1,0,0]\n"
    "v_mfma_f32_32x32x16_bf16 v[96:111], v[124:127], v[120:123], v[96:111]\n"
    "ds_read_b128 v[40:43], v5 offset:2752\n"
    "ds_read_b128 v[44:47], v5 offset:2768\n"
    "buffer_load_dwordx4 v[212:215], v2, s[4:7], s58 offen offset:1024 sc1\n"
    "s_waitcnt lgkmcnt(4)\n"
    "v_cvt_pk_bf16_f32 v60, v8, v9\n"
    "v_cvt_pk_bf16_f32 v61, v10, v11\n"
    "v_cvt_pk_bf16_f32 v62, v12, v13\n"
    "v_cvt_pk_bf16_f32 v63, v14, v15\n"
    "s_nop 1\n"
    "v_mfma_f32_32x32x16_bf16 v[64:79], v[48:51], v[60:63], v[64:79]\n"
    "ds_read_b128 v[112:115], v0 offset:46080\n"
    "ds_read_b128 v[116:119], v0 offset:47104\n"
    "ds_read_b128 v[120:123], v0 offset:48128\n"
    "s_waitcnt vmcnt(7) lgkmcnt(4)\n"
    "v_fma_mix_f32 v8, v188, v40, v32 op_sel:[0,0,0] op_sel_hi:[1,0,0]\n"
    "v_fma_mix_f32 v9, v188, v41, v33 op_sel:[1,0,0] op_sel_hi:[1,0,0]\n"
    "v_fma_mix_f32 v10, v189, v42, v34 op_sel:[0,0,0] op_sel_hi:[1,0,0]\n"
    "v_fma_mix_f32 v11, v189, v43, v35 op_sel:[1,0,0] op_sel_hi:[1,0,0]\n"
    "v_mfma_f32_32x32x16_bf16 v[80:95], v[52:55], v[60:63], v[80:95]\n"
    "ds_read_b128 v[16:19], v5 offset:1024\n"
    "ds_read_b128 v[20:23], v5 offset:1040\n"
    "buffer_load_dwordx4 v[216:219], v2, s[4:7], s58 offen offset:2048 sc1\n"
    "s_waitcnt lgkmcnt(5)\n"
    "v_fma_mix_f32 v12, v190, v44, v36 op_sel:[0,0,0] op_sel_hi:[1,0,0]\n"
    "v_fma_mix_f32 v13, v190, v45, v37 op_sel:[1,0,0] op_sel_hi:[1,0,0]\n"
    "v_fma_mix_f32 v14, v191, v46, v38 op_sel:[0,0,0] op_sel_hi:[1,0,0]\n"
    "v_fma_mix_f32 v15, v191, v47, v39 op_sel:[1,0,0] op_sel_hi:[1,0,0]\n"
    "v_mfma_f32_32x32x16_bf16 v[96:111], v[60:63], v[56:59], v[96:111]\n"
    "ds_read_b128 v[24:27], v5 offset:2816\n"
    "ds_read_b128 v[28:31], v5 offset:2832\n"
    "buffer_load_dwordx4 v[220:223], v2, s[4:7], s58 offen offset:3072 sc1\n"
    "s_waitcnt lgkmcnt(4)\n"
    "v_cvt_pk_bf16_f32 v124, v8, v9\n"
    "v_cvt_pk_bf16_f32 v125, v10, v11\n"
    "v_cvt_pk_bf16_f32 v126, v12, v13\n"
    "v_cvt_pk_bf16_f32 v127, v14, v15\n"
    "s_nop 1\n"
    "v_mfma_f32_32x32x16_bf16 v[64:79], v[112:115], v[124:127], v[64:79]\n"
    "ds_read_b128 v[48:51], v0 offset:49152\n"
    "ds_read_b128 v[52:55], v0 offset:50176\n"
    "ds_read_b128 v[56:59], v0 offset:51200\n"
    "s_waitcnt vmcnt(8) lgkmcnt(4)\n"
    "v_fma_mix_f32 v8, v192, v24, v16 op_sel:[0,0,0] op_sel_hi:[1,0,0]\n"
    "v_fma_mix_f32 v9, v192, v25, v17 op_sel:[1,0,0] op_sel_hi:[1,0,0]\n"
    "v_fma_mix_f32 v10, v193, v26, v18 op_sel:[0,0,0] op_sel_hi:[1,0,0]\n"
    "v_fma_mix_f32 v11, v193, v27, v19 op_sel:[1,0,0] op_sel_hi:[1,0,0]\n"
    "v_mfma_f32_32x32x16_bf16 v[80:95], v[116:119], v[124:127], v[80:95]\n"
    "ds_read_b128 v[32:35], v5 offset:1088\n"
    "ds_read_b128 v[36:39], v5 offset:1104\n"
    "buffer_load_dwordx4 v[224:227], v3, s[4:7], s58 offen offset:0 sc1\n"
    "s_waitcnt lgkmcnt(5)\n"
    "v_fma_mix_f32 v12, v194, v28, v20 op_sel:[0,0,0] op_sel_hi:[1,0,0]\n"
    "v_fma_mix_f32 v13, v194, v29, v21 op_sel:[1,0,0] op_sel_hi:[1,0,0]\n"
    "v_fma_mix_f32 v14, v195, v30, v22 op_sel:[0,0,0] op_sel_hi:[1,0,0]\n"
    "v_fma_mix_f32 v15, v195, v31, v23 op_sel:[1,0,0] op_sel_hi:[1,0,0]\n"
    "v_mfma_f32_32x32x16_bf16 v[96:111], v[124:127], v[120:123], v[96:111]\n"
    "ds_read_b128 v[40:43], v5 offset:2880\n"
    "ds_read_b128 v[44:47], v5 offset:2896\n"
    "buffer_load_dwordx4 v[228:231], v3, s[4:7], s58 offen offset:1024 sc1\n"
    "s_waitcnt lgkmcnt(4)\n"
    "v_cvt_pk_bf16_f32 v60, v8, v9\n"
    "v_cvt_pk_bf16_f32 v61, v10, v11\n"
    "v_cvt_pk_bf16_f32 v62, v12, v13\n"
    "v_cvt_pk_bf16_f32 v63, v14, v15\n"
    "s_nop 1\n"
    "v_mfma_f32_32x32x16_bf16 v[64:79], v[48:51], v[60:63], v[64:79]\n"
    "ds_read_b128 v[112:115], v0 offset:52224\n"
    "ds_read_b128 v[116:119], v0 offset:53248\n"
    "ds_read_b128 v[120:123], v0 offset:54272\n"
    "s_waitcnt vmcnt(9) lgkmcnt(4)\n"
    "v_fma_mix_f32 v8, v196, v40, v32 op_sel:[0,0,0] op_sel_hi:[1,0,0]\n"
    "v_fma_mix_f32 v9, v196, v41, v33 op_sel:[1,0,0] op_sel_hi:[1,0,0]\n"
    "v_fma_mix_f32 v10, v197, v42, v34 op_sel:[0,0,0] op_sel_hi:[1,0,0]\n"
    "v_fma_mix_f32 v11, v197, v43, v35 op_sel:[1,0,0] op_sel_hi:[1,0,0]\n"
    "v_mfma_f32_32x32x16_bf16 v[80:95], v[52:55], v[60:63], v[80:95]\n"
    "ds_read_b128 v[16:19], v5 offset:1152\n"
    "ds_read_b128 v[20:23], v5 offset:1168\n"
    "buffer_load_dwordx4 v[232:235], v3, s[4:7], s58 offen offset:2048 sc1\n"
    "s_waitcnt lgkmcnt(5)\n"
    "v_fma_mix_f32 v12, v198, v44, v36 op_sel:[0,0,0] op_sel_hi:[1,0,0]\n"
    "v_fma_mix_f32 v13, v198, v45, v37 op_sel:[1,0,0] op_sel_hi:[1,0,0]\n"
    "v_fma_mix_f32 v14, v199, v46, v38 op_sel:[0,0,0] op_sel_hi:[1,0,0]\n"
    "v_fma_mix_f32 v15, v199, v47, v39 op_sel:[1,0,0] op_sel_hi:[1,0,0]\n"
    "v_mfma_f32_32x32x16_bf16 v[96:111], v[60:63], v[56:59], v[96:111]\n"
    "ds_read_b128 v[24:27], v5 offset:2944\n"
    "ds_read_b128 v[28:31], v5 offset:2960\n"
    "buffer_load_dwordx4 v[236:239], v3, s[4:7], s58 offen offset:3072 sc1\n"
    "s_waitcnt lgkmcnt(4)\n"
    "v_cvt_pk_bf16_f32 v124, v8, v9\n"
    "v_cvt_pk_bf16_f32 v125, v10, v11\n"
    "v_cvt_pk_bf16_f32 v126, v12, v13\n"
    "v_cvt_pk_bf16_f32 v127, v14, v15\n"
    "s_nop 1\n"
    "v_mfma_f32_32x32x16_bf16 v[64:79], v[112:115], v[124:127], v[64:79]\n"
    "ds_read_b128 v[48:51], v0 offset:55296\n"
    "ds_read_b128 v[52:55], v0 offset:56320\n"
    "ds_read_b128 v[56:59], v0 offset:57344\n"
    "s_waitcnt vmcnt(10) lgkmcnt(4)\n"
    "v_fma_mix_f32 v8, v200, v24, v16 op_sel:[0,0,0] op_sel_hi:[1,0,0]\n"
    "v_fma_mix_f32 v9, v200, v25, v17 op_sel:[1,0,0] op_sel_hi:[1,0,0]\n"
    "v_fma_mix_f32 v10, v201, v26, v18 op_sel:[0,0,0] op_sel_hi:[1,0,0]\n"
    "v_fma_mix_f32 v11, v201, v27, v19 op_sel:[1,0,0] op_sel_hi:[1,0,0]\n"
    "v_mfma_f32_32x32x16_bf16 v[80:95], v[116:119], v[124:127], v[80:95]\n"
    "ds_read_b128 v[32:35], v5 offset:1216\n"
    "ds_read_b128 v[36:39], v5 offset:1232\n"
    "s_waitcnt lgkmcnt(5)\n"
    "v_fma_mix_f32 v12, v202, v28, v20 op_sel:[0,0,0] op_sel_hi:[1,0,0]\n"
    "v_fma_mix_f32 v13, v202, v29, v21 op_sel:[1,0,0] op_sel_hi:[1,0,0]\n"
    "v_fma_mix_f32 v14, v203, v30, v22 op_sel:[0,0,0] op_sel_hi:[1,0,0]\n"
    "v_fma_mix_f32 v15, v203, v31, v23 op_sel:[1,0,0] op_sel_hi:[1,0,0]\n"
    "v_mfma_f32_32x32x16_bf16 v[96:111], v[124:127], v[120:123], v[96:111]\n"
    "ds_read_b128 v[40:43], v5 offset:3008\n"
    "ds_read_b128 v[44:47], v5 offset:3024\n"
    "s_waitcnt lgkmcnt(4)\n"
    "v_cvt_pk_bf16_f32 v60, v8, v9\n"
    "v_cvt_pk_bf16_f32 v61, v10, v11\n"
    "v_cvt_pk_bf16_f32 v62, v12, v13\n"
    "v_cvt_pk_bf16_f32 v63, v14, v15\n"
    "s_nop 1\n"
    "v_mfma_f32_32x32x16_bf16 v[64:79], v[48:51], v[60:63], v[64:79]\n"
    "ds_read_b128 v[112:115], v0 offset:58368\n"
    "ds_read_b128 v[116:119], v0 offset:59392\n"
    "ds_read_b128 v[120:123], v0 offset:60416\n"
    "s_waitcnt vmcnt(9) lgkmcnt(4)\n"
    "v_fma_mix_f32 v8, v204, v40, v32 op_sel:[0,0,0] op_sel_hi:[1,0,0]\n"
    "v_fma_mix_f32 v9, v204, v41, v33 op_sel:[1,0,0] op_sel_hi:[1,0,0]\n"
    "v_fma_mix_f32 v10, v205, v42, v34 op_sel:[0,0,0] op_sel_hi:[1,0,0]\n"
    "v_fma_mix_f32 v11, v205, v43, v35 op_sel:[1,0,0] op_sel_hi:[1,0,0]\n"
    "v_mfma_f32_32x32x16_bf16 v[80:95], v[52:55], v[60:63], v[80:95]\n"
    "ds_read_b128 v[16:19], v5 offset:1280\n"
    "ds_read_b128 v[20:23], v5 offset:1296\n"
    "s_waitcnt lgkmcnt(5)\n"
    "v_fma_mix_f32 v12, v206, v44, v36 op_sel:[0,0,0] op_sel_hi:[1,0,0]\n"
    "v_fma_mix_f32 v13, v206, v45, v37 op_sel:[1,0,0] op_sel_hi:[1,0,0]\n"
    "v_fma_mix_f32 v14, v207, v46, v38 op_sel:[0,0,0] op_sel_hi:[1,0,0]\n"
    "v_fma_mix_f32 v15, v207, v47, v39 op_sel:[1,0,0] op_sel_hi:[1,0,0]\n"
    "v_mfma_f32_32x32x16_bf16 v[96:111], v[60:63], v[56:59], v[96:111]\n"
    "ds_read_b128 v[24:27], v5 offset:3072\n"
    "ds_read_b128 v[28:31], v5 offset:3088\n"
    "s_waitcnt lgkmcnt(4)\n"
    "v_cvt_pk_bf16_f32 v124, v8, v9\n"
    "v_cvt_pk_bf16_f32 v125, v10, v11\n"
    "v_cvt_pk_bf16_f32 v126, v12, v13\n"
    "v_cvt_pk_bf16_f32 v127, v14, v15\n"
    "s_nop 1\n"
    "v_mfma_f32_32x32x16_bf16 v[64:79], v[112:115], v[124:127], v[64:79]\n"
    "ds_read_b128 v[48:51], v0 offset:61440\n"
    "ds_read_b128 v[52:55], v0 offset:62464\n"
    "ds_read_b128 v[56:59], v0 offset:63488\n"
    "s_waitcnt vmcnt(7) lgkmcnt(4)\n"
    "v_fma_mix_f32 v8, v208, v24, v16 op_sel:[0,0,0] op_sel_hi:[1,0,0]\n"
    "v_fma_mix_f32 v9, v208, v25, v17 op_sel:[1,0,0] op_sel_hi:[1,0,0]\n"
    "v_fma_mix_f32 v10, v209, v26, v18 op_sel:[0,0,0] op_sel_hi:[1,0,0]\n"
    "v_fma_mix_f32 v11, v209, v27, v19 op_sel:[1,0,0] op_sel_hi:[1,0,0]\n"
    "v_mfma_f32_32x32x16_bf16 v[80:95], v[116:119], v[124:127], v[80:95]\n"
    "ds_read_b128 v[32:35], v5 offset:1344\n"
    "ds_read_b128 v[36:39], v5 offset:1360\n"
    "s_waitcnt lgkmcnt(5)\n"
    "v_fma_mix_f32 v12, v210, v28, v20 op_sel:[0,0,0] op_sel_hi:[1,0,0]\n"
    "v_fma_mix_f32 v13, v210, v29, v21 op_sel:[1,0,0] op_sel_hi:[1,0,0]\n"
    "v_fma_mix_f32 v14, v211, v30, v22 op_sel:[0,0,0] op_sel_hi:[1,0,0]\n"
    "v_fma_mix_f32 v15, v211, v31, v23 op_sel:[1,0,0] op_sel_hi:[1,0,0]\n"
    "v_mfma_f32_32x32x16_bf16 v[96:111], v[124:127], v[120:123], v[96:111]\n"
    "ds_read_b128 v[40:43], v5 offset:3136\n"
    "ds_read_b128 v[44:47], v5 offset:3152\n"
    "s_waitcnt lgkmcnt(4)\n"
    "v_cvt_pk_bf16_f32 v60, v8, v9\n"
    "v_cvt_pk_bf16_f32 v61, v10, v11\n"
    "v_cvt_pk_bf16_f32 v62, v12, v13\n"
    "v_cvt_pk_bf16_f32 v63, v14, v15\n"
    "s_nop 1\n"
    "v_mfma_f32_32x32x16_bf16 v[64:79], v[48:51], v[60:63], v[64:79]\n"
    "ds_read_b128 v[112:115], v0 offset:64512\n"
    "ds_read_b128 v[116:119], v6 offset:0\n"
    "ds_read_b128 v[120:123], v6 offset:1024\n"
    "s_waitcnt vmcnt(6) lgkmcnt(4)\n"
    "v_fma_mix_f32 v8, v212, v40, v32 op_sel:[0,0,0] op_sel_hi:[1,0,0]\n"
    "v_fma_mix_f32 v9, v212, v41, v33 op_sel:[1,0,0] op_sel_hi:[1,0,0]\n"
    "v_fma_mix_f32 v10, v213, v42, v34 op_sel:[0,0,0] op_sel_hi:[1,0,0]\n"
    "v_fma_mix_f32 v11, v213, v43, v35 op_sel:[1,0,0] op_sel_hi:[1,0,0]\n"
    "v_mfma_f32_32x32x16_bf16 v[80:95], v[52:55], v[60:63], v[80:95]\n"
    "ds_read_b128 v[16:19], v5 offset:1408\n"
    "ds_read_b128 v[20:23], v5 offset:1424\n"
    "s_waitcnt lgkmcnt(5)\n"
    "v_fma_mix_f32 v12, v214, v44, v36 op_sel:[0,0,0] op_sel_hi:[1,0,0]\n"
    "v_fma_mix_f32 v13, v214, v45, v37 op_sel:[1,0,0] op_sel_hi:[1,0,0]\n"
    "v_fma_mix_f32 v14, v215, v46, v38 op_sel:[0,0,0] op_sel_hi:[1,0,0]\n"
    "v_fma_mix_f32 v15, v215, v47, v39 op_sel:[1,0,0] op_sel_hi:[1,0,0]\n"
    "v_mfma_f32_32x32x16_bf16 v[96:111], v[60:63], v[56:59], v[96:111]\n"
    "ds_read_b128 v[24:27], v5 offset:3200\n"
    "ds_read_b128 v[28:31], v5 offset:3216\n"
    "s_waitcnt lgkmcnt(4)\n"
    "v_cvt_pk_bf16_f32 v124, v8, v9\n"
    "v_cvt_pk_bf16_f32 v125, v10, v11\n"
    "v_cvt_pk_bf16_f32 v126, v12, v13\n"
    "v_cvt_pk_bf16_f32 v127, v14, v15\n"
    "s_nop 1\n"
    "v_mfma_f32_32x32x16_bf16 v[64:79], v[112:115], v[124:127], v[64:79]\n"
    "ds_read_b128 v[48:51], v6 offset:2048\n"
    "ds_read_b128 v[52:55], v6 offset:3072\n"
    "ds_read_b128 v[56:59], v6 offset:4096\n"
    "s_waitcnt vmcnt(5) lgkmcnt(4)\n"
    "v_fma_mix_f32 v8, v216, v24, v16 op_sel:[0,0,0] op_sel_hi:[1,0,0]\n"
    "v_fma_mix_f32 v9, v216, v25, v17 op_sel:[1,0,0] op_sel_hi:[1,0,0]\n"
    "v_fma_mix_f32 v10, v217, v26, v18 op_sel:[0,0,0] op_sel_hi:[1,0,0]\n"
    "v_fma_mix_f32 v11, v217, v27, v19 op_sel:[1,0,0] op_sel_hi:[1,0,0]\n"
    "v_mfma_f32_32x32x16_bf16 v[80:95], v[116:119], v[124:127], v[80:95]\n"
    "ds_read_b128 v[32:35], v5 offset:1472\n"
    "ds_read_b128 v[36:39], v5 offset:1488\n"
    "s_waitcnt lgkmcnt(5)\n"
    "v_fma_mix_f32 v12, v218, v28, v20 op_sel:[0,0,0] op_sel_hi:[1,0,0]\n"
    "v_fma_mix_f32 v13, v218, v29, v21 op_sel:[1,0,0] op_sel_hi:[1,0,0]\n"
    "v_fma_mix_f32 v14, v219, v30, v22 op_sel:[0,0,0] op_sel_hi:[1,0,0]\n"
    "v_fma_mix_f32 v15, v219, v31, v23 op_sel:[1,0,0] op_sel_hi:[1,0,0]\n"
    "v_mfma_f32_32x32x16_bf16 v[96:111], v[124:127], v[120:123], v[96:111]\n"
    "ds_read_b128 v[40:43], v5 offset:3264\n"
    "ds_read_b128 v[44:47], v5 offset:3280\n"
    "s_waitcnt lgkmcnt(4)\n"
    "v_cvt_pk_bf16_f32 v60, v8, v9\n"
    "v_cvt_pk_bf16_f32 v61, v10, v11\n"
    "v_cvt_pk_bf16_f32 v62, v12, v13\n"
    "v_cvt_pk_bf16_f32 v63, v14, v15\n"
    "s_nop 1\n"
    "v_mfma_f32_32x32x16_bf16 v[64:79], v[48:51], v[60:63], v[64:79]\n"
    "ds_read_b128 v[112:115], v6 offset:5120\n"
    "ds_read_b128 v[116:119], v6 offset:6144\n"
    "ds_read_b128 v[120:123], v6 offset:7168\n"
    "s_waitcnt vmcnt(4) lgkmcnt(4)\n"
    "v_fma_mix_f32 v8, v220, v40, v32 op_sel:[0,0,0] op_sel_hi:[1,0,0]\n"
    "v_fma_mix_f32 v9, v220, v41, v33 op_sel:[1,0,0] op_sel_hi:[1,0,0]\n"
    "v_fma_mix_f32 v10, v221, v42, v34 op_sel:[0,0,0] op_sel_hi:[1,0,0]\n"
    "v_fma_mix_f32 v11, v221, v43, v35 op_sel:[1,0,0] op_sel_hi:[1,0,0]\n"
    "v_mfma_f32_32x32x16_bf16 v[80:95], v[52:55], v[60:63], v[80:95]\n"
    "ds_read_b128 v[16:19], v5 offset:1536\n"
    "ds_read_b128 v[20:23], v5 offset:1552\n"
    "s_waitcnt lgkmcnt(5)\n"
    "v_fma_mix_f32 v12, v222, v44, v36 op_sel:[0,0,0] op_sel_hi:[1,0,0]\n"
    "v_fma_mix_f32 v13, v222, v45, v37 op_sel:[1,0,0] op_sel_hi:[1,0,0]\n"
    "v_fma_mix_f32 v14, v223, v46, v38 op_sel:[0,0,0] op_sel_hi:[1,0,0]\n"
    "v_fma_mix_f32 v15, v223, v47, v39 op_sel:[1,0,0] op_sel_hi:[1,0,0]\n"
    "v_mfma_f32_32x32x16_bf16 v[96:111], v[60:63], v[56:59], v[96:111]\n"
    "ds_read_b128 v[24:27], v5 offset:3328\n"
    "ds_read_b128 v[28:31], v5 offset:3344\n"
    "s_waitcnt lgkmcnt(4)\n"
    "v_cvt_pk_bf16_f32 v124, v8, v9\n"
    "v_cvt_pk_bf16_f32 v125, v10, v11\n"
    "v_cvt_pk_bf16_f32 v126, v12, v13\n"
    "v_cvt_pk_bf16_f32 v127, v14, v15\n"
    "s_nop 1\n"
    "v_mfma_f32_32x32x16_bf16 v[64:79], v[112:115], v[124:127], v[64:79]\n"
    "ds_read_b128 v[48:51], v6 offset:8192\n"
    "ds_read_b128 v[52:55], v6 offset:9216\n"
    "ds_read_b128 v[56:59], v6 offset:10240\n"
    "s_waitcnt vmcnt(3) lgkmcnt(4)\n"
    "v_fma_mix_f32 v8, v224, v24, v16 op_sel:[0,0,0] op_sel_hi:[1,0,0]\n"
    "v_fma_mix_f32 v9, v224, v25, v17 op_sel:[1,0,0] op_sel_hi:[1,0,0]\n"
    "v_fma_mix_f32 v10, v225, v26, v18 op_sel:[0,0,0] op_sel_hi:[1,0,0]\n"
    "v_fma_mix_f32 v11, v225, v27, v19 op_sel:[1,0,0] op_sel_hi:[1,0,0]\n"
    "v_mfma_f32_32x32x16_bf16 v[80:95], v[116:119], v[124:127], v[80:95]\n"
    "ds_read_b128 v[32:35], v5 offset:1600\n"
    "ds_read_b128 v[36:39], v5 offset:1616\n"
    "s_waitcnt lgkmcnt(5)\n"
    "v_fma_mix_f32 v12, v226, v28, v20 op_sel:[0,0,0] op_sel_hi:[1,0,0]\n"
    "v_fma_mix_f32 v13, v226, v29, v21 op_sel:[1,0,0] op_sel_hi:[1,0,0]\n"
    "v_fma_mix_f32 v14, v227, v30, v22 op_sel:[0,0,0] op_sel_hi:[1,0,0]\n"
    "v_fma_mix_f32 v15, v227, v31, v23 op_sel:[1,0,0] op_sel_hi:[1,0,0]\n"
    "v_mfma_f32_32x32x16_bf16 v[96:111], v[124:127], v[120:123], v[96:111]\n"
    "ds_read_b128 v[40:43], v5 offset:3392\n"
    "ds_read_b128 v[44:47], v5 offset:3408\n"
    "s_waitcnt lgkmcnt(4)\n"
    "v_cvt_pk_bf16_f32 v60, v8, v9\n"
    "v_cvt_pk_bf16_f32 v61, v10, v11\n"
    "v_cvt_pk_bf16_f32 v62, v12, v13\n"
    "v_cvt_pk_bf16_f32 v63, v14, v15\n"
    "s_nop 1\n"
    "v_mfma_f32_32x32x16_bf16 v[64:79], v[48:51], v[60:63], v[64:79]\n"
    "ds_read_b128 v[112:115], v6 offset:11264\n"
    "ds_read_b128 v[116:119], v6 offset:12288\n"
    "ds_read_b128 v[120:123], v6 offset:13312\n"
    "s_waitcnt vmcnt(2) lgkmcnt(4)\n"
    "v_fma_mix_f32 v8, v228, v40, v32 op_sel:[0,0,0] op_sel_hi:[1,0,0]\n"
    "v_fma_mix_f32 v9, v228, v41, v33 op_sel:[1,0,0] op_sel_hi:[1,0,0]\n"
    "v_fma_mix_f32 v10, v229, v42, v34 op_sel:[0,0,0] op_sel_hi:[1,0,0]\n"
    "v_fma_mix_f32 v11, v229, v43, v35 op_sel:[1,0,0] op_sel_hi:[1,0,0]\n"
    "v_mfma_f32_32x32x16_bf16 v[80:95], v[52:55], v[60:63], v[80:95]\n"
    "ds_read_b128 v[16:19], v5 offset:1664\n"
    "ds_read_b128 v[20:23], v5 offset:1680\n"
    "s_waitcnt lgkmcnt(5)\n"
    "v_fma_mix_f32 v12, v230, v44, v36 op_sel:[0,0,0] op_sel_hi:[1,0,0]\n"
    "v_fma_mix_f32 v13, v230, v45, v37 op_sel:[1,0,0] op_sel_hi:[1,0,0]\n"
    "v_fma_mix_f32 v14, v231, v46, v38 op_sel:[0,0,0] op_sel_hi:[1,0,0]\n"
    "v_fma_mix_f32 v15, v231, v47, v39 op_sel:[1,0,0] op_sel_hi:[1,0,0]\n"
    "v_mfma_f32_32x32x16_bf16 v[96:111], v[60:63], v[56:59], v[96:111]\n"
    "ds_read_b128 v[24:27], v5 offset:3456\n"
    "ds_read_b128 v[28:31], v5 offset:3472\n"
    "s_waitcnt lgkmcnt(4)\n"
    "v_cvt_pk_bf16_f32 v124, v8, v9\n"
    "v_cvt_pk_bf16_f32 v125, v10, v11\n"
    "v_cvt_pk_bf16_f32 v126, v12, v13\n"
    "v_cvt_pk_bf16_f32 v127, v14, v15\n"
    "s_nop 1\n"
    "v_mfma_f32_32x32x16_bf16 v[64:79], v[112:115], v[124:127], v[64:79]\n"
    "ds_read_b128 v[48:51], v6 offset:14336\n"
    "ds_read_b128 v[52:55], v6 offset:15360\n"
    "ds_read_b128 v[56:59], v6 offset:16384\n"
    "s_waitcnt vmcnt(1) lgkmcnt(4)\n"
    "v_fma_mix_f32 v8, v232, v24, v16 op_sel:[0,0,0] op_sel_hi:[1,0,0]\n"
    "v_fma_mix_f32 v9, v232, v25, v17 op_sel:[1,0,0] op_sel_hi:[1,0,0]\n"
    "v_fma_mix_f32 v10, v233, v26, v18 op_sel:[0,0,0] op_sel_hi:[1,0,0]\n"
    "v_fma_mix_f32 v11, v233, v27, v19 op_sel:[1,0,0] op_sel_hi:[1,0,0]\n"
    "v_mfma_f32_32x32x16_bf16 v[80:95], v[116:119], v[124:127], v[80:95]\n"
    "ds_read_b128 v[32:35], v5 offset:1728\n"
    "ds_read_b128 v[36:39], v5 offset:1744\n"
    "s_waitcnt lgkmcnt(5)\n"
    "v_fma_mix_f32 v12, v234, v28, v20 op_sel:[0,0,0] op_sel_hi:[1,0,0]\n"
    "v_fma_mix_f32 v13, v234, v29, v21 op_sel:[1,0,0] op_sel_hi:[1,0,0]\n"
    "v_fma_mix_f32 v14, v235, v30, v22 op_sel:[0,0,0] op_sel_hi:[1,0,0]\n"
    "v_fma_mix_f32 v15, v235, v31, v23 op_sel:[1,0,0] op_sel_hi:[1,0,0]\n"
    "v_mfma_f32_32x32x16_bf16 v[96:111], v[124:127], v[120:123], v[96:111]\n"
    "ds_read_b128 v[40:43], v5 offset:3520\n"
    "ds_read_b128 v[44:47], v5 offset:3536\n"
    "s_waitcnt lgkmcnt(4)\n"
    "v_cvt_pk_bf16_f32 v60, v8, v9\n"
    "v_cvt_pk_bf16_f32 v61, v10, v11\n"
    "v_cvt_pk_bf16_f32 v62, v12, v13\n"
    "v_cvt_pk_bf16_f32 v63, v14, v15\n"
    "s_nop 1\n"
    "v_mfma_f32_32x32x16_bf16 v[64:79], v[48:51], v[60:63], v[64:79]\n"
    "ds_read_b128 v[112:115], v6 offset:17408\n"
    "ds_read_b128 v[116:119], v6 offset:18432\n"
    "ds_read_b128 v[120:123], v6 offset:19456\n"
    "s_waitcnt vmcnt(0) lgkmcnt(4)\n"
    "v_fma_mix_f32 v8, v236, v40, v32 op_sel:[0,0,0] op_sel_hi:[1,0,0]\n"
    "v_fma_mix_f32 v9, v236, v41, v33 op_sel:[1,0,0] op_sel_hi:[1,0,0]\n"
    "v_fma_mix_f32 v10, v237, v42, v34 op_sel:[0,0,0] op_sel_hi:[1,0,0]\n"
    "v_fma_mix_f32 v11, v237, v43, v35 op_sel:[1,0,0] op_sel_hi:[1,0,0]\n"
    "v_mfma_f32_32x32x16_bf16 v[80:95], v[52:55], v[60:63], v[80:95]\n"
    "s_waitcnt lgkmcnt(3)\n"
    "v_fma_mix_f32 v12, v238, v44, v36 op_sel:[0,0,0] op_sel_hi:[1,0,0]\n"
    "v_fma_mix_f32 v13, v238, v45, v37 op_sel:[1,0,0] op_sel_hi:[1,0,0]\n"
    "v_fma_mix_f32 v14, v239, v46, v38 op_sel:[0,0,0] op_sel_hi:[1,0,0]\n"
    "v_fma_mix_f32 v15, v239, v47, v39 op_sel:[1,0,0] op_sel_hi:[1,0,0]\n"
    "v_mfma_f32_32x32x16_bf16 v[96:111], v[60:63], v[56:59], v[96:111]\n"
    "s_waitcnt lgkmcnt(0)\n"
    "v_cvt_pk_bf16_f32 v124, v8, v9\n"
    "v_cvt_pk_bf16_f32 v125, v10, v11\n"
    "v_cvt_pk_bf16_f32 v126, v12, v13\n"
    "v_cvt_pk_bf16_f32 v127, v14, v15\n"
    "s_nop 1\n"
    "v_mfma_f32_32x32x16_bf16 v[64:79], v[112:115], v[124:127], v[64:79]\n"
    "v_mfma_f32_32x32x16_bf16 v[80:95], v[116:119], v[124:127], v[80:95]\n"
    "v_mfma_f32_32x32x16_bf16 v[96:111], v[124:127], v[120:123], v[96:111]\n"
    "v_cmp_ne_u64 vcc, s[16:17], v[242:243]\n"
    "s_cbranch_vccnz Lchk_10%=\n"
    "Lchkb_11%=:\n"
    "buffer_load_dwordx4 v[32:35], v244, s[4:7], s48 offen offset:0 sc1\n"
    "buffer_load_dwordx4 v[36:39], v244, s[4:7], s48 offen offset:256 sc1\n"
    "buffer_load_dwordx4 v[40:43], v244, s[4:7], s48 offen offset:512 sc1\n"
    "buffer_load_dwordx4 v[44:47], v244, s[4:7], s48 offen offset:768 sc1\n"
    "v_and_b32 v7, 31, v1\n"
    "v_cmp_le_u32 s[62:63], 28, v7\n"
    "v_cmp_eq_u32 vcc, 28, v7\n"
    "v_mov_b32 v7, 0x3f803f80\n"
    "s_nop 0\n"
    "v_cndmask_b32 v7, 0, v7, vcc\n"
    "s_lshl_b32 s44, s21, 6\n"
    "s_add_u32 s44, s44, s23\n"
    "s_lshl_b32 s44, s44, 12\n"
    "s_add_u32 s44, s44, 9125888\n"
    "v_cvt_pk_bf16_f32 v128, v80, v81\n"
    "v_cvt_pk_bf16_f32 v129, v82, v83\n"
    "v_cvt_pk_bf16_f32 v130, v84, v85\n"
    "v_cvt_pk_bf16_f32 v131, v86, v87\n"
    "v_cvt_pk_bf16_f32 v132, v88, v89\n"
    "v_cvt_pk_bf16_f32 v133, v90, v91\n"
    "v_cvt_pk_bf16_f32 v134, v92, v93\n"
    "v_cvt_pk_bf16_f32 v135, v94, v95\n"
    "buffer_store_dwordx4 v[128:131], v0, s[4:7], s44 offen sc1\n"
    "v_mul_f32 v8, v80, v80\n"
    "v_mul_f32 v9, v92, v92\n"
    "v_fmac_f32 v8, v81, v81\n"
    "v_fmac_f32 v9, v93, v93\n"
    "v_fmac_f32 v8, v82, v82\n"
    "v_fmac_f32 v9, v94, v94\n"
    "v_fmac_f32 v8, v83, v83\n"
    "v_fmac_f32 v9, v95, v95\n"
    "v_fmac_f32 v8, v84, v84\n"
    "v_fmac_f32 v8, v85, v85\n"
    "v_fmac_f32 v8, v86, v86\n"
    "v_fmac_f32 v8, v87, v87\n"
    "v_fmac_f32 v8, v88, v88\n"
    "v_fmac_f32 v8, v89, v89\n"
    "v_fmac_f32 v8, v90, v90\n"
    "v_fmac_f32 v8, v91, v91\n"
    "v_cndmask_b32 v9, v9, 0, s[78:79]\n"
    "v_add_f32 v8, v8, v9\n"
    "v_mov_b32 v9, v8\n"
    "s_nop 1\n"
    "v_permlane32_swap_b32 v8, v9\n"
    "v_add_f32 v8, v8, v9\n"
    "v_sqrt_f32 v8, v8\n"
    "s_nop 0\n"
    "v_mul_f32 v8, 0x3f8147ae, v8\n"
    "v_cvt_pk_bf16_f32 v9, 1.0, v8\n"
    "v_cndmask_b32 v134, v134, v9, s[78:79]\n"
    "v_cndmask_b32 v135, v135, 0, s[78:79]\n"
    "buffer_store_dwordx4 v[132:135], v0, s[4:7], s44 offen offset:1024 sc1\n"
    "v_cvt_pk_bf16_f32 v136, v96, v97\n"
    "v_cvt_pk_bf16_f32 v137, v98, v99\n"
    "v_cvt_pk_bf16_f32 v138, v100, v101\n"
    "v_cvt_pk_bf16_f32 v139, v102, v103\n"
    "v_cvt_pk_bf16_f32 v140, v104, v105\n"
    "v_cvt_pk_bf16_f32 v141, v106, v107\n"
    "v_cvt_pk_bf16_f32 v142, v108, v109\n"
    "v_cvt_pk_bf16_f32 v143, v110, v111\n"
    "v_cndmask_b32 v136, v136, v7, s[62:63]\n"
    "v_cndmask_b32 v137, v137, v7, s[62:63]\n"
    "v_cndmask_b32 v138, v138, v7, s[62:63]\n"
    "v_cndmask_b32 v139, v139, v7, s[62:63]\n"
    "v_cndmask_b32 v140, v140, v7, s[62:63]\n"
    "v_cndmask_b32 v141, v141, v7, s[62:63]\n"
    "v_cndmask_b32 v142, v142, v7, s[62:63]\n"
    "v_cndmask_b32 v143, v143, v7, s[62:63]\n"
    "buffer_store_dwordx4 v[136:139], v0, s[4:7], s44 offen offset:2048 sc1\n"
    "buffer_store_dwordx4 v[140:143], v0, s[4:7], s44 offen offset:3072 sc1\n"
    "s_cmp_eq_u32 s31, 1\n"
    "s_movk_i32 s44, 896\n"
    "s_cmovk_i32 s44, 3584\n"
    "s_cselect_b32 s45, 12, 5\n"
    "s_cselect_b32 s46, 2048, 64\n"
    "s_cselect_b32 s47, 32, 2048\n"
    "v_lshrrev_b32 v14, 3, v1\n"
    "v_and_b32 v15, 7, v1\n"
    "v_mul_u32_u24 v12, 896, v14\n"
    "v_lshl_add_u32 v220, v15, 4, v12\n"
    "v_mul_u32_u24 v12, s44, v14\n"
    "v_lshl_add_u32 v228, v15, 4, v12\n"
    "s_lshl_b32 s44, s44, 3\n"
    "s_sub_u32 s44, s44, 1024\n"
    "v_add_u32 v221, 6144, v220\n"
    "v_add_u32 v229, s44, v228\n"
    "v_add_u32 v222, 6144, v221\n"
    "v_add_u32 v230, s44, v229\n"
    "v_add_u32 v223, 6144, v222\n"
    "v_add_u32 v231, s44, v230\n"
    "v_lshrrev_b32 v12, 5, v1\n"
    "v_lshlrev_b32 v12, 2, v12\n"
    "v_bfe_u32 v13, v1, 2, 2\n"
    "v_add_u32 v12, v12, v13\n"
    "v_lshlrev_b32 v12, 7, v12\n"
    "v_and_b32 v13, 3, v1\n"
    "v_lshl_add_u32 v12, v13, 3, v12\n"
    "v_bfe_u32 v13, v1, 4, 1\n"
    "v_lshlrev_b32 v14, s45, v13\n"
    "v_add3_u32 v232, v12, v14, s40\n"
    "v_add_u32 v233, s46, v232\n"
    "v_add_u32 v234, s47, v232\n"
    "v_add_u32 v235, s47, v233\n"
    "v_add_u32 v236, s47, v234\n"
    "v_add_u32 v237, s47, v235\n"
    "v_add_u32 v238, s47, v236\n"
    "v_add_u32 v239, s47, v237\n"
    "v_lshl_add_u32 v12, v13, 5, v12\n"
    "s_and_b32 s44, s18, 1\n"
    "s_lshl_b32 s44, s44, 6\n"
    "s_add_u32 s45, s44, s41\n"
    "v_add_u32 v14, s45, v12\n"
    "s_xor_b32 s45, s45, 64\n"
    "v_add_u32 v15, s45, v12\n"
    "v_mul_f32 v64, 0x3e8b97f6, v64\n"
    "v_mul_f32 v65, 0x3e8b97f6, v65\n"
    "v_mul_f32 v66, 0x3e8b97f6, v66\n"
    "v_mul_f32 v67, 0x3e8b97f6, v67\n"
    "v_mul_f32 v68, 0x3e8b97f6, v68\n"
    "v_mul_f32 v69, 0x3e8b97f6, v69\n"
    "v_mul_f32 v70, 0x3e8b97f6, v70\n"
    "v_mul_f32 v71, 0x3e8b97f6, v71\n"
    "v_mul_f32 v72, 0x3e8b97f6, v72\n"
    "v_mul_f32 v73, 0x3e8b97f6, v73\n"
    "v_mul_f32 v74, 0x3e8b97f6, v74\n"
    "v_mul_f32 v75, 0x3e8b97f6, v75\n"
    "v_mul_f32 v76, 0x3e8b97f6, v76\n"
    "v_mul_f32 v77, 0x3e8b97f6, v77\n"
    "v_mul_f32 v78, 0x3e8b97f6, v78\n"
    "v_mul_f32 v79, 0x3e8b97f6, v79\n"
    "v_max_f32_dpp v8, v8, v8 quad_perm:[1,0,3,2] row_mask:0xf bank_mask:0xf\n"
    "s_nop 1\n"
    "v_max_f32_dpp v8, v8, v8 quad_perm:[2,3,0,1] row_mask:0xf bank_mask:0xf\n"
    "s_nop 1\n"
    "v_max_f32_dpp v8, v8, v8 row_half_mirror row_mask:0xf bank_mask:0xf\n"
    "s_nop 1\n"
    "v_max_f32_dpp v8, v8, v8 row_mirror row_mask:0xf bank_mask:0xf\n"
    "v_mov_b32 v9, v8\n"
    "s_nop 1\n"
    "v_permlane16_swap_b32 v8, v9\n"
    "v_max_f32 v8, v8, v9\n"
    "v_mul_f32 v10, v64, v64\n"
    "v_mul_f32 v11, v76, v76\n"
    "v_fmac_f32 v10, v65, v65\n"
    "v_fmac_f32 v11, v77, v77\n"
    "v_fmac_f32 v10, v66, v66\n"
    "v_fmac_f32 v11, v78, v78\n"
    "v_fmac_f32 v10, v67, v67\n"
    "v_fmac_f32 v11, v79, v79\n"
    "v_fmac_f32 v10, v68, v68\n"
    "v_fmac_f32 v10, v69, v69\n"
    "v_fmac_f32 v10, v70, v70\n"
    "v_fmac_f32 v10, v71, v71\n"
    "v_fmac_f32 v10, v72, v72\n"
    "v_fmac_f32 v10, v73, v73\n"
    "v_fmac_f32 v10, v74, v74\n"
    "v_fmac_f32 v10, v75, v75\n"
    "v_cndmask_b32 v11, v11, 0, s[78:79]\n"
    "v_add_f32 v10, v10, v11\n"
    "v_mov_b32 v11, v10\n"
    "s_nop 1\n"
    "v_permlane32_swap_b32 v10, v11\n"
    "v_add_f32 v10, v10, v11\n"
    "v_sqrt_f32 v10, v10\n"
    "s_nop 0\n"
    "v_mul_f32 v10, 0x3f8147ae, v10\n"
    "v_cvt_pk_bf16_f32 v16, v64, v65\n"
    "v_cvt_pk_bf16_f32 v17, v66, v67\n"
    "v_cvt_pk_bf16_f32 v18, v68, v69\n"
    "v_cvt_pk_bf16_f32 v19, v70, v71\n"
    "v_cvt_pk_bf16_f32 v20, v72, v73\n"
    "v_cvt_pk_bf16_f32 v21, v74, v75\n"
    "v_cvt_pk_bf16_f32 v22, v76, v77\n"
    "v_cvt_pk_bf16_f32 v23, v78, v79\n"
    "v_mul_f32 v8, v8, v10\n"
    "v_cvt_pk_bf16_f32 v8, -v8, 0\n"
    "v_cvt_pk_bf16_f32 v10, v10, 0\n"
    "v_cndmask_b32 v22, v22, v8, s[78:79]\n"
    "v_cndmask_b32 v23, v23, v10, s[78:79]\n"
    "s_lshl_b32 s45, s18, 11\n"
    "s_add_u32 s45, s45, 143360\n"
    "v_add_u32 v7, s45, v0\n"
    "ds_write_b128 v7, v[16:19]\n"
    "ds_write_b128 v7, v[20:23] offset:1024\n"
    "s_lshl_b32 s50, s21, 6\n"
    "s_add_u32 s50, s50, s18\n"
    "s_lshl_b32 s50, s50, 12\n"
    "s_add_u32 s50, s50, 9125888\n"
    "s_add_u32 s12, s4, s50\n"
    "s_addc_u32 s13, s5, 0\n"
    "s_mov_b32 s14, 249856\n"
    "s_mov_b32 s15, 0x20000\n"
    "s_lshl_b32 s51, s22, 14\n"
    "s_lshl_b32 s59, s19, 2\n"
    "s_add_u32 s59, s59, s20\n"
    "s_sub_u32 s59, s59, 28\n"
    "s_lshl_b32 s59, s59, 2\n"
    "s_add_u32 s59, s59, s18\n"
    "s_min_u32 s59, s59, 111\n"
    "s_mul_i32 s53, s59, 29696\n"
    "s_add_u32 s53, s53, 4194304\n"
    "s_add_u32 s54, s53, 16384\n"
    "s_and_b32 s44, s18, 1\n"
    "s_lshl_b32 s44, s44, 10\n"
    "s_xor_b32 s45, s44, 1024\n"
    "s_mul_i32 s52, s72, 112\n"
    "s_mul_i32 s57, s18, 28\n"
    "s_add_u32 s52, s52, s57\n"
    "s_lshl_b32 s52, s52, 11\n"
    "s_add_u32 s52, s52, 7520256\n"
    "s_add_u32 s57, s52, s44\n"
    "s_add_u32 s58, s52, s45\n"
    "s_mul_i32 s55, s74, 207872\n"
    "s_mul_i32 s56, s18, 7168\n"
    "s_add_u32 s55, s55, s56\n"
    "s_add_u32 s55, s55, 4194304\n"
    "s_lshr_b32 s44, s18, 1\n"
    "s_xor_b32 s44, s44, s72\n"
    "s_and_b32 s44, s44, 1\n"
    "s_lshl_b32 s52, s71, 1\n"
    "s_add_u32 s65, s52, s44\n"
    "s_mul_i32 s52, s65, 28672\n"
    "s_lshl_b32 s44, s72, 7\n"
    "s_add_u32 s52, s52, s44\n"
    "s_and_b32 s44, s18, 1\n"
    "s_lshl_b32 s44, s44, 6\n"
    "s_add_u32 s77, s52, s44\n"
    "v_and_b32 v249, 31, v1\n"
    "v_mul_u32_u24 v249, 896, v249\n"
    "v_lshrrev_b32 v7, 5, v1\n"
    "v_lshl_add_u32 v249, v7, 3, v249\n"
    "s_lshl_b32 s45, s21, 2\n"
    "s_add_u32 s45, s45, s18\n"
    "s_lshl_b32 s45, s45, 7\n"
    "s_add_u32 s45, s45, 16384\n"
    "s_lshl_b32 s44, s22, 3\n"
    "s_add_u32 s44, s44, s45\n"
    "v_lshlrev_b32 v5, 3, v1\n"
    "v_add_u32 v5, s45, v5\n"
    "s_waitcnt vmcnt(0)\n"
    "s_mov_b64 exec, 1\n"
    "buffer_atomic_swap_x2 v[254:255], off, s[4:7], s44 sc1\n"
    "s_mov_b64 exec, -1\n"
    "s_lshl_b32 s64, s23, 5\n"
    "s_lshl_b32 s44, s21, 1\n"
    "s_add_u32 s64, s64, s44\n"
    "s_lshl_b32 s64, s64, 10\n"
    "s_add_u32 s64, s64, 13320192\n"
    "s_lshl_b32 s52, s72, 1\n"
    "s_and_b32 s44, s18, 1\n"
    "s_add_u32 s46, s52, s44\n"
    "s_mul_i32 s52, s65, 14\n"
    "s_add_u32 s52, s52, s46\n"
    "s_lshl_b32 s63, s52, 11\n"
    "s_add_u32 s63, s63, 15417344\n"
    "s_lshl_b32 s62, s52, 8\n"
    "s_add_u32 s62, s62, 294912\n"
    "s_lshl_b32 s52, s65, 4\n"
    "s_add_u32 s52, s52, s46\n"
    "s_lshl_b32 s52, s52, 3\n"
    "s_add_u32 s47, s52, 24576\n"
    "s_cmp_ge_u32 s20, 28\n"
    "s_cselect_b32 s63, 24723456, s63\n"
    "s_cselect_b32 s62, 24723456, s62\n"
    "s_cselect_b32 s47, 24723456, s47\n"
    "s_mul_i32 s67, s75, 112\n"
    "s_mul_i32 s52, s74, 7\n"
    "s_add_u32 s67, s67, s52\n"
    "s_lshl_b32 s67, s67, 10\n"
    "s_add_u32 s67, s67, 17252352\n"
    "s_lshr_b32 s46, s23, 1\n"
    "s_lshl_b32 s46, s46, 2\n"
    "s_lshr_b32 s45, s21, 2\n"
    "s_add_u32 s46, s46, s45\n"
    "s_lshl_b32 s46, s46, 4\n"
    "s_and_b32 s45, s23, 1\n"
    "s_lshl_b32 s45, s45, 2\n"
    "s_add_u32 s46, s46, s45\n"
    "s_and_b32 s45, s21, 3\n"
    "s_add_u32 s46, s46, s45\n"
    "s_lshl_b32 s46, s46, 3\n"
    "s_add_u32 s46, s46, 32768\n"
    "s_lshr_b32 s52, s75, 1\n"
    "s_lshl_b32 s52, s52, 2\n"
    "s_lshr_b32 s45, s74, 2\n"
    "s_add_u32 s52, s52, s45\n"
    "s_lshl_b32 s52, s52, 4\n"
    "s_and_b32 s45, s75, 1\n"
    "s_lshl_b32 s45, s45, 2\n"
    "s_add_u32 s52, s52, s45\n"
    "s_and_b32 s45, s74, 3\n"
    "s_add_u32 s52, s52, s45\n"
    "s_lshl_b32 s52, s52, 3\n"
    "s_add_u32 s52, s52, 49152\n"
    "s_mov_b32 s48, s46\n"
    "s_mov_b32 s49, s47\n"
    "s_mov_b32 s50, s52\n"
    "v_mov_b32 v13, s46\n"
    "v_writelane_b32 v13, s47, 1\n"
    "v_writelane_b32 v13, s52, 2\n"
    "s_lshl_b32 s66, s71, 2\n"
    "s_add_u32 s66, s66, s18\n"
    "s_lshl_b32 s66, s66, 7\n"
    "v_accvgpr_write_b32 a248, -1\n"
    "v_accvgpr_write_b32 a249, -1\n"
    "v_accvgpr_write_b32 a250, -1\n"
    "v_accvgpr_write_b32 a251, -1\n"
    "s_mul_i32 s52, s74, 7\n"
    "s_add_u32 s44, s52, 6\n"
    "s_lshr_b32 s52, s52, 2\n"
    "s_lshr_b32 s44, s44, 2\n"
    "s_mul_i32 s52, s52, 14\n"
    "s_mul_i32 s44, s44, 14\n"
    "s_add_u32 s44, s44, 13\n"
    "v_add_u32 v6, s52, v1\n"
    "v_min_u32 v6, s44, v6\n"
    "s_lshr_b32 s45, s59, 2\n"
    "s_mul_i32 s45, s45, 14\n"
    "s_sub_u32 s45, s45, 42\n"
    "v_min_u32 v7, 55, v1\n"
    "v_add_u32 v7, s45, v7\n"
    "v_cmp_le_u32 vcc, 42, v1\n"
    "s_nop 1\n"
    "v_cndmask_b32 v6, v6, v7, vcc\n"
    "v_lshlrev_b32 v6, 3, v6\n"
    "v_add_u32 v6, 8192, v6\n"
    "s_mul_i32 s44, s72, 28\n"
    "s_mul_i32 s45, s18, 7\n"
    "s_add_u32 s44, s44, s45\n"
    "s_lshl_b32 s44, s44, 3\n"
    "s_add_u32 s44, s44, 11328\n"
    "v_min_u32 v7, 6, v1\n"
    "v_lshlrev_b32 v7, 3, v7\n"
    "v_add_u32 v7, s44, v7\n"
    "s_add_u32 s44, s18, 0\n"
    "s_and_b32 s44, s44, 3\n"
    "s_lshl_b32 s45, s44, 11\n"
    "s_add_u32 s45, s45, 143360\n"
    "v_add_u32 v24, s45, v0\n"
    "s_mul_i32 s44, s44, 12288\n"
    "v_add_u32 v250, s44, v0\n"
    "s_add_u32 s44, s18, 1\n"
    "s_and_b32 s44, s44, 3\n"
    "s_lshl_b32 s45, s44, 11\n"
    "s_add_u32 s45, s45, 143360\n"
    "v_add_u32 v25, s45, v0\n"
    "s_mul_i32 s44, s44, 12288\n"
    "s_add_u32 s44, s44, 0\n"
    "v_add_u32 v251, s44, v0\n"
    "s_add_u32 s44, s18, 2\n"
    "s_and_b32 s44, s44, 3\n"
    "s_lshl_b32 s45, s44, 11\n"
    "s_add_u32 s45, s45, 143360\n"
    "v_add_u32 v26, s45, v0\n"
    "s_mul_i32 s44, s44, 12288\n"
    "s_add_u32 s44, s44, 4096\n"
    "v_add_u32 v252, s44, v0\n"
    "s_add_u32 s44, s18, 3\n"
    "s_and_b32 s44, s44, 3\n"
    "s_lshl_b32 s45, s44, 11\n"
    "s_add_u32 s45, s45, 143360\n"
    "v_add_u32 v27, s45, v0\n"
    "s_mul_i32 s44, s44, 12288\n"
    "s_add_u32 s44, s44, 8192\n"
    "v_add_u32 v253, s44, v0\n"
    "v_add_f32 v32, v32, v36\n"
    "v_add_f32 v33, v33, v37\n"
    "v_add_f32 v34, v34, v38\n"
    "v_add_f32 v35, v35, v39\n"
    "v_add_f32 v40, v40, v44\n"
    "v_add_f32 v41, v41, v45\n"
    "v_add_f32 v42, v42, v46\n"
    "v_add_f32 v43, v43, v47\n"
    "v_add_f32 v32, v32, v40\n"
    "v_add_f32 v33, v33, v41\n"
    "v_add_f32 v34, v34, v42\n"
    "v_add_f32 v35, v35, v43\n"
    "v_cvt_pk_bf16_f32 v36, v32, v33\n"
    "v_cvt_pk_bf16_f32 v37, v34, v35\n"
    "v_lshlrev_b32 v32, 16, v36\n"
    "v_and_b32 v33, 0xffff0000, v36\n"
    "v_lshlrev_b32 v34, 16, v37\n"
    "v_and_b32 v35, 0xffff0000, v37\n"
    "v_add_f32 v40, 1.0, v32\n"
    "v_add_f32 v41, 1.0, v33\n"
    "v_add_f32 v42, 1.0, v34\n"
    "v_add_f32 v43, 1.0, v35\n"
    "v_cvt_pk_bf16_f32 v36, v40, v41\n"
    "v_cvt_pk_bf16_f32 v37, v42, v43\n"
    "v_lshlrev_b32 v40, 16, v36\n"
    "v_and_b32 v41, 0xffff0000, v36\n"
    "v_lshlrev_b32 v42, 16, v37\n"
    "v_and_b32 v43, 0xffff0000, v37\n"
    "v_cndmask_b32 v32, v32, v40, s[60:61]\n"
    "v_cndmask_b32 v33, v33, v41, s[60:61]\n"
    "v_cndmask_b32 v34, v34, v42, s[60:61]\n"
    "v_cndmask_b32 v35, v35, v43, s[60:61]\n"
    "ds_write_b128 v245, v[32:35]\n"
    "s_waitcnt lgkmcnt(0)\n"
    "s_barrier\n"
    "ds_read_b128 a[0:3], v24\n"
    "ds_read_b128 a[4:7], v24 offset:1024\n"
    "ds_read_b128 a[8:11], v25\n"
    "ds_read_b128 a[12:15], v25 offset:1024\n"
    "ds_read_b128 a[16:19], v26\n"
    "ds_read_b128 a[20:23], v26 offset:1024\n"
    "ds_read_b128 a[24:27], v27\n"
    "ds_read_b128 a[28:31], v27 offset:1024\n"
    "s_mov_b32 s70, 0\n"
    "s_mov_b64 exec, 65535\n"
    "Lpoll_12%=:\n"
    "v_mov_b32 v28, 0\n"
    "v_mov_b32 v29, 0\n"
    "buffer_atomic_or_x2 v[28:29], v5, s[4:7], 0 offen sc0 sc1\n"
    "s_waitcnt vmcnt(0)\n"
    "v_cmp_ne_u64 vcc, s[16:17], v[28:29]\n"
    "s_nop 0\n"
    "s_and_b64 exec, exec, vcc\n"
    "s_cbranch_scc0 Lpollok_13%=\n"
    "s_sleep 2\n"
    "s_add_u32 s70, s70, 1\n"
    "s_cmp_lt_u32 s70, 0x40000\n"
    "s_cbranch_scc1 Lpoll_12%=\n"
    "Lpollok_13%=:\n"
    "s_mov_b64 exec, -1\n"
    "s_add_u32 s51, s51, 0x4000\n"
    "s_and_b32 s51, s51, 0x3ffff\n"
    "buffer_load_dwordx4 v[144:147], v0, s[12:15], s51 offen offset:0 sc1\n"
    "buffer_load_dwordx4 v[148:151], v0, s[12:15], s51 offen offset:1024 sc1\n"
    "buffer_load_dwordx4 v[152:155], v0, s[12:15], s51 offen offset:2048 sc1\n"
    "buffer_load_dwordx4 v[156:159], v0, s[12:15], s51 offen offset:3072 sc1\n"
    "s_add_u32 s51, s51, 0x4000\n"
    "s_and_b32 s51, s51, 0x3ffff\n"
    "buffer_load_dwordx4 v[160:163], v0, s[12:15], s51 offen offset:0 sc1\n"
    "buffer_load_dwordx4 v[164:167], v0, s[12:15], s51 offen offset:1024 sc1\n"
    "buffer_load_dwordx4 v[168:171], v0, s[12:15], s51 offen offset:2048 sc1\n"
    "buffer_load_dwordx4 v[172:175], v0, s[12:15], s51 offen offset:3072 sc1\n"
    "s_add_u32 s51, s51, 0x4000\n"
    "s_and_b32 s51, s51, 0x3ffff\n"
    "buffer_load_dwordx4 v[176:179], v0, s[12:15], s51 offen offset:0 sc1\n"
    "buffer_load_dwordx4 v[180:183], v0, s[12:15], s51 offen offset:1024 sc1\n"
    "buffer_load_dwordx4 v[184:187], v0, s[12:15], s51 offen offset:2048 sc1\n"
    "buffer_load_dwordx4 v[188:191], v0, s[12:15], s51 offen offset:3072 sc1\n"
    "v_mov_b32 v248, v134\n"
    "v_and_b32 v5, 31, v1\n"
    "v_lshlrev_b32 v5, 3, v5\n"
    "s_waitcnt lgkmcnt(5)\n"
    "v_mfma_f32_32x32x16_bf16 v[16:31], v[128:131], a[8:11], 0\n"
    "s_waitcnt lgkmcnt(4)\n"
    "v_mfma_f32_32x32x16_bf16 v[16:31], v[132:135], a[12:15], v[16:31]\n"
    "s_waitcnt lgkmcnt(3)\n"
    "v_mfma_f32_32x32x16_bf16 v[32:47], v[128:131], a[16:19], 0\n"
    "s_nop 9\n"
    "v_exp_f32 v16, v16\n"
    "v_exp_f32 v17, v17\n"
    "v_exp_f32 v18, v18\n"
    "v_exp_f32 v19, v19\n"
    "s_waitcnt lgkmcnt(2)\n"
    "v_mfma_f32_32x32x16_bf16 v[32:47], v[132:135], a[20:23], v[32:47]\n"
    "v_exp_f32 v20, v20\n"
    "v_exp_f32 v21, v21\n"
    "v_exp_f32 v22, v22\n"
    "v_exp_f32 v23, v23\n"
    "v_cvt_pk_bf16_f32 v48, v16, v17\n"
    "v_cvt_pk_bf16_f32 v49, v18, v19\n"
    "v_exp_f32 v24, v24\n"
    "v_exp_f32 v25, v25\n"
    "v_exp_f32 v26, v26\n"
    "v_exp_f32 v27, v27\n"
    "v_cvt_pk_bf16_f32 v50, v20, v21\n"
    "v_cvt_pk_bf16_f32 v51, v22, v23\n"
    "v_exp_f32 v28, v28\n"
    "v_exp_f32 v29, v29\n"
    "v_exp_f32 v30, v30\n"
    "v_exp_f32 v31, v31\n"
    "v_cvt_pk_bf16_f32 v52, v24, v25\n"
    "v_cvt_pk_bf16_f32 v53, v26, v27\n"
    "v_cvt_pk_bf16_f32 v54, v28, v29\n"
    "v_cvt_pk_bf16_f32 v55, v30, v31\n"
    "s_waitcnt lgkmcnt(1)\n"
    "v_mfma_f32_32x32x16_bf16 v[16:31], v[128:131], a[24:27], 0\n"
    "s_mov_b32 m0, s40\n"
    "s_nop 0\n"
    "buffer_load_dwordx4 v228, s[32:35], s0 offen offset:0 lds\n"
    "v_exp_f32 v32, v32\n"
    "v_exp_f32 v33, v33\n"
    "v_exp_f32 v34, v34\n"
    "v_exp_f32 v35, v35\n"
    "s_waitcnt lgkmcnt(0)\n"
    "v_mfma_f32_32x32x16_bf16 v[16:31], v[132:135], a[28:31], v[16:31]\n"
    "buffer_load_dwordx4 v229, s[32:35], s0 offen offset:1024 lds\n"
    "v_exp_f32 v36, v36\n"
    "v_exp_f32 v37, v37\n"
    "v_exp_f32 v38, v38\n"
    "v_exp_f32 v39, v39\n"
    "v_cvt_pk_bf16_f32 v56, v32, v33\n"
    "v_cvt_pk_bf16_f32 v57, v34, v35\n"
    "v_mfma_f32_32x32x16_bf16 v[80:95], v[136:139], v[48:51], 0\n"
    "v_exp_f32 v40, v40\n"
    "v_exp_f32 v41, v41\n"
    "v_exp_f32 v42, v42\n"
    "v_exp_f32 v43, v43\n"
    "v_cvt_pk_bf16_f32 v58, v36, v37\n"
    "v_cvt_pk_bf16_f32 v59, v38, v39\n"
    "v_mfma_f32_32x32x16_bf16 v[80:95], v[140:143], v[52:55], v[80:95]\n"
    "buffer_load_dwordx4 v230, s[32:35], s0 offen offset:2048 lds\n"
    "v_exp_f32 v44, v44\n"
    "v_exp_f32 v45, v45\n"
    "v_exp_f32 v46, v46\n"
    "v_exp_f32 v47, v47\n"
    "v_cvt_pk_bf16_f32 v60, v40, v41\n"
    "v_cvt_pk_bf16_f32 v61, v42, v43\n"
    "v_cvt_pk_bf16_f32 v62, v44, v45\n"
    "v_cvt_pk_bf16_f32 v63, v46, v47\n"
    "v_mfma_f32_32x32x16_bf16 v[32:47], v[128:131], a[0:3], 0\n"
    "v_exp_f32 v16, v16\n"
    "v_exp_f32 v17, v17\n"
    "v_exp_f32 v18, v18\n"
    "v_exp_f32 v19, v19\n"
    "v_mfma_f32_32x32x16_bf16 v[32:47], v[132:135], a[4:7], v[32:47]\n"
    "buffer_load_dwordx4 v231, s[32:35], s0 offen offset:3072 lds\n"
    "v_exp_f32 v20, v20\n"
    "v_exp_f32 v21, v21\n"
    "v_exp_f32 v22, v22\n"
    "v_exp_f32 v23, v23\n"
    "v_cvt_pk_bf16_f32 v48, v16, v17\n"
    "v_cvt_pk_bf16_f32 v49, v18, v19\n"
    "v_mfma_f32_32x32x16_bf16 v[96:111], v[136:139], v[56:59], 0\n"
    "v_exp_f32 v24, v24\n"
    "v_exp_f32 v25, v25\n"
    "v_exp_f32 v26, v26\n"
    "v_exp_f32 v27, v27\n"
    "v_cvt_pk_bf16_f32 v50, v20, v21\n"
    "v_cvt_pk_bf16_f32 v51, v22, v23\n"
    "v_mfma_f32_32x32x16_bf16 v[96:111], v[140:143], v[60:63], v[96:111]\n"
    "s_waitcnt vmcnt(12)\n"
    "s_add_u32 m0, s40, 4096\n"
    "s_nop 0\n"
    "buffer_load_dwordx4 v228, s[96:99], s1 offen offset:0 lds\n"
    "v_exp_f32 v28, v28\n"
    "v_exp_f32 v29, v29\n"
    "v_exp_f32 v30, v30\n"
    "v_exp_f32 v31, v31\n"
    "v_cvt_pk_bf16_f32 v52, v24, v25\n"
    "v_cvt_pk_bf16_f32 v53, v26, v27\n"
    "v_cvt_pk_bf16_f32 v54, v28, v29\n"
    "v_cvt_pk_bf16_f32 v55, v30, v31\n"
    "v_mfma_f32_32x32x16_bf16 v[16:31], v[144:147], a[8:11], 0\n"
    "v_exp_f32 v32, v32\n"
    "v_exp_f32 v33, v33\n"
    "v_exp_f32 v34, v34\n"
    "v_exp_f32 v35, v35\n"
    "v_mfma_f32_32x32x16_bf16 v[16:31], v[148:151], a[12:15], v[16:31]\n"
    "buffer_load_dwordx4 v229, s[96:99], s1 offen offset:1024 lds\n"
    "v_exp_f32 v36, v36\n"
    "v_exp_f32 v37, v37\n"
    "v_exp_f32 v38, v38\n"
    "v_exp_f32 v39, v39\n"
    "v_cvt_pk_bf16_f32 v56, v32, v33\n"
    "v_cvt_pk_bf16_f32 v57, v34, v35\n"
    "v_mfma_f32_32x32x16_bf16 v[112:127], v[136:139], v[48:51], 0\n"
    "s_add_u32 s51, s51, 0x4000\n"
    "v_exp_f32 v40, v40\n"
    "v_exp_f32 v41, v41\n"
    "v_exp_f32 v42, v42\n"
    "v_exp_f32 v43, v43\n"
    "v_cvt_pk_bf16_f32 v58, v36, v37\n"
    "v_cvt_pk_bf16_f32 v59, v38, v39\n"
    "v_mfma_f32_32x32x16_bf16 v[112:127], v[140:143], v[52:55], v[112:127]\n"
    "s_and_b32 s51, s51, 0x3ffff\n"
    "buffer_load_dwordx4 v230, s[96:99], s1 offen offset:2048 lds\n"
    "v_exp_f32 v44, v44\n"
    "v_exp_f32 v45, v45\n"
    "v_exp_f32 v46, v46\n"
    "v_exp_f32 v47, v47\n"
    "v_cvt_pk_bf16_f32 v60, v40, v41\n"
    "v_cvt_pk_bf16_f32 v61, v42, v43\n"
    "v_cvt_pk_bf16_f32 v62, v44, v45\n"
    "v_cvt_pk_bf16_f32 v63, v46, v47\n"
    "v_mfma_f32_32x32x16_bf16 v[32:47], v[144:147], a[16:19], 0\n"
    "buffer_load_dwordx4 v[128:131], v0, s[12:15], s51 offen offset:0 sc1\n"
    "v_exp_f32 v16, v16\n"
    "v_exp_f32 v17, v17\n"
    "v_exp_f32 v18, v18\n"
    "v_exp_f32 v19, v19\n"
    "v_mfma_f32_32x32x16_bf16 v[32:47], v[148:151], a[20:23], v[32:47]\n"
    "v_exp_f32 v20, v20\n"
    "v_exp_f32 v21, v21\n"
    "v_exp_f32 v22, v22\n"
    "v_exp_f32 v23, v23\n"
    "v_cvt_pk_bf16_f32 v48, v16, v17\n"
    "v_cvt_pk_bf16_f32 v49, v18, v19\n"
    "v_mfma_f32_32x32x16_bf16 v[64:79], v[136:139], v[56:59], 0\n"
    "buffer_load_dwordx4 v231, s[96:99], s1 offen offset:3072 lds\n"
    "v_exp_f32 v24, v24\n"
    "v_exp_f32 v25, v25\n"
    "v_exp_f32 v26, v26\n"
    "v_exp_f32 v27, v27\n"
    "v_cvt_pk_bf16_f32 v50, v20, v21\n"
    "v_cvt_pk_bf16_f32 v51, v22, v23\n"
    "v_mfma_f32_32x32x16_bf16 v[64:79], v[140:143], v[60:63], v[64:79]\n"
    "v_exp_f32 v28, v28\n"
    "v_exp_f32 v29, v29\n"
    "v_exp_f32 v30, v30\n"
    "v_exp_f32 v31, v31\n"
    "v_cvt_pk_bf16_f32 v52, v24, v25\n"
    "v_cvt_pk_bf16_f32 v53, v26, v27\n"
    "v_cvt_pk_bf16_f32 v54, v28, v29\n"
    "v_cvt_pk_bf16_f32 v55, v30, v31\n"
    "v_mfma_f32_32x32x16_bf16 v[16:31], v[144:147], a[24:27], 0\n"
    "buffer_load_dwordx4 v[132:135], v0, s[12:15], s51 offen offset:1024 sc1\n"
    "v_exp_f32 v32, v32\n"
    "v_exp_f32 v33, v33\n"
    "v_exp_f32 v34, v34\n"
    "v_exp_f32 v35, v35\n"
    "v_mfma_f32_32x32x16_bf16 v[16:31], v[148:151], a[28:31], v[16:31]\n"
    "v_exp_f32 v36, v36\n"
    "v_exp_f32 v37, v37\n"
    "v_exp_f32 v38, v38\n"
    "v_exp_f32 v39, v39\n"
    "v_cvt_pk_bf16_f32 v56, v32, v33\n"
    "v_cvt_pk_bf16_f32 v57, v34, v35\n"
    "v_mfma_f32_32x32x16_bf16 v[80:95], v[152:155], v[48:51], v[80:95]\n"
    "v_exp_f32 v40, v40\n"
    "v_exp_f32 v41, v41\n"
    "v_exp_f32 v42, v42\n"
    "v_exp_f32 v43, v43\n"
    "v_cvt_pk_bf16_f32 v58, v36, v37\n"
    "v_cvt_pk_bf16_f32 v59, v38, v39\n"
    "v_mfma_f32_32x32x16_bf16 v[80:95], v[156:159], v[52:55], v[80:95]\n"
    "v_exp_f32 v44, v44\n"
    "v_exp_f32 v45, v45\n"
    "v_exp_f32 v46, v46\n"
    "v_exp_f32 v47, v47\n"
    "v_cvt_pk_bf16_f32 v60, v40, v41\n"
    "v_cvt_pk_bf16_f32 v61, v42, v43\n"
    "v_cvt_pk_bf16_f32 v62, v44, v45\n"
    "v_cvt_pk_bf16_f32 v63, v46, v47\n"
    "v_mfma_f32_32x32x16_bf16 v[32:47], v[144:147], a[0:3], 0\n"
    "buffer_load_dwordx4 v[136:139], v0, s[12:15], s51 offen offset:2048 sc1\n"
    "v_exp_f32 v16, v16\n"
    "v_exp_f32 v17, v17\n"
    "v_exp_f32 v18, v18\n"
    "v_exp_f32 v19, v19\n"
    "v_mfma_f32_32x32x16_bf16 v[32:47], v[148:151], a[4:7], v[32:47]\n"
    "v_max_u32 v248, v248, v150\n"
    "v_exp_f32 v20, v20\n"
    "v_exp_f32 v21, v21\n"
    "v_exp_f32 v22, v22\n"
    "v_exp_f32 v23, v23\n"
    "v_cvt_pk_bf16_f32 v48, v16, v17\n"
    "v_cvt_pk_bf16_f32 v49, v18, v19\n"
    "v_mfma_f32_32x32x16_bf16 v[96:111], v[152:155], v[56:59], v[96:111]\n"
    "v_exp_f32 v24, v24\n"
    "v_exp_f32 v25, v25\n"
    "v_exp_f32 v26, v26\n"
    "v_exp_f32 v27, v27\n"
    "v_cvt_pk_bf16_f32 v50, v20, v21\n"
    "v_cvt_pk_bf16_f32 v51, v22, v23\n"
    "v_mfma_f32_32x32x16_bf16 v[96:111], v[156:159], v[60:63], v[96:111]\n"
    "s_waitcnt vmcnt(15)\n"
    "v_exp_f32 v28, v28\n"
    "v_exp_f32 v29, v29\n"
    "v_exp_f32 v30, v30\n"
    "v_exp_f32 v31, v31\n"
    "v_cvt_pk_bf16_f32 v52, v24, v25\n"
    "v_cvt_pk_bf16_f32 v53, v26, v27\n"
    "v_cvt_pk_bf16_f32 v54, v28, v29\n"
    "v_cvt_pk_bf16_f32 v55, v30, v31\n"
    "v_mfma_f32_32x32x16_bf16 v[16:31], v[160:163], a[8:11], 0\n"
    "buffer_load_dwordx4 v[140:143], v0, s[12:15], s51 offen offset:3072 sc1\n"
    "v_exp_f32 v32, v32\n"
    "v_exp_f32 v33, v33\n"
    "v_exp_f32 v34, v34\n"
    "v_exp_f32 v35, v35\n"
    "v_mfma_f32_32x32x16_bf16 v[16:31], v[164:167], a[12:15], v[16:31]\n"
    "v_exp_f32 v36, v36\n"
    "v_exp_f32 v37, v37\n"
    "v_exp_f32 v38, v38\n"
    "v_exp_f32 v39, v39\n"
    "v_cvt_pk_bf16_f32 v56, v32, v33\n"
    "v_cvt_pk_bf16_f32 v57, v34, v35\n"
    "v_mfma_f32_32x32x16_bf16 v[112:127], v[152:155], v[48:51], v[112:127]\n"
    "s_add_u32 s51, s51, 0x4000\n"
    "v_exp_f32 v40, v40\n"
    "v_exp_f32 v41, v41\n"
    "v_exp_f32 v42, v42\n"
    "v_exp_f32 v43, v43\n"
    "v_cvt_pk_bf16_f32 v58, v36, v37\n"
    "v_cvt_pk_bf16_f32 v59, v38, v39\n"
    "v_mfma_f32_32x32x16_bf16 v[112:127], v[156:159], v[52:55], v[112:127]\n"
    "s_and_b32 s51, s51, 0x3ffff\n"
    "v_exp_f32 v44, v44\n"
    "v_exp_f32 v45, v45\n"
    "v_exp_f32 v46, v46\n"
    "v_exp_f32 v47, v47\n"
    "v_cvt_pk_bf16_f32 v60, v40, v41\n"
    "v_cvt_pk_bf16_f32 v61, v42, v43\n"
    "v_cvt_pk_bf16_f32 v62, v44, v45\n"
    "v_cvt_pk_bf16_f32 v63, v46, v47\n"
    "v_mfma_f32_32x32x16_bf16 v[32:47], v[160:163], a[16:19], 0\n"
    "buffer_load_dwordx4 v[144:147], v0, s[12:15], s51 offen offset:0 sc1\n"
    "v_exp_f32 v16, v16\n"
    "v_exp_f32 v17, v17\n"
    "v_exp_f32 v18, v18\n"
    "v_exp_f32 v19, v19\n"
    "v_mfma_f32_32x32x16_bf16 v[32:47], v[164:167], a[20:23], v[32:47]\n"
    "v_exp_f32 v20, v20\n"
    "v_exp_f32 v21, v21\n"
    "v_exp_f32 v22, v22\n"
    "v_exp_f32 v23, v23\n"
    "v_cvt_pk_bf16_f32 v48, v16, v17\n"
    "v_cvt_pk_bf16_f32 v49, v18, v19\n"
    "v_mfma_f32_32x32x16_bf16 v[64:79], v[152:155], v[56:59], v[64:79]\n"
    "v_exp_f32 v24, v24\n"
    "v_exp_f32 v25, v25\n"
    "v_exp_f32 v26, v26\n"
    "v_exp_f32 v27, v27\n"
    "v_cvt_pk_bf16_f32 v50, v20, v21\n"
    "v_cvt_pk_bf16_f32 v51, v22, v23\n"
    "v_mfma_f32_32x32x16_bf16 v[64:79], v[156:159], v[60:63], v[64:79]\n"
    "v_exp_f32 v28, v28\n"
    "v_exp_f32 v29, v29\n"
    "v_exp_f32 v30, v30\n"
    "v_exp_f32 v31, v31\n"
    "v_cvt_pk_bf16_f32 v52, v24, v25\n"
    "v_cvt_pk_bf16_f32 v53, v26, v27\n"
    "v_cvt_pk_bf16_f32 v54, v28, v29\n"
    "v_cvt_pk_bf16_f32 v55, v30, v31\n"
    "v_mfma_f32_32x32x16_bf16 v[16:31], v[160:163], a[24:27], 0\n"
    "buffer_load_dwordx4 v[148:151], v0, s[12:15], s51 offen offset:1024 sc1\n"
    "v_exp_f32 v32, v32\n"
    "v_exp_f32 v33, v33\n"
    "v_exp_f32 v34, v34\n"
    "v_exp_f32 v35, v35\n"
    "v_mfma_f32_32x32x16_bf16 v[16:31], v[164:167], a[28:31], v[16:31]\n"
    "v_exp_f32 v36, v36\n"
    "v_exp_f32 v37, v37\n"
    "v_exp_f32 v38, v38\n"
    "v_exp_f32 v39, v39\n"
    "v_cvt_pk_bf16_f32 v56, v32, v33\n"
    "v_cvt_pk_bf16_f32 v57, v34, v35\n"
    "v_mfma_f32_32x32x16_bf16 v[80:95], v[168:171], v[48:51], v[80:95]\n"
    "v_exp_f32 v40, v40\n"
    "v_exp_f32 v41, v41\n"
    "v_exp_f32 v42, v42\n"
    "v_exp_f32 v43, v43\n"
    "v_cvt_pk_bf16_f32 v58, v36, v37\n"
    "v_cvt_pk_bf16_f32 v59, v38, v39\n"
    "v_mfma_f32_32x32x16_bf16 v[80:95], v[172:175], v[52:55], v[80:95]\n"
    "v_exp_f32 v44, v44\n"
    "v_exp_f32 v45, v45\n"
    "v_exp_f32 v46, v46\n"
    "v_exp_f32 v47, v47\n"
    "v_cvt_pk_bf16_f32 v60, v40, v41\n"
    "v_cvt_pk_bf16_f32 v61, v42, v43\n"
    "v_cvt_pk_bf16_f32 v62, v44, v45\n"
    "v_cvt_pk_bf16_f32 v63, v46, v47\n"
    "v_mfma_f32_32x32x16_bf16 v[32:47], v[160:163], a[0:3], 0\n"
    "buffer_load_dwordx4 v[152:155], v0, s[12:15], s51 offen offset:2048 sc1\n"
    "v_exp_f32 v16, v16\n"
    "v_exp_f32 v17, v17\n"
    "v_exp_f32 v18, v18\n"
    "v_exp_f32 v19, v19\n"
    "v_mfma_f32_32x32x16_bf16 v[32:47], v[164:167], a[4:7], v[32:47]\n"
    "v_max_u32 v248, v248, v166\n"
    "v_exp_f32 v20, v20\n"
    "v_exp_f32 v21, v21\n"
    "v_exp_f32 v22, v22\n"
    "v_exp_f32 v23, v23\n"
    "v_cvt_pk_bf16_f32 v48, v16, v17\n"
    "v_cvt_pk_bf16_f32 v49, v18, v19\n"
    "v_mfma_f32_32x32x16_bf16 v[96:111], v[168:171], v[56:59], v[96:111]\n"
    "v_exp_f32 v24, v24\n"
    "v_exp_f32 v25, v25\n"
    "v_exp_f32 v26, v26\n"
    "v_exp_f32 v27, v27\n"
    "v_cvt_pk_bf16_f32 v50, v20, v21\n"
    "v_cvt_pk_bf16_f32 v51, v22, v23\n"
    "v_mfma_f32_32x32x16_bf16 v[96:111], v[172:175], v[60:63], v[96:111]\n"
    "s_waitcnt vmcnt(15)\n"
    "v_exp_f32 v28, v28\n"
    "v_exp_f32 v29, v29\n"
    "v_exp_f32 v30, v30\n"
    "v_exp_f32 v31, v31\n"
    "v_cvt_pk_bf16_f32 v52, v24, v25\n"
    "v_cvt_pk_bf16_f32 v53, v26, v27\n"
    "v_cvt_pk_bf16_f32 v54, v28, v29\n"
    "v_cvt_pk_bf16_f32 v55, v30, v31\n"
    "v_mfma_f32_32x32x16_bf16 v[16:31], v[176:179], a[8:11], 0\n"
    "buffer_load_dwordx4 v[156:159], v0, s[12:15], s51 offen offset:3072 sc1\n"
    "v_exp_f32 v32, v32\n"
    "v_exp_f32 v33, v33\n"
    "v_exp_f32 v34, v34\n"
    "v_exp_f32 v35, v35\n"
    "v_mfma_f32_32x32x16_bf16 v[16:31], v[180:183], a[12:15], v[16:31]\n"
    "v_exp_f32 v36, v36\n"
    "v_exp_f32 v37, v37\n"
    "v_exp_f32 v38, v38\n"
    "v_exp_f32 v39, v39\n"
    "v_cvt_pk_bf16_f32 v56, v32, v33\n"
    "v_cvt_pk_bf16_f32 v57, v34, v35\n"
    "v_mfma_f32_32x32x16_bf16 v[112:127], v[168:171], v[48:51], v[112:127]\n"
    "s_add_u32 s51, s51, 0x4000\n"
    "v_exp_f32 v40, v40\n"
    "v_exp_f32 v41, v41\n"
    "v_exp_f32 v42, v42\n"
    "v_exp_f32 v43, v43\n"
    "v_cvt_pk_bf16_f32 v58, v36, v37\n"
    "v_cvt_pk_bf16_f32 v59, v38, v39\n"
    "v_mfma_f32_32x32x16_bf16 v[112:127], v[172:175], v[52:55], v[112:127]\n"
    "s_and_b32 s51, s51, 0x3ffff\n"
    "v_exp_f32 v44, v44\n"
    "v_exp_f32 v45, v45\n"
    "v_exp_f32 v46, v46\n"
    "v_exp_f32 v47, v47\n"
    "v_cvt_pk_bf16_f32 v60, v40, v41\n"
    "v_cvt_pk_bf16_f32 v61, v42, v43\n"
    "v_cvt_pk_bf16_f32 v62, v44, v45\n"
    "v_cvt_pk_bf16_f32 v63, v46, v47\n"
    "v_mfma_f32_32x32x16_bf16 v[32:47], v[176:179], a[16:19], 0\n"
    "buffer_load_dwordx4 v[160:163], v0, s[12:15], s51 offen offset:0 sc1\n"
    "v_exp_f32 v16, v16\n"
    "v_exp_f32 v17, v17\n"
    "v_exp_f32 v18, v18\n"
    "v_exp_f32 v19, v19\n"
    "v_mfma_f32_32x32x16_bf16 v[32:47], v[180:183], a[20:23], v[32:47]\n"
    "s_mov_b32 m0, s41\n"
    "s_nop 0\n"
    "buffer_load_dwordx4 v220, s[36:39], s26 offen offset:0 lds\n"
    "v_exp_f32 v20, v20\n"
    "v_exp_f32 v21, v21\n"
    "v_exp_f32 v22, v22\n"
    "v_exp_f32 v23, v23\n"
    "v_cvt_pk_bf16_f32 v48, v16, v17\n"
    "v_cvt_pk_bf16_f32 v49, v18, v19\n"
    "v_mfma_f32_32x32x16_bf16 v[64:79], v[168:171], v[56:59], v[64:79]\n"
    "v_exp_f32 v24, v24\n"
    "v_exp_f32 v25, v25\n"
    "v_exp_f32 v26, v26\n"
    "v_exp_f32 v27, v27\n"
    "v_cvt_pk_bf16_f32 v50, v20, v21\n"
    "v_cvt_pk_bf16_f32 v51, v22, v23\n"
    "v_mfma_f32_32x32x16_bf16 v[64:79], v[172:175], v[60:63], v[64:79]\n"
    "buffer_load_dwordx4 v221, s[36:39], s26 offen offset:1024 lds\n"
    "v_exp_f32 v28, v28\n"
    "v_exp_f32 v29, v29\n"
    "v_exp_f32 v30, v30\n"
    "v_exp_f32 v31, v31\n"
    "v_cvt_pk_bf16_f32 v52, v24, v25\n"
    "v_cvt_pk_bf16_f32 v53, v26, v27\n"
    "v_cvt_pk_bf16_f32 v54, v28, v29\n"
    "v_cvt_pk_bf16_f32 v55, v30, v31\n"
    "v_mfma_f32_32x32x16_bf16 v[16:31], v[176:179], a[24:27], 0\n"
    "buffer_load_dwordx4 v[164:167], v0, s[12:15], s51 offen offset:1024 sc1\n"
    "v_exp_f32 v32, v32\n"
    "v_exp_f32 v33, v33\n"
    "v_exp_f32 v34, v34\n"
    "v_exp_f32 v35, v35\n"
    "v_mfma_f32_32x32x16_bf16 v[16:31], v[180:183], a[28:31], v[16:31]\n"
    "v_exp_f32 v36, v36\n"
    "v_exp_f32 v37, v37\n"
    "v_exp_f32 v38, v38\n"
    "v_exp_f32 v39, v39\n"
    "v_cvt_pk_bf16_f32 v56, v32, v33\n"
    "v_cvt_pk_bf16_f32 v57, v34, v35\n"
    "v_mfma_f32_32x32x16_bf16 v[80:95], v[184:187], v[48:51], v[80:95]\n"
    "v_exp_f32 v40, v40\n"
    "v_exp_f32 v41, v41\n"
    "v_exp_f32 v42, v42\n"
    "v_exp_f32 v43, v43\n"
    "v_cvt_pk_bf16_f32 v58, v36, v37\n"
    "v_cvt_pk_bf16_f32 v59, v38, v39\n"
    "v_mfma_f32_32x32x16_bf16 v[80:95], v[188:191], v[52:55], v[80:95]\n"
    "buffer_load_dwordx4 v222, s[36:39], s26 offen offset:2048 lds\n"
    "v_exp_f32 v44, v44\n"
    "v_exp_f32 v45, v45\n"
    "v_exp_f32 v46, v46\n"
    "v_exp_f32 v47, v47\n"
    "v_cvt_pk_bf16_f32 v60, v40, v41\n"
    "v_cvt_pk_bf16_f32 v61, v42, v43\n"
    "v_cvt_pk_bf16_f32 v62, v44, v45\n"
    "v_cvt_pk_bf16_f32 v63, v46, v47\n"
    "v_mfma_f32_32x32x16_bf16 v[32:47], v[176:179], a[0:3], 0\n"
    "buffer_load_dwordx4 v[168:171], v0, s[12:15], s51 offen offset:2048 sc1\n"
    "v_exp_f32 v16, v16\n"
    "v_exp_f32 v17, v17\n"
    "v_exp_f32 v18, v18\n"
    "v_exp_f32 v19, v19\n"
    "v_mfma_f32_32x32x16_bf16 v[32:47], v[180:183], a[4:7], v[32:47]\n"
    "v_max_u32 v248, v248, v182\n"
    "s_waitcnt vmcnt(13)\n"
    "ds_read_b64_tr_b16 v[192:193], v232 offset:0\n"
    "v_exp_f32 v20, v20\n"
    "v_exp_f32 v21, v21\n"
    "v_exp_f32 v22, v22\n"
    "v_exp_f32 v23, v23\n"
    "v_cvt_pk_bf16_f32 v48, v16, v17\n"
    "v_cvt_pk_bf16_f32 v49, v18, v19\n"
    "v_mfma_f32_32x32x16_bf16 v[96:111], v[184:187], v[56:59], v[96:111]\n"
    "buffer_load_dwordx4 v223, s[36:39], s26 offen offset:3072 lds\n"
    "v_exp_f32 v24, v24\n"
    "v_exp_f32 v25, v25\n"
    "v_exp_f32 v26, v26\n"
    "v_exp_f32 v27, v27\n"
    "v_cvt_pk_bf16_f32 v50, v20, v21\n"
    "v_cvt_pk_bf16_f32 v51, v22, v23\n"
    "v_mfma_f32_32x32x16_bf16 v[96:111], v[188:191], v[60:63], v[96:111]\n"
    "s_waitcnt vmcnt(11)\n"
    "ds_read_b64_tr_b16 v[194:195], v232 offset:1024\n"
    "v_exp_f32 v28, v28\n"
    "v_exp_f32 v29, v29\n"
    "v_exp_f32 v30, v30\n"
    "v_exp_f32 v31, v31\n"
    "v_cvt_pk_bf16_f32 v52, v24, v25\n"
    "v_cvt_pk_bf16_f32 v53, v26, v27\n"
    "v_cvt_pk_bf16_f32 v54, v28, v29\n"
    "v_cvt_pk_bf16_f32 v55, v30, v31\n"
    "v_mfma_f32_32x32x16_bf16 v[16:31], v[128:131], a[8:11], 0\n"
    "buffer_load_dwordx4 v[172:175], v0, s[12:15], s51 offen offset:3072 sc1\n"
    "v_exp_f32 v32, v32\n"
    "v_exp_f32 v33, v33\n"
    "v_exp_f32 v34, v34\n"
    "v_exp_f32 v35, v35\n"
    "v_mfma_f32_32x32x16_bf16 v[16:31], v[132:135], a[12:15], v[16:31]\n"
    "ds_read_b64_tr_b16 v[196:197], v233 offset:0\n"
    "v_exp_f32 v36, v36\n"
    "v_exp_f32 v37, v37\n"
    "v_exp_f32 v38, v38\n"
    "v_exp_f32 v39, v39\n"
    "v_cvt_pk_bf16_f32 v56, v32, v33\n"
    "v_cvt_pk_bf16_f32 v57, v34, v35\n"
    "v_mfma_f32_32x32x16_bf16 v[112:127], v[184:187], v[48:51], v[112:127]\n"
    "s_add_u32 s51, s51, 0x4000\n"
    "s_add_u32 m0, s41, 4096\n"
    "s_nop 0\n"
    "buffer_load_dwordx4 v220, s[36:39], s27 offen offset:0 lds\n"
    "v_exp_f32 v40, v40\n"
    "v_exp_f32 v41, v41\n"
    "v_exp_f32 v42, v42\n"
    "v_exp_f32 v43, v43\n"
    "v_cvt_pk_bf16_f32 v58, v36, v37\n"
    "v_cvt_pk_bf16_f32 v59, v38, v39\n"
    "v_mfma_f32_32x32x16_bf16 v[112:127], v[188:191], v[52:55], v[112:127]\n"
    "s_and_b32 s51, s51, 0x3ffff\n"
    "ds_read_b64_tr_b16 v[198:199], v233 offset:1024\n"
    "v_exp_f32 v44, v44\n"
    "v_exp_f32 v45, v45\n"
    "v_exp_f32 v46, v46\n"
    "v_exp_f32 v47, v47\n"
    "v_cvt_pk_bf16_f32 v60, v40, v41\n"
    "v_cvt_pk_bf16_f32 v61, v42, v43\n"
    "v_cvt_pk_bf16_f32 v62, v44, v45\n"
    "v_cvt_pk_bf16_f32 v63, v46, v47\n"
    "v_mfma_f32_32x32x16_bf16 v[32:47], v[128:131], a[16:19], 0\n"
    "buffer_load_dwordx4 v[176:179], v0, s[12:15], s51 offen offset:0 sc1\n"
    "v_exp_f32 v16, v16\n"
    "v_exp_f32 v17, v17\n"
    "v_exp_f32 v18, v18\n"
    "v_exp_f32 v19, v19\n"
    "v_mfma_f32_32x32x16_bf16 v[32:47], v[132:135], a[20:23], v[32:47]\n"
    "ds_read_b64_tr_b16 v[200:201], v234 offset:0\n"
    "v_exp_f32 v20, v20\n"
    "v_exp_f32 v21, v21\n"
    "v_exp_f32 v22, v22\n"
    "v_exp_f32 v23, v23\n"
    "v_cvt_pk_bf16_f32 v48, v16, v17\n"
    "v_cvt_pk_bf16_f32 v49, v18, v19\n"
    "v_mfma_f32_32x32x16_bf16 v[64:79], v[184:187], v[56:59], v[64:79]\n"
    "buffer_load_dwordx4 v221, s[36:39], s27 offen offset:1024 lds\n"
    "v_exp_f32 v24, v24\n"
    "v_exp_f32 v25, v25\n"
    "v_exp_f32 v26, v26\n"
    "v_exp_f32 v27, v27\n"
    "v_cvt_pk_bf16_f32 v50, v20, v21\n"
    "v_cvt_pk_bf16_f32 v51, v22, v23\n"
    "v_mfma_f32_32x32x16_bf16 v[64:79], v[188:191], v[60:63], v[64:79]\n"
    "ds_read_b64_tr_b16 v[202:203], v234 offset:1024\n"
    "v_exp_f32 v28, v28\n"
    "v_exp_f32 v29, v29\n"
    "v_exp_f32 v30, v30\n"
    "v_exp_f32 v31, v31\n"
    "v_cvt_pk_bf16_f32 v52, v24, v25\n"
    "v_cvt_pk_bf16_f32 v53, v26, v27\n"
    "v_cvt_pk_bf16_f32 v54, v28, v29\n"
    "v_cvt_pk_bf16_f32 v55, v30, v31\n"
    "v_mfma_f32_32x32x16_bf16 v[16:31], v[128:131], a[24:27], 0\n"
    "buffer_load_dwordx4 v[180:183], v0, s[12:15], s51 offen offset:1024 sc1\n"
    "v_exp_f32 v32, v32\n"
    "v_exp_f32 v33, v33\n"
    "v_exp_f32 v34, v34\n"
    "v_exp_f32 v35, v35\n"
    "v_mfma_f32_32x32x16_bf16 v[16:31], v[132:135], a[28:31], v[16:31]\n"
    "buffer_load_dwordx4 v222, s[36:39], s27 offen offset:2048 lds\n"
    "v_exp_f32 v36, v36\n"
    "v_exp_f32 v37, v37\n"
    "v_exp_f32 v38, v38\n"
    "v_exp_f32 v39, v39\n"
    "v_cvt_pk_bf16_f32 v56, v32, v33\n"
    "v_cvt_pk_bf16_f32 v57, v34, v35\n"
    "v_mfma_f32_32x32x16_bf16 v[80:95], v[136:139], v[48:51], v[80:95]\n"
    "v_exp_f32 v40, v40\n"
    "v_exp_f32 v41, v41\n"
    "v_exp_f32 v42, v42\n"
    "v_exp_f32 v43, v43\n"
    "v_cvt_pk_bf16_f32 v58, v36, v37\n"
    "v_cvt_pk_bf16_f32 v59, v38, v39\n"
    "v_mfma_f32_32x32x16_bf16 v[80:95], v[140:143], v[52:55], v[80:95]\n"
    "ds_read_b64_tr_b16 v[204:205], v235 offset:0\n"
    "v_exp_f32 v44, v44\n"
    "v_exp_f32 v45, v45\n"
    "v_exp_f32 v46, v46\n"
    "v_exp_f32 v47, v47\n"
    "v_cvt_pk_bf16_f32 v60, v40, v41\n"
    "v_cvt_pk_bf16_f32 v61, v42, v43\n"
    "v_cvt_pk_bf16_f32 v62, v44, v45\n"
    "v_cvt_pk_bf16_f32 v63, v46, v47\n"
    "v_mfma_f32_32x32x16_bf16 v[32:47], v[128:131], a[0:3], 0\n"
    "buffer_load_dwordx4 v[184:187], v0, s[12:15], s51 offen offset:2048 sc1\n"
    "v_exp_f32 v16, v16\n"
    "v_exp_f32 v17, v17\n"
    "v_exp_f32 v18, v18\n"
    "v_exp_f32 v19, v19\n"
    "v_mfma_f32_32x32x16_bf16 v[32:47], v[132:135], a[4:7], v[32:47]\n"
    "v_max_u32 v248, v248, v134\n"
    "buffer_load_dwordx4 v223, s[36:39], s27 offen offset:3072 lds\n"
    "v_exp_f32 v20, v20\n"
    "v_exp_f32 v21, v21\n"
    "v_exp_f32 v22, v22\n"
    "v_exp_f32 v23, v23\n"
    "v_cvt_pk_bf16_f32 v48, v16, v17\n"
    "v_cvt_pk_bf16_f32 v49, v18, v19\n"
    "v_mfma_f32_32x32x16_bf16 v[96:111], v[136:139], v[56:59], v[96:111]\n"
    "ds_read_b64_tr_b16 v[206:207], v235 offset:1024\n"
    "v_exp_f32 v24, v24\n"
    "v_exp_f32 v25, v25\n"
    "v_exp_f32 v26, v26\n"
    "v_exp_f32 v27, v27\n"
    "v_cvt_pk_bf16_f32 v50, v20, v21\n"
    "v_cvt_pk_bf16_f32 v51, v22, v23\n"
    "v_mfma_f32_32x32x16_bf16 v[96:111], v[140:143], v[60:63], v[96:111]\n"
    "s_waitcnt vmcnt(15)\n"
    "ds_read_b64_tr_b16 v[208:209], v236 offset:0\n"
    "v_exp_f32 v28, v28\n"
    "v_exp_f32 v29, v29\n"
    "v_exp_f32 v30, v30\n"
    "v_exp_f32 v31, v31\n"
    "v_cvt_pk_bf16_f32 v52, v24, v25\n"
    "v_cvt_pk_bf16_f32 v53, v26, v27\n"
    "v_cvt_pk_bf16_f32 v54, v28, v29\n"
    "v_cvt_pk_bf16_f32 v55, v30, v31\n"
    "v_mfma_f32_32x32x16_bf16 v[16:31], v[144:147], a[8:11], 0\n"
    "buffer_load_dwordx4 v[188:191], v0, s[12:15], s51 offen offset:3072 sc1\n"
    "v_exp_f32 v32, v32\n"
    "v_exp_f32 v33, v33\n"
    "v_exp_f32 v34, v34\n"
    "v_exp_f32 v35, v35\n"
    "v_mfma_f32_32x32x16_bf16 v[16:31], v[148:151], a[12:15], v[16:31]\n"
    "ds_read_b64_tr_b16 v[210:211], v236 offset:1024\n"
    "v_exp_f32 v36, v36\n"
    "v_exp_f32 v37, v37\n"
    "v_exp_f32 v38, v38\n"
    "v_exp_f32 v39, v39\n"
    "v_cvt_pk_bf16_f32 v56, v32, v33\n"
    "v_cvt_pk_bf16_f32 v57, v34, v35\n"
    "v_mfma_f32_32x32x16_bf16 v[112:127], v[136:139], v[48:51], v[112:127]\n"
    "s_add_u32 s51, s51, 0x4000\n"
    "ds_read_b64_tr_b16 v[212:213], v237 offset:0\n"
    "v_exp_f32 v40, v40\n"
    "v_exp_f32 v41, v41\n"
    "v_exp_f32 v42, v42\n"
    "v_exp_f32 v43, v43\n"
    "v_cvt_pk_bf16_f32 v58, v36, v37\n"
    "v_cvt_pk_bf16_f32 v59, v38, v39\n"
    "v_mfma_f32_32x32x16_bf16 v[112:127], v[140:143], v[52:55], v[112:127]\n"
    "s_and_b32 s51, s51, 0x3ffff\n"
    "s_waitcnt lgkmcnt(9)\n"
    "buffer_store_dwordx4 v[192:195], v0, s[4:7], s24 offen offset:0 sc1\n"
    "v_exp_f32 v44, v44\n"
    "v_exp_f32 v45, v45\n"
    "v_exp_f32 v46, v46\n"
    "v_exp_f32 v47, v47\n"
    "v_cvt_pk_bf16_f32 v60, v40, v41\n"
    "v_cvt_pk_bf16_f32 v61, v42, v43\n"
    "v_cvt_pk_bf16_f32 v62, v44, v45\n"
    "v_cvt_pk_bf16_f32 v63, v46, v47\n"
    "v_mfma_f32_32x32x16_bf16 v[32:47], v[144:147], a[16:19], 0\n"
    "buffer_load_dwordx4 v[128:131], v0, s[12:15], s51 offen offset:0 sc1\n"
    "v_exp_f32 v16, v16\n"
    "v_exp_f32 v17, v17\n"
    "v_exp_f32 v18, v18\n"
    "v_exp_f32 v19, v19\n"
    "v_mfma_f32_32x32x16_bf16 v[32:47], v[148:151], a[20:23], v[32:47]\n"
    "ds_read_b64_tr_b16 v[214:215], v237 offset:1024\n"
    "v_exp_f32 v20, v20\n"
    "v_exp_f32 v21, v21\n"
    "v_exp_f32 v22, v22\n"
    "v_exp_f32 v23, v23\n"
    "v_cvt_pk_bf16_f32 v48, v16, v17\n"
    "v_cvt_pk_bf16_f32 v49, v18, v19\n"
    "v_mfma_f32_32x32x16_bf16 v[64:79], v[136:139], v[56:59], v[64:79]\n"
    "ds_read_b64_tr_b16 v[216:217], v238 offset:0\n"
    "v_exp_f32 v24, v24\n"
    "v_exp_f32 v25, v25\n"
    "v_exp_f32 v26, v26\n"
    "v_exp_f32 v27, v27\n"
    "v_cvt_pk_bf16_f32 v50, v20, v21\n"
    "v_cvt_pk_bf16_f32 v51, v22, v23\n"
    "v_mfma_f32_32x32x16_bf16 v[64:79], v[140:143], v[60:63], v[64:79]\n"
    "ds_read_b64_tr_b16 v[218:219], v238 offset:1024\n"
    "v_exp_f32 v28, v28\n"
    "v_exp_f32 v29, v29\n"
    "v_exp_f32 v30, v30\n"
    "v_exp_f32 v31, v31\n"
    "v_cvt_pk_bf16_f32 v52, v24, v25\n"
    "v_cvt_pk_bf16_f32 v53, v26, v27\n"
    "v_cvt_pk_bf16_f32 v54, v28, v29\n"
    "v_cvt_pk_bf16_f32 v55, v30, v31\n"
    "v_mfma_f32_32x32x16_bf16 v[16:31], v[144:147], a[24:27], 0\n"
    "buffer_load_dwordx4 v[132:135], v0, s[12:15], s51 offen offset:1024 sc1\n"
    "v_exp_f32 v32, v32\n"
    "v_exp_f32 v33, v33\n"
    "v_exp_f32 v34, v34\n"
    "v_exp_f32 v35, v35\n"
    "v_mfma_f32_32x32x16_bf16 v[16:31], v[148:151], a[28:31], v[16:31]\n"
    "ds_read_b64_tr_b16 v[224:225], v239 offset:0\n"
    "v_exp_f32 v36, v36\n"
    "v_exp_f32 v37, v37\n"
    "v_exp_f32 v38, v38\n"
    "v_exp_f32 v39, v39\n"
    "v_cvt_pk_bf16_f32 v56, v32, v33\n"
    "v_cvt_pk_bf16_f32 v57, v34, v35\n"
    "v_mfma_f32_32x32x16_bf16 v[80:95], v[152:155], v[48:51], v[80:95]\n"
    "ds_read_b64_tr_b16 v[226:227], v239 offset:1024\n"
    "v_exp_f32 v40, v40\n"
    "v_exp_f32 v41, v41\n"
    "v_exp_f32 v42, v42\n"
    "v_exp_f32 v43, v43\n"
    "v_cvt_pk_bf16_f32 v58, v36, v37\n"
    "v_cvt_pk_bf16_f32 v59, v38, v39\n"
    "v_mfma_f32_32x32x16_bf16 v[80:95], v[156:159], v[52:55], v[80:95]\n"
    "s_waitcnt lgkmcnt(12)\n"
    "buffer_store_dwordx4 v[196:199], v0, s[4:7], s24 offen offset:1024 sc1\n"
    "s_add_u32 s24, s24, s25\n"
    "v_exp_f32 v44, v44\n"
    "v_exp_f32 v45, v45\n"
    "v_exp_f32 v46, v46\n"
    "v_exp_f32 v47, v47\n"
    "v_cvt_pk_bf16_f32 v60, v40, v41\n"
    "v_cvt_pk_bf16_f32 v61, v42, v43\n"
    "v_cvt_pk_bf16_f32 v62, v44, v45\n"
    "v_cvt_pk_bf16_f32 v63, v46, v47\n"
    "v_mfma_f32_32x32x16_bf16 v[32:47], v[144:147], a[0:3], 0\n"
    "buffer_load_dwordx4 v[136:139], v0, s[12:15], s51 offen offset:2048 sc1\n"
    "v_exp_f32 v16, v16\n"
    "v_exp_f32 v17, v17\n"
    "v_exp_f32 v18, v18\n"
    "v_exp_f32 v19, v19\n"
    "v_mfma_f32_32x32x16_bf16 v[32:47], v[148:151], a[4:7], v[32:47]\n"
    "v_max_u32 v248, v248, v150\n"
    "s_waitcnt lgkmcnt(10)\n"
    "buffer_store_dwordx4 v[200:203], v0, s[4:7], s24 offen offset:0 sc1\n"
    "v_exp_f32 v20, v20\n"
    "v_exp_f32 v21, v21\n"
    "v_exp_f32 v22, v22\n"
    "v_exp_f32 v23, v23\n"
    "v_cvt_pk_bf16_f32 v48, v16, v17\n"
    "v_cvt_pk_bf16_f32 v49, v18, v19\n"
    "v_mfma_f32_32x32x16_bf16 v[96:111], v[152:155], v[56:59], v[96:111]\n"
    "v_exp_f32 v24, v24\n"
    "v_exp_f32 v25, v25\n"
    "v_exp_f32 v26, v26\n"
    "v_exp_f32 v27, v27\n"
    "v_cvt_pk_bf16_f32 v50, v20, v21\n"
    "v_cvt_pk_bf16_f32 v51, v22, v23\n"
    "v_mfma_f32_32x32x16_bf16 v[96:111], v[156:159], v[60:63], v[96:111]\n"
    "s_waitcnt vmcnt(14)\n"
    "s_waitcnt lgkmcnt(8)\n"
    "buffer_store_dwordx4 v[204:207], v0, s[4:7], s24 offen offset:1024 sc1\n"
    "s_add_u32 s24, s24, s25\n"
    "v_exp_f32 v28, v28\n"
    "v_exp_f32 v29, v29\n"
    "v_exp_f32 v30, v30\n"
    "v_exp_f32 v31, v31\n"
    "v_cvt_pk_bf16_f32 v52, v24, v25\n"
    "v_cvt_pk_bf16_f32 v53, v26, v27\n"
    "v_cvt_pk_bf16_f32 v54, v28, v29\n"
    "v_cvt_pk_bf16_f32 v55, v30, v31\n"
    "v_mfma_f32_32x32x16_bf16 v[16:31], v[160:163], a[8:11], 0\n"
    "buffer_load_dwordx4 v[140:143], v0, s[12:15], s51 offen offset:3072 sc1\n"
    "v_exp_f32 v32, v32\n"
    "v_exp_f32 v33, v33\n"
    "v_exp_f32 v34, v34\n"
    "v_exp_f32 v35, v35\n"
    "v_mfma_f32_32x32x16_bf16 v[16:31], v[164:167], a[12:15], v[16:31]\n"
    "s_waitcnt lgkmcnt(6)\n"
    "buffer_store_dwordx4 v[208:211], v0, s[4:7], s24 offen offset:0 sc1\n"
    "v_exp_f32 v36, v36\n"
    "v_exp_f32 v37, v37\n"
    "v_exp_f32 v38, v38\n"
    "v_exp_f32 v39, v39\n"
    "v_cvt_pk_bf16_f32 v56, v32, v33\n"
    "v_cvt_pk_bf16_f32 v57, v34, v35\n"
    "v_mfma_f32_32x32x16_bf16 v[112:127], v[152:155], v[48:51], v[112:127]\n"
    "s_add_u32 s51, s51, 0x4000\n"
    "s_waitcnt lgkmcnt(4)\n"
    "buffer_store_dwordx4 v[212:215], v0, s[4:7], s24 offen offset:1024 sc1\n"
    "s_add_u32 s24, s24, s25\n"
    "v_exp_f32 v40, v40\n"
    "v_exp_f32 v41, v41\n"
    "v_exp_f32 v42, v42\n"
    "v_exp_f32 v43, v43\n"
    "v_cvt_pk_bf16_f32 v58, v36, v37\n"
    "v_cvt_pk_bf16_f32 v59, v38, v39\n"
    "v_mfma_f32_32x32x16_bf16 v[112:127], v[156:159], v[52:55], v[112:127]\n"
    "s_and_b32 s51, s51, 0x3ffff\n"
    "v_exp_f32 v44, v44\n"
    "v_exp_f32 v45, v45\n"
    "v_exp_f32 v46, v46\n"
    "v_exp_f32 v47, v47\n"
    "v_cvt_pk_bf16_f32 v60, v40, v41\n"
    "v_cvt_pk_bf16_f32 v61, v42, v43\n"
    "v_cvt_pk_bf16_f32 v62, v44, v45\n"
    "v_cvt_pk_bf16_f32 v63, v46, v47\n"
    "v_mfma_f32_32x32x16_bf16 v[32:47], v[160:163], a[16:19], 0\n"
    "buffer_load_dwordx4 v[144:147], v0, s[12:15], s51 offen offset:0 sc1\n"
    "v_exp_f32 v16, v16\n"
    "v_exp_f32 v17, v17\n"
    "v_exp_f32 v18, v18\n"
    "v_exp_f32 v19, v19\n"
    "v_mfma_f32_32x32x16_bf16 v[32:47], v[164:167], a[20:23], v[32:47]\n"
    "s_waitcnt lgkmcnt(2)\n"
    "buffer_store_dwordx4 v[216:219], v0, s[4:7], s24 offen offset:0 sc1\n"
    "v_exp_f32 v20, v20\n"
    "v_exp_f32 v21, v21\n"
    "v_exp_f32 v22, v22\n"
    "v_exp_f32 v23, v23\n"
    "v_cvt_pk_bf16_f32 v48, v16, v17\n"
    "v_cvt_pk_bf16_f32 v49, v18, v19\n"
    "v_mfma_f32_32x32x16_bf16 v[64:79], v[152:155], v[56:59], v[64:79]\n"
    "s_waitcnt lgkmcnt(0)\n"
    "buffer_store_dwordx4 v[224:227], v0, s[4:7], s24 offen offset:1024 sc1\n"
    "s_add_u32 s24, s24, s25\n"
    "v_exp_f32 v24, v24\n"
    "v_exp_f32 v25, v25\n"
    "v_exp_f32 v26, v26\n"
    "v_exp_f32 v27, v27\n"
    "v_cvt_pk_bf16_f32 v50, v20, v21\n"
    "v_cvt_pk_bf16_f32 v51, v22, v23\n"
    "v_mfma_f32_32x32x16_bf16 v[64:79], v[156:159], v[60:63], v[64:79]\n"
    "s_waitcnt vmcnt(14)\n"
    "ds_read_b64_tr_b16 a[32:33], v14 offset:0\n"
    "v_exp_f32 v28, v28\n"
    "v_exp_f32 v29, v29\n"
    "v_exp_f32 v30, v30\n"
    "v_exp_f32 v31, v31\n"
    "v_cvt_pk_bf16_f32 v52, v24, v25\n"
    "v_cvt_pk_bf16_f32 v53, v26, v27\n"
    "v_cvt_pk_bf16_f32 v54, v28, v29\n"
    "v_cvt_pk_bf16_f32 v55, v30, v31\n"
    "v_mfma_f32_32x32x16_bf16 v[16:31], v[160:163], a[24:27], 0\n"
    "buffer_load_dwordx4 v[148:151], v0, s[12:15], s51 offen offset:1024 sc1\n"
    "v_exp_f32 v32, v32\n"
    "v_exp_f32 v33, v33\n"
    "v_exp_f32 v34, v34\n"
    "v_exp_f32 v35, v35\n"
    "v_mfma_f32_32x32x16_bf16 v[16:31], v[164:167], a[28:31], v[16:31]\n"
    "ds_read_b64_tr_b16 a[34:35], v14 offset:1024\n"
    "v_exp_f32 v36, v36\n"
    "v_exp_f32 v37, v37\n"
    "v_exp_f32 v38, v38\n"
    "v_exp_f32 v39, v39\n"
    "v_cvt_pk_bf16_f32 v56, v32, v33\n"
    "v_cvt_pk_bf16_f32 v57, v34, v35\n"
    "v_mfma_f32_32x32x16_bf16 v[80:95], v[168:171], v[48:51], v[80:95]\n"
    "ds_read_b64_tr_b16 a[36:37], v15 offset:0\n"
    "v_exp_f32 v40, v40\n"
    "v_exp_f32 v41, v41\n"
    "v_exp_f32 v42, v42\n"
    "v_exp_f32 v43, v43\n"
    "v_cvt_pk_bf16_f32 v58, v36, v37\n"
    "v_cvt_pk_bf16_f32 v59, v38, v39\n"
    "v_mfma_f32_32x32x16_bf16 v[80:95], v[172:175], v[52:55], v[80:95]\n"
    "ds_read_b64_tr_b16 a[38:39], v15 offset:1024\n"
    "v_exp_f32 v44, v44\n"
    "v_exp_f32 v45, v45\n"
    "v_exp_f32 v46, v46\n"
    "v_exp_f32 v47, v47\n"
    "v_cvt_pk_bf16_f32 v60, v40, v41\n"
    "v_cvt_pk_bf16_f32 v61, v42, v43\n"
    "v_cvt_pk_bf16_f32 v62, v44, v45\n"
    "v_cvt_pk_bf16_f32 v63, v46, v47\n"
    "v_mfma_f32_32x32x16_bf16 v[32:47], v[160:163], a[0:3], 0\n"
    "buffer_load_dwordx4 v[152:155], v0, s[12:15], s51 offen offset:2048 sc1\n"
    "v_exp_f32 v16, v16\n"
    "v_exp_f32 v17, v17\n"
    "v_exp_f32 v18, v18\n"
    "v_exp_f32 v19, v19\n"
    "v_mfma_f32_32x32x16_bf16 v[32:47], v[164:167], a[4:7], v[32:47]\n"
    "v_max_u32 v248, v248, v166\n"
    "ds_read_b64_tr_b16 a[40:41], v14 offset:2048\n"
    "v_exp_f32 v20, v20\n"
    "v_exp_f32 v21, v21\n"
    "v_exp_f32 v22, v22\n"
    "v_exp_f32 v23, v23\n"
    "v_cvt_pk_bf16_f32 v48, v16, v17\n"
    "v_cvt_pk_bf16_f32 v49, v18, v19\n"
    "v_mfma_f32_32x32x16_bf16 v[96:111], v[168:171], v[56:59], v[96:111]\n"
    "ds_read_b64_tr_b16 a[42:43], v14 offset:3072\n"
    "v_exp_f32 v24, v24\n"
    "v_exp_f32 v25, v25\n"
    "v_exp_f32 v26, v26\n"
    "v_exp_f32 v27, v27\n"
    "v_cvt_pk_bf16_f32 v50, v20, v21\n"
    "v_cvt_pk_bf16_f32 v51, v22, v23\n"
    "v_mfma_f32_32x32x16_bf16 v[96:111], v[172:175], v[60:63], v[96:111]\n"
    "s_waitcnt vmcnt(15)\n"
    "v_exp_f32 v28, v28\n"
    "v_exp_f32 v29, v29\n"
    "v_exp_f32 v30, v30\n"
    "v_exp_f32 v31, v31\n"
    "v_cvt_pk_bf16_f32 v52, v24, v25\n"
    "v_cvt_pk_bf16_f32 v53, v26, v27\n"
    "v_cvt_pk_bf16_f32 v54, v28, v29\n"
    "v_cvt_pk_bf16_f32 v55, v30, v31\n"
    "v_mfma_f32_32x32x16_bf16 v[16:31], v[176:179], a[8:11], 0\n"
    "buffer_load_dwordx4 v[156:159], v0, s[12:15], s51 offen offset:3072 sc1\n"
    "v_exp_f32 v32, v32\n"
    "v_exp_f32 v33, v33\n"
    "v_exp_f32 v34, v34\n"
    "v_exp_f32 v35, v35\n"
    "v_mfma_f32_32x32x16_bf16 v[16:31], v[180:183], a[12:15], v[16:31]\n"
    "ds_read_b64_tr_b16 a[44:45], v15 offset:2048\n"
    "v_exp_f32 v36, v36\n"
    "v_exp_f32 v37, v37\n"
    "v_exp_f32 v38, v38\n"
    "v_exp_f32 v39, v39\n"
    "v_cvt_pk_bf16_f32 v56, v32, v33\n"
    "v_cvt_pk_bf16_f32 v57, v34, v35\n"
    "v_mfma_f32_32x32x16_bf16 v[112:127], v[168:171], v[48:51], v[112:127]\n"
    "s_add_u32 s51, s51, 0x4000\n"
    "ds_read_b64_tr_b16 a[46:47], v15 offset:3072\n"
    "v_exp_f32 v40, v40\n"
    "v_exp_f32 v41, v41\n"
    "v_exp_f32 v42, v42\n"
    "v_exp_f32 v43, v43\n"
    "v_cvt_pk_bf16_f32 v58, v36, v37\n"
    "v_cvt_pk_bf16_f32 v59, v38, v39\n"
    "v_mfma_f32_32x32x16_bf16 v[112:127], v[172:175], v[52:55], v[112:127]\n"
    "s_and_b32 s51, s51, 0x3ffff\n"
    "ds_read_b64_tr_b16 a[48:49], v14 offset:4096\n"
    "v_exp_f32 v44, v44\n"
    "v_exp_f32 v45, v45\n"
    "v_exp_f32 v46, v46\n"
    "v_exp_f32 v47, v47\n"
    "v_cvt_pk_bf16_f32 v60, v40, v41\n"
    "v_cvt_pk_bf16_f32 v61, v42, v43\n"
    "v_cvt_pk_bf16_f32 v62, v44, v45\n"
    "v_cvt_pk_bf16_f32 v63, v46, v47\n"
    "v_mfma_f32_32x32x16_bf16 v[32:47], v[176:179], a[16:19], 0\n"
    "buffer_load_dwordx4 v[160:163], v0, s[12:15], s51 offen offset:0 sc1\n"
    "v_exp_f32 v16, v16\n"
    "v_exp_f32 v17, v17\n"
    "v_exp_f32 v18, v18\n"
    "v_exp_f32 v19, v19\n"
    "v_mfma_f32_32x32x16_bf16 v[32:47], v[180:183], a[20:23], v[32:47]\n"
    "ds_read_b64_tr_b16 a[50:51], v14 offset:5120\n"
    "v_exp_f32 v20, v20\n"
    "v_exp_f32 v21, v21\n"
    "v_exp_f32 v22, v22\n"
    "v_exp_f32 v23, v23\n"
    "v_cvt_pk_bf16_f32 v48, v16, v17\n"
    "v_cvt_pk_bf16_f32 v49, v18, v19\n"
    "v_mfma_f32_32x32x16_bf16 v[64:79], v[168:171], v[56:59], v[64:79]\n"
    "ds_read_b64_tr_b16 a[52:53], v15 offset:4096\n"
    "v_exp_f32 v24, v24\n"
    "v_exp_f32 v25, v25\n"
    "v_exp_f32 v26, v26\n"
    "v_exp_f32 v27, v27\n"
    "v_cvt_pk_bf16_f32 v50, v20, v21\n"
    "v_cvt_pk_bf16_f32 v51, v22, v23\n"
    "v_mfma_f32_32x32x16_bf16 v[64:79], v[172:175], v[60:63], v[64:79]\n"
    "s_waitcnt vmcnt(4)\n"
    "s_mov_b64 exec, 1\n"
    "buffer_atomic_swap_x2 v[254:255], off, s[4:7], s3 sc1\n"
    "s_mov_b64 exec, -1\n"
    "v_exp_f32 v28, v28\n"
    "v_exp_f32 v29, v29\n"
    "v_exp_f32 v30, v30\n"
    "v_exp_f32 v31, v31\n"
    "v_cvt_pk_bf16_f32 v52, v24, v25\n"
    "v_cvt_pk_bf16_f32 v53, v26, v27\n"
    "v_cvt_pk_bf16_f32 v54, v28, v29\n"
    "v_cvt_pk_bf16_f32 v55, v30, v31\n"
    "v_mfma_f32_32x32x16_bf16 v[16:31], v[176:179], a[24:27], 0\n"
    "buffer_load_dwordx4 v[164:167], v0, s[12:15], s51 offen offset:1024 sc1\n"
    "v_exp_f32 v32, v32\n"
    "v_exp_f32 v33, v33\n"
    "v_exp_f32 v34, v34\n"
    "v_exp_f32 v35, v35\n"
    "v_mfma_f32_32x32x16_bf16 v[16:31], v[180:183], a[28:31], v[16:31]\n"
    "ds_read_b64_tr_b16 a[54:55], v15 offset:5120\n"
    "v_exp_f32 v36, v36\n"
    "v_exp_f32 v37, v37\n"
    "v_exp_f32 v38, v38\n"
    "v_exp_f32 v39, v39\n"
    "v_cvt_pk_bf16_f32 v56, v32, v33\n"
    "v_cvt_pk_bf16_f32 v57, v34, v35\n"
    "v_mfma_f32_32x32x16_bf16 v[80:95], v[184:187], v[48:51], v[80:95]\n"
    "ds_read_b64_tr_b16 a[56:57], v14 offset:6144\n"
    "v_exp_f32 v40, v40\n"
    "v_exp_f32 v41, v41\n"
    "v_exp_f32 v42, v42\n"
    "v_exp_f32 v43, v43\n"
    "v_cvt_pk_bf16_f32 v58, v36, v37\n"
    "v_cvt_pk_bf16_f32 v59, v38, v39\n"
    "v_mfma_f32_32x32x16_bf16 v[80:95], v[188:191], v[52:55], v[80:95]\n"
    "ds_read_b64_tr_b16 a[58:59], v14 offset:7168\n"
    "v_exp_f32 v44, v44\n"
    "v_exp_f32 v45, v45\n"
    "v_exp_f32 v46, v46\n"
    "v_exp_f32 v47, v47\n"
    "v_cvt_pk_bf16_f32 v60, v40, v41\n"
    "v_cvt_pk_bf16_f32 v61, v42, v43\n"
    "v_cvt_pk_bf16_f32 v62, v44, v45\n"
    "v_cvt_pk_bf16_f32 v63, v46, v47\n"
    "v_mfma_f32_32x32x16_bf16 v[32:47], v[176:179], a[0:3], 0\n"
    "buffer_load_dwordx4 v[168:171], v0, s[12:15], s51 offen offset:2048 sc1\n"
    "v_exp_f32 v16, v16\n"
    "v_exp_f32 v17, v17\n"
    "v_exp_f32 v18, v18\n"
    "v_exp_f32 v19, v19\n"
    "v_mfma_f32_32x32x16_bf16 v[32:47], v[180:183], a[4:7], v[32:47]\n"
    "v_max_u32 v248, v248, v182\n"
    "ds_read_b64_tr_b16 a[60:61], v15 offset:6144\n"
    "v_exp_f32 v20, v20\n"
    "v_exp_f32 v21, v21\n"
    "v_exp_f32 v22, v22\n"
    "v_exp_f32 v23, v23\n"
    "v_cvt_pk_bf16_f32 v48, v16, v17\n"
    "v_cvt_pk_bf16_f32 v49, v18, v19\n"
    "v_mfma_f32_32x32x16_bf16 v[96:111], v[184:187], v[56:59], v[96:111]\n"
    "ds_read_b64_tr_b16 a[62:63], v15 offset:7168\n"
    "v_exp_f32 v24, v24\n"
    "v_exp_f32 v25, v25\n"
    "v_exp_f32 v26, v26\n"
    "v_exp_f32 v27, v27\n"
    "v_cvt_pk_bf16_f32 v50, v20, v21\n"
    "v_cvt_pk_bf16_f32 v51, v22, v23\n"
    "v_mfma_f32_32x32x16_bf16 v[96:111], v[188:191], v[60:63], v[96:111]\n"
    "v_exp_f32 v28, v28\n"
    "v_exp_f32 v29, v29\n"
    "v_exp_f32 v30, v30\n"
    "v_exp_f32 v31, v31\n"
    "v_cvt_pk_bf16_f32 v52, v24, v25\n"
    "v_cvt_pk_bf16_f32 v53, v26, v27\n"
    "v_cvt_pk_bf16_f32 v54, v28, v29\n"
    "v_cvt_pk_bf16_f32 v55, v30, v31\n"
    "v_mfma_f32_32x32x16_bf16 v[16:31], v[128:131], a[8:11], 0\n"
    "buffer_load_dwordx4 v[172:175], v0, s[12:15], s51 offen offset:3072 sc1\n"
    "v_exp_f32 v32, v32\n"
    "v_exp_f32 v33, v33\n"
    "v_exp_f32 v34, v34\n"
    "v_exp_f32 v35, v35\n"
    "v_mfma_f32_32x32x16_bf16 v[16:31], v[132:135], a[12:15], v[16:31]\n"
    "s_waitcnt lgkmcnt(0)\n"
    "s_mov_b32 m0, s41\n"
    "s_nop 0\n"
    "buffer_load_dwordx4 v220, s[36:39], s28 offen offset:0 lds\n"
    "v_exp_f32 v36, v36\n"
    "v_exp_f32 v37, v37\n"
    "v_exp_f32 v38, v38\n"
    "v_exp_f32 v39, v39\n"
    "v_cvt_pk_bf16_f32 v56, v32, v33\n"
    "v_cvt_pk_bf16_f32 v57, v34, v35\n"
    "v_mfma_f32_32x32x16_bf16 v[112:127], v[184:187], v[48:51], v[112:127]\n"
    "s_add_u32 s51, s51, 0x4000\n"
    "buffer_load_dwordx4 v221, s[36:39], s28 offen offset:1024 lds\n"
    "v_exp_f32 v40, v40\n"
    "v_exp_f32 v41, v41\n"
    "v_exp_f32 v42, v42\n"
    "v_exp_f32 v43, v43\n"
    "v_cvt_pk_bf16_f32 v58, v36, v37\n"
    "v_cvt_pk_bf16_f32 v59, v38, v39\n"
    "v_mfma_f32_32x32x16_bf16 v[112:127], v[188:191], v[52:55], v[112:127]\n"
    "s_and_b32 s51, s51, 0x3ffff\n"
    "v_exp_f32 v44, v44\n"
    "v_exp_f32 v45, v45\n"
    "v_exp_f32 v46, v46\n"
    "v_exp_f32 v47, v47\n"
    "v_cvt_pk_bf16_f32 v60, v40, v41\n"
    "v_cvt_pk_bf16_f32 v61, v42, v43\n"
    "v_cvt_pk_bf16_f32 v62, v44, v45\n"
    "v_cvt_pk_bf16_f32 v63, v46, v47\n"
    "v_mfma_f32_32x32x16_bf16 v[32:47], v[128:131], a[16:19], 0\n"
    "buffer_load_dwordx4 v[176:179], v0, s[12:15], s51 offen offset:0 sc1\n"
    "v_exp_f32 v16, v16\n"
    "v_exp_f32 v17, v17\n"
    "v_exp_f32 v18, v18\n"
    "v_exp_f32 v19, v19\n"
    "v_mfma_f32_32x32x16_bf16 v[32:47], v[132:135], a[20:23], v[32:47]\n"
    "buffer_load_dwordx4 v222, s[36:39], s28 offen offset:2048 lds\n"
    "v_exp_f32 v20, v20\n"
    "v_exp_f32 v21, v21\n"
    "v_exp_f32 v22, v22\n"
    "v_exp_f32 v23, v23\n"
    "v_cvt_pk_bf16_f32 v48, v16, v17\n"
    "v_cvt_pk_bf16_f32 v49, v18, v19\n"
    "v_mfma_f32_32x32x16_bf16 v[64:79], v[184:187], v[56:59], v[64:79]\n"
    "v_exp_f32 v24, v24\n"
    "v_exp_f32 v25, v25\n"
    "v_exp_f32 v26, v26\n"
    "v_exp_f32 v27, v27\n"
    "v_cvt_pk_bf16_f32 v50, v20, v21\n"
    "v_cvt_pk_bf16_f32 v51, v22, v23\n"
    "v_mfma_f32_32x32x16_bf16 v[64:79], v[188:191], v[60:63], v[64:79]\n"
    "buffer_load_dwordx4 v223, s[36:39], s28 offen offset:3072 lds\n"
    "v_exp_f32 v28, v28\n"
    "v_exp_f32 v29, v29\n"
    "v_exp_f32 v30, v30\n"
    "v_exp_f32 v31, v31\n"
    "v_cvt_pk_bf16_f32 v52, v24, v25\n"
    "v_cvt_pk_bf16_f32 v53, v26, v27\n"
    "v_cvt_pk_bf16_f32 v54, v28, v29\n"
    "v_cvt_pk_bf16_f32 v55, v30, v31\n"
    "v_mfma_f32_32x32x16_bf16 v[16:31], v[128:131], a[24:27], 0\n"
    "buffer_load_dwordx4 v[180:183], v0, s[12:15], s51 offen offset:1024 sc1\n"
    "v_exp_f32 v32, v32\n"
    "v_exp_f32 v33, v33\n"
    "v_exp_f32 v34, v34\n"
    "v_exp_f32 v35, v35\n"
    "v_mfma_f32_32x32x16_bf16 v[16:31], v[132:135], a[28:31], v[16:31]\n"
    "v_exp_f32 v36, v36\n"
    "v_exp_f32 v37, v37\n"
    "v_exp_f32 v38, v38\n"
    "v_exp_f32 v39, v39\n"
    "v_cvt_pk_bf16_f32 v56, v32, v33\n"
    "v_cvt_pk_bf16_f32 v57, v34, v35\n"
    "v_mfma_f32_32x32x16_bf16 v[80:95], v[136:139], v[48:51], v[80:95]\n"
    "s_add_u32 m0, s41, 4096\n"
    "s_nop 0\n"
    "buffer_load_dwordx4 v220, s[36:39], s29 offen offset:0 lds\n"
    "v_exp_f32 v40, v40\n"
    "v_exp_f32 v41, v41\n"
    "v_exp_f32 v42, v42\n"
    "v_exp_f32 v43, v43\n"
    "v_cvt_pk_bf16_f32 v58, v36, v37\n"
    "v_cvt_pk_bf16_f32 v59, v38, v39\n"
    "v_mfma_f32_32x32x16_bf16 v[80:95], v[140:143], v[52:55], v[80:95]\n"
    "buffer_load_dwordx4 v221, s[36:39], s29 offen offset:1024 lds\n"
    "v_exp_f32 v44, v44\n"
    "v_exp_f32 v45, v45\n"
    "v_exp_f32 v46, v46\n"
    "v_exp_f32 v47, v47\n"
    "v_cvt_pk_bf16_f32 v60, v40, v41\n"
    "v_cvt_pk_bf16_f32 v61, v42, v43\n"
    "v_cvt_pk_bf16_f32 v62, v44, v45\n"
    "v_cvt_pk_bf16_f32 v63, v46, v47\n"
    "v_mfma_f32_32x32x16_bf16 v[32:47], v[128:131], a[0:3], 0\n"
    "buffer_load_dwordx4 v[184:187], v0, s[12:15], s51 offen offset:2048 sc1\n"
    "v_exp_f32 v16, v16\n"
    "v_exp_f32 v17, v17\n"
    "v_exp_f32 v18, v18\n"
    "v_exp_f32 v19, v19\n"
    "v_mfma_f32_32x32x16_bf16 v[32:47], v[132:135], a[4:7], v[32:47]\n"
    "v_max_u32 v248, v248, v134\n"
    "v_exp_f32 v20, v20\n"
    "v_exp_f32 v21, v21\n"
    "v_exp_f32 v22, v22\n"
    "v_exp_f32 v23, v23\n"
    "v_cvt_pk_bf16_f32 v48, v16, v17\n"
    "v_cvt_pk_bf16_f32 v49, v18, v19\n"
    "v_mfma_f32_32x32x16_bf16 v[96:111], v[136:139], v[56:59], v[96:111]\n"
    "buffer_load_dwordx4 v222, s[36:39], s29 offen offset:2048 lds\n"
    "v_exp_f32 v24, v24\n"
    "v_exp_f32 v25, v25\n"
    "v_exp_f32 v26, v26\n"
    "v_exp_f32 v27, v27\n"
    "v_cvt_pk_bf16_f32 v50, v20, v21\n"
    "v_cvt_pk_bf16_f32 v51, v22, v23\n"
    "v_mfma_f32_32x32x16_bf16 v[96:111], v[140:143], v[60:63], v[96:111]\n"
    "s_waitcnt vmcnt(15)\n"
    "v_exp_f32 v28, v28\n"
    "v_exp_f32 v29, v29\n"
    "v_exp_f32 v30, v30\n"
    "v_exp_f32 v31, v31\n"
    "v_cvt_pk_bf16_f32 v52, v24, v25\n"
    "v_cvt_pk_bf16_f32 v53, v26, v27\n"
    "v_cvt_pk_bf16_f32 v54, v28, v29\n"
    "v_cvt_pk_bf16_f32 v55, v30, v31\n"
    "v_mfma_f32_32x32x16_bf16 v[16:31], v[144:147], a[8:11], 0\n"
    "buffer_load_dwordx4 v[188:191], v0, s[12:15], s51 offen offset:3072 sc1\n"
    "v_exp_f32 v32, v32\n"
    "v_exp_f32 v33, v33\n"
    "v_exp_f32 v34, v34\n"
    "v_exp_f32 v35, v35\n"
    "v_mfma_f32_32x32x16_bf16 v[16:31], v[148:151], a[12:15], v[16:31]\n"
    "buffer_load_dwordx4 v223, s[36:39], s29 offen offset:3072 lds\n"
    "v_exp_f32 v36, v36\n"
    "v_exp_f32 v37, v37\n"
    "v_exp_f32 v38, v38\n"
    "v_exp_f32 v39, v39\n"
    "v_cvt_pk_bf16_f32 v56, v32, v33\n"
    "v_cvt_pk_bf16_f32 v57, v34, v35\n"
    "v_mfma_f32_32x32x16_bf16 v[112:127], v[136:139], v[48:51], v[112:127]\n"
    "s_add_u32 s51, s51, 0x4000\n"
    "v_exp_f32 v40, v40\n"
    "v_exp_f32 v41, v41\n"
    "v_exp_f32 v42, v42\n"
    "v_exp_f32 v43, v43\n"
    "v_cvt_pk_bf16_f32 v58, v36, v37\n"
    "v_cvt_pk_bf16_f32 v59, v38, v39\n"
    "v_mfma_f32_32x32x16_bf16 v[112:127], v[140:143], v[52:55], v[112:127]\n"
    "s_and_b32 s51, s51, 0x3ffff\n"
    "buffer_load_dwordx2 v[240:241], v249, s[8:11], s77 offen offset:0\n"
    "v_exp_f32 v44, v44\n"
    "v_exp_f32 v45, v45\n"
    "v_exp_f32 v46, v46\n"
    "v_exp_f32 v47, v47\n"
    "v_cvt_pk_bf16_f32 v60, v40, v41\n"
    "v_cvt_pk_bf16_f32 v61, v42, v43\n"
    "v_cvt_pk_bf16_f32 v62, v44, v45\n"
    "v_cvt_pk_bf16_f32 v63, v46, v47\n"
    "v_mfma_f32_32x32x16_bf16 v[32:47], v[144:147], a[16:19], 0\n"
    "buffer_load_dwordx4 v[128:131], v0, s[12:15], s51 offen offset:0 sc1\n"
    "v_exp_f32 v16, v16\n"
    "v_exp_f32 v17, v17\n"
    "v_exp_f32 v18, v18\n"
    "v_exp_f32 v19, v19\n"
    "v_mfma_f32_32x32x16_bf16 v[32:47], v[148:151], a[20:23], v[32:47]\n"
    "buffer_load_dwordx2 v[242:243], v249, s[8:11], s77 offen offset:16\n"
    "v_exp_f32 v20, v20\n"
    "v_exp_f32 v21, v21\n"
    "v_exp_f32 v22, v22\n"
    "v_exp_f32 v23, v23\n"
    "v_cvt_pk_bf16_f32 v48, v16, v17\n"
    "v_cvt_pk_bf16_f32 v49, v18, v19\n"
    "v_mfma_f32_32x32x16_bf16 v[64:79], v[136:139], v[56:59], v[64:79]\n"
    "v_exp_f32 v24, v24\n"
    "v_exp_f32 v25, v25\n"
    "v_exp_f32 v26, v26\n"
    "v_exp_f32 v27, v27\n"
    "v_cvt_pk_bf16_f32 v50, v20, v21\n"
    "v_cvt_pk_bf16_f32 v51, v22, v23\n"
    "v_mfma_f32_32x32x16_bf16 v[64:79], v[140:143], v[60:63], v[64:79]\n"
    "buffer_load_dwordx2 v[244:245], v249, s[8:11], s77 offen offset:32\n"
    "v_exp_f32 v28, v28\n"
    "v_exp_f32 v29, v29\n"
    "v_exp_f32 v30, v30\n"
    "v_exp_f32 v31, v31\n"
    "v_cvt_pk_bf16_f32 v52, v24, v25\n"
    "v_cvt_pk_bf16_f32 v53, v26, v27\n"
    "v_cvt_pk_bf16_f32 v54, v28, v29\n"
    "v_cvt_pk_bf16_f32 v55, v30, v31\n"
    "v_mfma_f32_32x32x16_bf16 v[16:31], v[144:147], a[24:27], 0\n"
    "buffer_load_dwordx4 v[132:135], v0, s[12:15], s51 offen offset:1024 sc1\n"
    "v_exp_f32 v32, v32\n"
    "v_exp_f32 v33, v33\n"
    "v_exp_f32 v34, v34\n"
    "v_exp_f32 v35, v35\n"
    "v_mfma_f32_32x32x16_bf16 v[16:31], v[148:151], a[28:31], v[16:31]\n"
    "v_exp_f32 v36, v36\n"
    "v_exp_f32 v37, v37\n"
    "v_exp_f32 v38, v38\n"
    "v_exp_f32 v39, v39\n"
    "v_cvt_pk_bf16_f32 v56, v32, v33\n"
    "v_cvt_pk_bf16_f32 v57, v34, v35\n"
    "v_mfma_f32_32x32x16_bf16 v[80:95], v[152:155], v[48:51], v[80:95]\n"
    "buffer_load_dwordx2 v[246:247], v249, s[8:11], s77 offen offset:48\n"
    "v_exp_f32 v40, v40\n"
    "v_exp_f32 v41, v41\n"
    "v_exp_f32 v42, v42\n"
    "v_exp_f32 v43, v43\n"
    "v_cvt_pk_bf16_f32 v58, v36, v37\n"
    "v_cvt_pk_bf16_f32 v59, v38, v39\n"
    "v_mfma_f32_32x32x16_bf16 v[80:95], v[156:159], v[52:55], v[80:95]\n"
    "v_exp_f32 v44, v44\n"
    "v_exp_f32 v45, v45\n"
    "v_exp_f32 v46, v46\n"
    "v_exp_f32 v47, v47\n"
    "v_cvt_pk_bf16_f32 v60, v40, v41\n"
    "v_cvt_pk_bf16_f32 v61, v42, v43\n"
    "v_cvt_pk_bf16_f32 v62, v44, v45\n"
    "v_cvt_pk_bf16_f32 v63, v46, v47\n"
    "v_mfma_f32_32x32x16_bf16 v[32:47], v[144:147], a[0:3], 0\n"
    "buffer_load_dwordx4 v[136:139], v0, s[12:15], s51 offen offset:2048 sc1\n"
    "v_exp_f32 v16, v16\n"
    "v_exp_f32 v17, v17\n"
    "v_exp_f32 v18, v18\n"
    "v_exp_f32 v19, v19\n"
    "v_mfma_f32_32x32x16_bf16 v[32:47], v[148:151], a[4:7], v[32:47]\n"
    "v_max_u32 v248, v248, v150\n"
    "buffer_load_dwordx2 v[8:9], v6, s[4:7], 0 offen sc1\n"
    "v_exp_f32 v20, v20\n"
    "v_exp_f32 v21, v21\n"
    "v_exp_f32 v22, v22\n"
    "v_exp_f32 v23, v23\n"
    "v_cvt_pk_bf16_f32 v48, v16, v17\n"
    "v_cvt_pk_bf16_f32 v49, v18, v19\n"
    "v_mfma_f32_32x32x16_bf16 v[96:111], v[152:155], v[56:59], v[96:111]\n"
    "buffer_load_dwordx2 v[10:11], v7, s[4:7], 0 offen sc1\n"
    "v_exp_f32 v24, v24\n"
    "v_exp_f32 v25, v25\n"
    "v_exp_f32 v26, v26\n"
    "v_exp_f32 v27, v27\n"
    "v_cvt_pk_bf16_f32 v50, v20, v21\n"
    "v_cvt_pk_bf16_f32 v51, v22, v23\n"
    "v_mfma_f32_32x32x16_bf16 v[96:111], v[156:159], v[60:63], v[96:111]\n"
    "s_waitcnt vmcnt(21)\n"
    "v_exp_f32 v28, v28\n"
    "v_exp_f32 v29, v29\n"
    "v_exp_f32 v30, v30\n"
    "v_exp_f32 v31, v31\n"
    "v_cvt_pk_bf16_f32 v52, v24, v25\n"
    "v_cvt_pk_bf16_f32 v53, v26, v27\n"
    "v_cvt_pk_bf16_f32 v54, v28, v29\n"
    "v_cvt_pk_bf16_f32 v55, v30, v31\n"
    "v_mfma_f32_32x32x16_bf16 v[16:31], v[160:163], a[8:11], 0\n"
    "buffer_load_dwordx4 v[140:143], v0, s[12:15], s51 offen offset:3072 sc1\n"
    "v_exp_f32 v32, v32\n"
    "v_exp_f32 v33, v33\n"
    "v_exp_f32 v34, v34\n"
    "v_exp_f32 v35, v35\n"
    "v_mfma_f32_32x32x16_bf16 v[16:31], v[164:167], a[12:15], v[16:31]\n"
    "s_waitcnt vmcnt(10)\n"
    "ds_read_b64_tr_b16 a[64:65], v14 offset:0\n"
    "v_exp_f32 v36, v36\n"
    "v_exp_f32 v37, v37\n"
    "v_exp_f32 v38, v38\n"
    "v_exp_f32 v39, v39\n"
    "v_cvt_pk_bf16_f32 v56, v32, v33\n"
    "v_cvt_pk_bf16_f32 v57, v34, v35\n"
    "v_mfma_f32_32x32x16_bf16 v[112:127], v[152:155], v[48:51], v[112:127]\n"
    "s_add_u32 s51, s51, 0x4000\n"
    "ds_read_b64_tr_b16 a[66:67], v14 offset:1024\n"
    "v_exp_f32 v40, v40\n"
    "v_exp_f32 v41, v41\n"
    "v_exp_f32 v42, v42\n"
    "v_exp_f32 v43, v43\n"
    "v_cvt_pk_bf16_f32 v58, v36, v37\n"
    "v_cvt_pk_bf16_f32 v59, v38, v39\n"
    "v_mfma_f32_32x32x16_bf16 v[112:127], v[156:159], v[52:55], v[112:127]\n"
    "s_and_b32 s51, s51, 0x3ffff\n"
    "ds_read_b64_tr_b16 a[68:69], v15 offset:0\n"
    "v_exp_f32 v44, v44\n"
    "v_exp_f32 v45, v45\n"
    "v_exp_f32 v46, v46\n"
    "v_exp_f32 v47, v47\n"
    "v_cvt_pk_bf16_f32 v60, v40, v41\n"
    "v_cvt_pk_bf16_f32 v61, v42, v43\n"
    "v_cvt_pk_bf16_f32 v62, v44, v45\n"
    "v_cvt_pk_bf16_f32 v63, v46, v47\n"
    "v_mfma_f32_32x32x16_bf16 v[32:47], v[160:163], a[16:19], 0\n"
    "buffer_load_dwordx4 v[144:147], v0, s[12:15], s51 offen offset:0 sc1\n"
    "v_exp_f32 v16, v16\n"
    "v_exp_f32 v17, v17\n"
    "v_exp_f32 v18, v18\n"
    "v_exp_f32 v19, v19\n"
    "v_mfma_f32_32x32x16_bf16 v[32:47], v[164:167], a[20:23], v[32:47]\n"
    "ds_read_b64_tr_b16 a[70:71], v15 offset:1024\n"
    "v_exp_f32 v20, v20\n"
    "v_exp_f32 v21, v21\n"
    "v_exp_f32 v22, v22\n"
    "v_exp_f32 v23, v23\n"
    "v_cvt_pk_bf16_f32 v48, v16, v17\n"
    "v_cvt_pk_bf16_f32 v49, v18, v19\n"
    "v_mfma_f32_32x32x16_bf16 v[64:79], v[152:155], v[56:59], v[64:79]\n"
    "ds_read_b64_tr_b16 a[72:73], v14 offset:2048\n"
    "v_exp_f32 v24, v24\n"
    "v_exp_f32 v25, v25\n"
    "v_exp_f32 v26, v26\n"
    "v_exp_f32 v27, v27\n"
    "v_cvt_pk_bf16_f32 v50, v20, v21\n"
    "v_cvt_pk_bf16_f32 v51, v22, v23\n"
    "v_mfma_f32_32x32x16_bf16 v[64:79], v[156:159], v[60:63], v[64:79]\n"
    "ds_read_b64_tr_b16 a[74:75], v14 offset:3072\n"
    "v_exp_f32 v28, v28\n"
    "v_exp_f32 v29, v29\n"
    "v_exp_f32 v30, v30\n"
    "v_exp_f32 v31, v31\n"
    "v_cvt_pk_bf16_f32 v52, v24, v25\n"
    "v_cvt_pk_bf16_f32 v53, v26, v27\n"
    "v_cvt_pk_bf16_f32 v54, v28, v29\n"
    "v_cvt_pk_bf16_f32 v55, v30, v31\n"
    "v_mfma_f32_32x32x16_bf16 v[16:31], v[160:163], a[24:27], 0\n"
    "buffer_load_dwordx4 v[148:151], v0, s[12:15], s51 offen offset:1024 sc1\n"
    "v_exp_f32 v32, v32\n"
    "v_exp_f32 v33, v33\n"
    "v_exp_f32 v34, v34\n"
    "v_exp_f32 v35, v35\n"
    "v_mfma_f32_32x32x16_bf16 v[16:31], v[164:167], a[28:31], v[16:31]\n"
    "ds_read_b64_tr_b16 a[76:77], v15 offset:2048\n"
    "v_exp_f32 v36, v36\n"
    "v_exp_f32 v37, v37\n"
    "v_exp_f32 v38, v38\n"
    "v_exp_f32 v39, v39\n"
    "v_cvt_pk_bf16_f32 v56, v32, v33\n"
    "v_cvt_pk_bf16_f32 v57, v34, v35\n"
    "v_mfma_f32_32x32x16_bf16 v[80:95], v[168:171], v[48:51], v[80:95]\n"
    "ds_read_b64_tr_b16 a[78:79], v15 offset:3072\n"
    "v_exp_f32 v40, v40\n"
    "v_exp_f32 v41, v41\n"
    "v_exp_f32 v42, v42\n"
    "v_exp_f32 v43, v43\n"
    "v_cvt_pk_bf16_f32 v58, v36, v37\n"
    "v_cvt_pk_bf16_f32 v59, v38, v39\n"
    "v_mfma_f32_32x32x16_bf16 v[80:95], v[172:175], v[52:55], v[80:95]\n"
    "v_exp_f32 v44, v44\n"
    "v_exp_f32 v45, v45\n"
    "v_exp_f32 v46, v46\n"
    "v_exp_f32 v47, v47\n"
    "v_cvt_pk_bf16_f32 v60, v40, v41\n"
    "v_cvt_pk_bf16_f32 v61, v42, v43\n"
    "v_cvt_pk_bf16_f32 v62, v44, v45\n"
    "v_cvt_pk_bf16_f32 v63, v46, v47\n"
    "v_mfma_f32_32x32x16_bf16 v[32:47], v[160:163], a[0:3], 0\n"
    "buffer_load_dwordx4 v[152:155], v0, s[12:15], s51 offen offset:2048 sc1\n"
    "v_exp_f32 v16, v16\n"
    "v_exp_f32 v17, v17\n"
    "v_exp_f32 v18, v18\n"
    "v_exp_f32 v19, v19\n"
    "v_mfma_f32_32x32x16_bf16 v[32:47], v[164:167], a[4:7], v[32:47]\n"
    "v_max_u32 v248, v248, v166\n"
    "ds_read_b64_tr_b16 a[80:81], v14 offset:4096\n"
    "v_exp_f32 v20, v20\n"
    "v_exp_f32 v21, v21\n"
    "v_exp_f32 v22, v22\n"
    "v_exp_f32 v23, v23\n"
    "v_cvt_pk_bf16_f32 v48, v16, v17\n"
    "v_cvt_pk_bf16_f32 v49, v18, v19\n"
    "v_mfma_f32_32x32x16_bf16 v[96:111], v[168:171], v[56:59], v[96:111]\n"
    "ds_read_b64_tr_b16 a[82:83], v14 offset:5120\n"
    "v_exp_f32 v24, v24\n"
    "v_exp_f32 v25, v25\n"
    "v_exp_f32 v26, v26\n"
    "v_exp_f32 v27, v27\n"
    "v_cvt_pk_bf16_f32 v50, v20, v21\n"
    "v_cvt_pk_bf16_f32 v51, v22, v23\n"
    "v_mfma_f32_32x32x16_bf16 v[96:111], v[172:175], v[60:63], v[96:111]\n"
    "ds_read_b64_tr_b16 a[84:85], v15 offset:4096\n"
    "v_exp_f32 v28, v28\n"
    "v_exp_f32 v29, v29\n"
    "v_exp_f32 v30, v30\n"
    "v_exp_f32 v31, v31\n"
    "v_cvt_pk_bf16_f32 v52, v24, v25\n"
    "v_cvt_pk_bf16_f32 v53, v26, v27\n"
    "v_cvt_pk_bf16_f32 v54, v28, v29\n"
    "v_cvt_pk_bf16_f32 v55, v30, v31\n"
    "v_mfma_f32_32x32x16_bf16 v[16:31], v[176:179], a[8:11], 0\n"
    "buffer_load_dwordx4 v[156:159], v0, s[12:15], s51 offen offset:3072 sc1\n"
    "v_exp_f32 v32, v32\n"
    "v_exp_f32 v33, v33\n"
    "v_exp_f32 v34, v34\n"
    "v_exp_f32 v35, v35\n"
    "v_mfma_f32_32x32x16_bf16 v[16:31], v[180:183], a[12:15], v[16:31]\n"
    "ds_read_b64_tr_b16 a[86:87], v15 offset:5120\n"
    "v_exp_f32 v36, v36\n"
    "v_exp_f32 v37, v37\n"
    "v_exp_f32 v38, v38\n"
    "v_exp_f32 v39, v39\n"
    "v_cvt_pk_bf16_f32 v56, v32, v33\n"
    "v_cvt_pk_bf16_f32 v57, v34, v35\n"
    "v_mfma_f32_32x32x16_bf16 v[112:127], v[168:171], v[48:51], v[112:127]\n"
    "s_add_u32 s51, s51, 0x4000\n"
    "ds_read_b64_tr_b16 a[88:89], v14 offset:6144\n"
    "v_exp_f32 v40, v40\n"
    "v_exp_f32 v41, v41\n"
    "v_exp_f32 v42, v42\n"
    "v_exp_f32 v43, v43\n"
    "v_cvt_pk_bf16_f32 v58, v36, v37\n"
    "v_cvt_pk_bf16_f32 v59, v38, v39\n"
    "v_mfma_f32_32x32x16_bf16 v[112:127], v[172:175], v[52:55], v[112:127]\n"
    "s_and_b32 s51, s51, 0x3ffff\n"
    "ds_read_b64_tr_b16 a[90:91], v14 offset:7168\n"
    "v_exp_f32 v44, v44\n"
    "v_exp_f32 v45, v45\n"
    "v_exp_f32 v46, v46\n"
    "v_exp_f32 v47, v47\n"
    "v_cvt_pk_bf16_f32 v60, v40, v41\n"
    "v_cvt_pk_bf16_f32 v61, v42, v43\n"
    "v_cvt_pk_bf16_f32 v62, v44, v45\n"
    "v_cvt_pk_bf16_f32 v63, v46, v47\n"
    "v_mfma_f32_32x32x16_bf16 v[32:47], v[176:179], a[16:19], 0\n"
    "buffer_load_dwordx4 v[160:163], v0, s[12:15], s51 offen offset:0 sc1\n"
    "v_exp_f32 v16, v16\n"
    "v_exp_f32 v17, v17\n"
    "v_exp_f32 v18, v18\n"
    "v_exp_f32 v19, v19\n"
    "v_mfma_f32_32x32x16_bf16 v[32:47], v[180:183], a[20:23], v[32:47]\n"
    "ds_read_b64_tr_b16 a[92:93], v15 offset:6144\n"
    "v_exp_f32 v20, v20\n"
    "v_exp_f32 v21, v21\n"
    "v_exp_f32 v22, v22\n"
    "v_exp_f32 v23, v23\n"
    "v_cvt_pk_bf16_f32 v48, v16, v17\n"
    "v_cvt_pk_bf16_f32 v49, v18, v19\n"
    "v_mfma_f32_32x32x16_bf16 v[64:79], v[168:171], v[56:59], v[64:79]\n"
    "ds_read_b64_tr_b16 a[94:95], v15 offset:7168\n"
    "s_mov_b32 s3, s48\n"
    "v_exp_f32 v24, v24\n"
    "v_exp_f32 v25, v25\n"
    "v_exp_f32 v26, v26\n"
    "v_exp_f32 v27, v27\n"
    "v_cvt_pk_bf16_f32 v50, v20, v21\n"
    "v_cvt_pk_bf16_f32 v51, v22, v23\n"
    "v_mfma_f32_32x32x16_bf16 v[64:79], v[172:175], v[60:63], v[64:79]\n"
    "s_mov_b32 s40, s49\n"
    "v_exp_f32 v28, v28\n"
    "v_exp_f32 v29, v29\n"
    "v_exp_f32 v30, v30\n"
    "v_exp_f32 v31, v31\n"
    "v_cvt_pk_bf16_f32 v52, v24, v25\n"
    "v_cvt_pk_bf16_f32 v53, v26, v27\n"
    "v_cvt_pk_bf16_f32 v54, v28, v29\n"
    "v_cvt_pk_bf16_f32 v55, v30, v31\n"
    "v_mfma_f32_32x32x16_bf16 v[16:31], v[176:179], a[24:27], 0\n"
    "buffer_load_dwordx4 v[164:167], v0, s[12:15], s51 offen offset:1024 sc1\n"
    "s_waitcnt vmcnt(8)\n"
    "v_cmp_ne_u64 vcc, s[16:17], v[8:9]\n"
    "s_cbranch_vccnz Lchk_14%=\n"
    "Lchkb_15%=:\n"
    "s_waitcnt vmcnt(7)\n"
    "v_cmp_ne_u64 vcc, s[16:17], v[10:11]\n"
    "s_cbranch_vccnz Lchk_16%=\n"
    "Lchkb_17%=:\n"
    "s_waitcnt lgkmcnt(0)\n"
    "s_barrier\n"
    "v_exp_f32 v32, v32\n"
    "v_exp_f32 v33, v33\n"
    "v_exp_f32 v34, v34\n"
    "v_exp_f32 v35, v35\n"
    "v_mfma_f32_32x32x16_bf16 v[16:31], v[180:183], a[28:31], v[16:31]\n"
    "s_add_u32 s44, s55, 0\n"
    "s_add_u32 m0, s56, 57344\n"
    "v_exp_f32 v36, v36\n"
    "v_exp_f32 v37, v37\n"
    "v_exp_f32 v38, v38\n"
    "v_exp_f32 v39, v39\n"
    "v_cvt_pk_bf16_f32 v56, v32, v33\n"
    "v_cvt_pk_bf16_f32 v57, v34, v35\n"
    "v_mfma_f32_32x32x16_bf16 v[80:95], v[184:187], v[48:51], v[80:95]\n"
    "buffer_load_dwordx4 v0, s[4:7], s44 offen offset:0 sc1 lds\n"
    "s_mov_b32 s41, s50\n"
    "v_exp_f32 v40, v40\n"
    "v_exp_f32 v41, v41\n"
    "v_exp_f32 v42, v42\n"
    "v_exp_f32 v43, v43\n"
    "v_cvt_pk_bf16_f32 v58, v36, v37\n"
    "v_cvt_pk_bf16_f32 v59, v38, v39\n"
    "v_mfma_f32_32x32x16_bf16 v[80:95], v[188:191], v[52:55], v[80:95]\n"
    "buffer_load_dwordx4 v0, s[4:7], s44 offen offset:1024 sc1 lds\n"
    "s_lshl_b32 s30, s18, 13\n"
    "v_exp_f32 v44, v44\n"
    "v_exp_f32 v45, v45\n"
    "v_exp_f32 v46, v46\n"
    "v_exp_f32 v47, v47\n"
    "v_cvt_pk_bf16_f32 v60, v40, v41\n"
    "v_cvt_pk_bf16_f32 v61, v42, v43\n"
    "v_cvt_pk_bf16_f32 v62, v44, v45\n"
    "v_cvt_pk_bf16_f32 v63, v46, v47\n"
    "v_mfma_f32_32x32x16_bf16 v[32:47], v[176:179], a[0:3], 0\n"
    "buffer_load_dwordx4 v[168:171], v0, s[12:15], s51 offen offset:2048 sc1\n"
    "s_add_u32 s30, s30, 13320192\n"
    "v_exp_f32 v16, v16\n"
    "v_exp_f32 v17, v17\n"
    "v_exp_f32 v18, v18\n"
    "v_exp_f32 v19, v19\n"
    "v_mfma_f32_32x32x16_bf16 v[32:47], v[180:183], a[4:7], v[32:47]\n"
    "v_max_u32 v248, v248, v182\n"
    "buffer_load_dwordx4 v0, s[4:7], s44 offen offset:2048 sc1 lds\n"
    "s_lshl_b32 s61, s65, 15\n"
    "v_exp_f32 v20, v20\n"
    "v_exp_f32 v21, v21\n"
    "v_exp_f32 v22, v22\n"
    "v_exp_f32 v23, v23\n"
    "v_cvt_pk_bf16_f32 v48, v16, v17\n"
    "v_cvt_pk_bf16_f32 v49, v18, v19\n"
    "v_mfma_f32_32x32x16_bf16 v[96:111], v[184:187], v[56:59], v[96:111]\n"
    "buffer_load_dwordx4 v0, s[4:7], s44 offen offset:3072 sc1 lds\n"
    "s_add_u32 s61, s61, s30\n"
    "v_exp_f32 v24, v24\n"
    "v_exp_f32 v25, v25\n"
    "v_exp_f32 v26, v26\n"
    "v_exp_f32 v27, v27\n"
    "v_cvt_pk_bf16_f32 v50, v20, v21\n"
    "v_cvt_pk_bf16_f32 v51, v22, v23\n"
    "v_mfma_f32_32x32x16_bf16 v[96:111], v[188:191], v[60:63], v[96:111]\n"
    "s_waitcnt vmcnt(11)\n"
    "v_exp_f32 v28, v28\n"
    "v_exp_f32 v29, v29\n"
    "v_exp_f32 v30, v30\n"
    "v_exp_f32 v31, v31\n"
    "v_cvt_pk_bf16_f32 v52, v24, v25\n"
    "v_cvt_pk_bf16_f32 v53, v26, v27\n"
    "v_cvt_pk_bf16_f32 v54, v28, v29\n"
    "v_cvt_pk_bf16_f32 v55, v30, v31\n"
    "v_mfma_f32_32x32x16_bf16 v[16:31], v[128:131], a[8:11], 0\n"
    "buffer_load_dwordx4 v[172:175], v0, s[12:15], s51 offen offset:3072 sc1\n"
    "s_xor_b32 s60, s65, 1\n"
    "v_exp_f32 v32, v32\n"
    "v_exp_f32 v33, v33\n"
    "v_exp_f32 v34, v34\n"
    "v_exp_f32 v35, v35\n"
    "v_mfma_f32_32x32x16_bf16 v[16:31], v[132:135], a[12:15], v[16:31]\n"
    "s_add_u32 m0, s56, 61440\n"
    "s_nop 0\n"
    "buffer_load_dwordx4 v2, s[4:7], s44 offen offset:0 sc1 lds\n"
    "v_exp_f32 v36, v36\n"
    "v_exp_f32 v37, v37\n"
    "v_exp_f32 v38, v38\n"
    "v_exp_f32 v39, v39\n"
    "v_cvt_pk_bf16_f32 v56, v32, v33\n"
    "v_cvt_pk_bf16_f32 v57, v34, v35\n"
    "v_mfma_f32_32x32x16_bf16 v[112:127], v[184:187], v[48:51], v[112:127]\n"
    "s_add_u32 s51, s51, 0x4000\n"
    "buffer_load_dwordx4 v2, s[4:7], s44 offen offset:1024 sc1 lds\n"
    "v_exp_f32 v40, v40\n"
    "v_exp_f32 v41, v41\n"
    "v_exp_f32 v42, v42\n"
    "v_exp_f32 v43, v43\n"
    "v_cvt_pk_bf16_f32 v58, v36, v37\n"
    "v_cvt_pk_bf16_f32 v59, v38, v39\n"
    "v_mfma_f32_32x32x16_bf16 v[112:127], v[188:191], v[52:55], v[112:127]\n"
    "s_and_b32 s51, s51, 0x3ffff\n"
    "buffer_load_dwordx4 v2, s[4:7], s44 offen offset:2048 sc1 lds\n"
    "v_exp_f32 v44, v44\n"
    "v_exp_f32 v45, v45\n"
    "v_exp_f32 v46, v46\n"
    "v_exp_f32 v47, v47\n"
    "v_cvt_pk_bf16_f32 v60, v40, v41\n"
    "v_cvt_pk_bf16_f32 v61, v42, v43\n"
    "v_cvt_pk_bf16_f32 v62, v44, v45\n"
    "v_cvt_pk_bf16_f32 v63, v46, v47\n"
    "v_mfma_f32_32x32x16_bf16 v[32:47], v[128:131], a[16:19], 0\n"
    "buffer_load_dwordx4 v[176:179], v0, s[12:15], s51 offen offset:0 sc1\n"
    "s_lshl_b32 s60, s60, 15\n"
    "v_exp_f32 v16, v16\n"
    "v_exp_f32 v17, v17\n"
    "v_exp_f32 v18, v18\n"
    "v_exp_f32 v19, v19\n"
    "v_mfma_f32_32x32x16_bf16 v[32:47], v[132:135], a[20:23], v[32:47]\n"
    "s_add_u32 s44, s55, 29696\n"
    "s_add_u32 m0, s56, 86016\n"
    "v_exp_f32 v20, v20\n"
    "v_exp_f32 v21, v21\n"
    "v_exp_f32 v22, v22\n"
    "v_exp_f32 v23, v23\n"
    "v_cvt_pk_bf16_f32 v48, v16, v17\n"
    "v_cvt_pk_bf16_f32 v49, v18, v19\n"
    "v_mfma_f32_32x32x16_bf16 v[64:79], v[184:187], v[56:59], v[64:79]\n"
    "buffer_load_dwordx4 v0, s[4:7], s44 offen offset:0 sc1 lds\n"
    "s_add_u32 s60, s60, s30\n"
    "v_exp_f32 v24, v24\n"
    "v_exp_f32 v25, v25\n"
    "v_exp_f32 v26, v26\n"
    "v_exp_f32 v27, v27\n"
    "v_cvt_pk_bf16_f32 v50, v20, v21\n"
    "v_cvt_pk_bf16_f32 v51, v22, v23\n"
    "v_mfma_f32_32x32x16_bf16 v[64:79], v[188:191], v[60:63], v[64:79]\n"
    "s_mul_i32 s46, s75, 28672\n"
    "v_exp_f32 v28, v28\n"
    "v_exp_f32 v29, v29\n"
    "v_exp_f32 v30, v30\n"
    "v_exp_f32 v31, v31\n"
    "v_cvt_pk_bf16_f32 v52, v24, v25\n"
    "v_cvt_pk_bf16_f32 v53, v26, v27\n"
    "v_cvt_pk_bf16_f32 v54, v28, v29\n"
    "v_cvt_pk_bf16_f32 v55, v30, v31\n"
    "v_mfma_f32_32x32x16_bf16 v[16:31], v[128:131], a[24:27], 0\n"
    "buffer_load_dwordx4 v[180:183], v0, s[12:15], s51 offen offset:1024 sc1\n"
    "s_add_u32 s46, s46, 15417344\n"
    "v_exp_f32 v32, v32\n"
    "v_exp_f32 v33, v33\n"
    "v_exp_f32 v34, v34\n"
    "v_exp_f32 v35, v35\n"
    "v_mfma_f32_32x32x16_bf16 v[16:31], v[132:135], a[28:31], v[16:31]\n"
    "buffer_load_dwordx4 v0, s[4:7], s44 offen offset:1024 sc1 lds\n"
    "s_add_u32 s47, s46, 16384\n"
    "v_exp_f32 v36, v36\n"
    "v_exp_f32 v37, v37\n"
    "v_exp_f32 v38, v38\n"
    "v_exp_f32 v39, v39\n"
    "v_cvt_pk_bf16_f32 v56, v32, v33\n"
    "v_cvt_pk_bf16_f32 v57, v34, v35\n"
    "v_mfma_f32_32x32x16_bf16 v[80:95], v[136:139], v[48:51], v[80:95]\n"
    "buffer_load_dwordx4 v0, s[4:7], s44 offen offset:2048 sc1 lds\n"
    "s_mul_i32 s48, s75, 3584\n"
    "v_exp_f32 v40, v40\n"
    "v_exp_f32 v41, v41\n"
    "v_exp_f32 v42, v42\n"
    "v_exp_f32 v43, v43\n"
    "v_cvt_pk_bf16_f32 v58, v36, v37\n"
    "v_cvt_pk_bf16_f32 v59, v38, v39\n"
    "v_mfma_f32_32x32x16_bf16 v[80:95], v[140:143], v[52:55], v[80:95]\n"
    "buffer_load_dwordx4 v0, s[4:7], s44 offen offset:3072 sc1 lds\n"
    "s_add_u32 s48, s48, 294912\n"
    "v_exp_f32 v44, v44\n"
    "v_exp_f32 v45, v45\n"
    "v_exp_f32 v46, v46\n"
    "v_exp_f32 v47, v47\n"
    "v_cvt_pk_bf16_f32 v60, v40, v41\n"
    "v_cvt_pk_bf16_f32 v61, v42, v43\n"
    "v_cvt_pk_bf16_f32 v62, v44, v45\n"
    "v_cvt_pk_bf16_f32 v63, v46, v47\n"
    "v_mfma_f32_32x32x16_bf16 v[32:47], v[128:131], a[0:3], 0\n"
    "buffer_load_dwordx4 v[184:187], v0, s[12:15], s51 offen offset:2048 sc1\n"
    "s_mul_i32 s49, s74, 207872\n"
    "v_exp_f32 v16, v16\n"
    "v_exp_f32 v17, v17\n"
    "v_exp_f32 v18, v18\n"
    "v_exp_f32 v19, v19\n"
    "v_mfma_f32_32x32x16_bf16 v[32:47], v[132:135], a[4:7], v[32:47]\n"
    "v_max_u32 v248, v248, v134\n"
    "s_add_u32 m0, s56, 90112\n"
    "s_nop 0\n"
    "buffer_load_dwordx4 v2, s[4:7], s44 offen offset:0 sc1 lds\n"
    "v_exp_f32 v20, v20\n"
    "v_exp_f32 v21, v21\n"
    "v_exp_f32 v22, v22\n"
    "v_exp_f32 v23, v23\n"
    "v_cvt_pk_bf16_f32 v48, v16, v17\n"
    "v_cvt_pk_bf16_f32 v49, v18, v19\n"
    "v_mfma_f32_32x32x16_bf16 v[96:111], v[136:139], v[56:59], v[96:111]\n"
    "s_add_u32 s49, s49, 4222976\n"
    "v_exp_f32 v24, v24\n"
    "v_exp_f32 v25, v25\n"
    "v_exp_f32 v26, v26\n"
    "v_exp_f32 v27, v27\n"
    "v_cvt_pk_bf16_f32 v50, v20, v21\n"
    "v_cvt_pk_bf16_f32 v51, v22, v23\n"
    "v_mfma_f32_32x32x16_bf16 v[96:111], v[140:143], v[60:63], v[96:111]\n"
    "s_waitcnt vmcnt(19)\n"
    "buffer_load_dwordx4 v2, s[4:7], s44 offen offset:1024 sc1 lds\n"
    "v_exp_f32 v28, v28\n"
    "v_exp_f32 v29, v29\n"
    "v_exp_f32 v30, v30\n"
    "v_exp_f32 v31, v31\n"
    "v_cvt_pk_bf16_f32 v52, v24, v25\n"
    "v_cvt_pk_bf16_f32 v53, v26, v27\n"
    "v_cvt_pk_bf16_f32 v54, v28, v29\n"
    "v_cvt_pk_bf16_f32 v55, v30, v31\n"
    "v_mfma_f32_32x32x16_bf16 v[16:31], v[144:147], a[8:11], 0\n"
    "buffer_load_dwordx4 v[188:191], v0, s[12:15], s51 offen offset:3072 sc1\n"
    "s_mul_i32 s0, s18, 28672\n"
    "v_exp_f32 v32, v32\n"
    "v_exp_f32 v33, v33\n"
    "v_exp_f32 v34, v34\n"
    "v_exp_f32 v35, v35\n"
    "v_mfma_f32_32x32x16_bf16 v[16:31], v[148:151], a[12:15], v[16:31]\n"
    "buffer_load_dwordx4 v2, s[4:7], s44 offen offset:2048 sc1 lds\n"
    "s_add_u32 s0, s0, 17252352\n"
    "v_exp_f32 v36, v36\n"
    "v_exp_f32 v37, v37\n"
    "v_exp_f32 v38, v38\n"
    "v_exp_f32 v39, v39\n"
    "v_cvt_pk_bf16_f32 v56, v32, v33\n"
    "v_cvt_pk_bf16_f32 v57, v34, v35\n"
    "v_mfma_f32_32x32x16_bf16 v[112:127], v[136:139], v[48:51], v[112:127]\n"
    "s_add_u32 s44, s55, 59392\n"
    "s_add_u32 m0, s56, 114688\n"
    "v_exp_f32 v40, v40\n"
    "v_exp_f32 v41, v41\n"
    "v_exp_f32 v42, v42\n"
    "v_exp_f32 v43, v43\n"
    "v_cvt_pk_bf16_f32 v58, v36, v37\n"
    "v_cvt_pk_bf16_f32 v59, v38, v39\n"
    "v_mfma_f32_32x32x16_bf16 v[112:127], v[140:143], v[52:55], v[112:127]\n"
    "buffer_load_dwordx4 v0, s[4:7], s44 offen offset:0 sc1 lds\n"
    "s_mul_i32 s24, s65, 114688\n"
    "v_exp_f32 v44, v44\n"
    "v_exp_f32 v45, v45\n"
    "v_exp_f32 v46, v46\n"
    "v_exp_f32 v47, v47\n"
    "v_cvt_pk_bf16_f32 v60, v40, v41\n"
    "v_cvt_pk_bf16_f32 v61, v42, v43\n"
    "v_cvt_pk_bf16_f32 v62, v44, v45\n"
    "v_cvt_pk_bf16_f32 v63, v46, v47\n"
    "v_mfma_f32_32x32x16_bf16 v[32:47], v[144:147], a[16:19], 0\n"
    "buffer_load_dwordx4 v0, s[4:7], s44 offen offset:1024 sc1 lds\n"
    "s_add_u32 s24, s24, s0\n"
    "v_exp_f32 v16, v16\n"
    "v_exp_f32 v17, v17\n"
    "v_exp_f32 v18, v18\n"
    "v_exp_f32 v19, v19\n"
    "v_mfma_f32_32x32x16_bf16 v[32:47], v[148:151], a[20:23], v[32:47]\n"
    "s_xor_b32 s1, s65, 1\n"
    "v_exp_f32 v20, v20\n"
    "v_exp_f32 v21, v21\n"
    "v_exp_f32 v22, v22\n"
    "v_exp_f32 v23, v23\n"
    "v_cvt_pk_bf16_f32 v48, v16, v17\n"
    "v_cvt_pk_bf16_f32 v49, v18, v19\n"
    "v_mfma_f32_32x32x16_bf16 v[64:79], v[136:139], v[56:59], v[64:79]\n"
    "buffer_load_dwordx4 v0, s[4:7], s44 offen offset:2048 sc1 lds\n"
    "s_mul_i32 s26, s1, 114688\n"
    "v_exp_f32 v24, v24\n"
    "v_exp_f32 v25, v25\n"
    "v_exp_f32 v26, v26\n"
    "v_exp_f32 v27, v27\n"
    "v_cvt_pk_bf16_f32 v50, v20, v21\n"
    "v_cvt_pk_bf16_f32 v51, v22, v23\n"
    "v_mfma_f32_32x32x16_bf16 v[64:79], v[140:143], v[60:63], v[64:79]\n"
    "buffer_load_dwordx4 v0, s[4:7], s44 offen offset:3072 sc1 lds\n"
    "s_add_u32 s26, s26, s0\n"
    "v_exp_f32 v28, v28\n"
    "v_exp_f32 v29, v29\n"
    "v_exp_f32 v30, v30\n"
    "v_exp_f32 v31, v31\n"
    "v_cvt_pk_bf16_f32 v52, v24, v25\n"
    "v_cvt_pk_bf16_f32 v53, v26, v27\n"
    "v_cvt_pk_bf16_f32 v54, v28, v29\n"
    "v_cvt_pk_bf16_f32 v55, v30, v31\n"
    "v_mfma_f32_32x32x16_bf16 v[16:31], v[144:147], a[24:27], 0\n"
    "s_add_u32 m0, s56, 118784\n"
    "s_nop 0\n"
    "buffer_load_dwordx4 v2, s[4:7], s44 offen offset:0 sc1 lds\n"
    "v_exp_f32 v32, v32\n"
    "v_exp_f32 v33, v33\n"
    "v_exp_f32 v34, v34\n"
    "v_exp_f32 v35, v35\n"
    "v_mfma_f32_32x32x16_bf16 v[16:31], v[148:151], a[28:31], v[16:31]\n"
    "buffer_load_dwordx4 v2, s[4:7], s44 offen offset:1024 sc1 lds\n"
    "s_add_u32 s25, s24, 16384\n"
    "v_exp_f32 v36, v36\n"
    "v_exp_f32 v37, v37\n"
    "v_exp_f32 v38, v38\n"
    "v_exp_f32 v39, v39\n"
    "v_cvt_pk_bf16_f32 v56, v32, v33\n"
    "v_cvt_pk_bf16_f32 v57, v34, v35\n"
    "v_mfma_f32_32x32x16_bf16 v[80:95], v[152:155], v[48:51], v[80:95]\n"
    "s_add_u32 s27, s26, 16384\n"
    "v_exp_f32 v40, v40\n"
    "v_exp_f32 v41, v41\n"
    "v_exp_f32 v42, v42\n"
    "v_exp_f32 v43, v43\n"
    "v_cvt_pk_bf16_f32 v58, v36, v37\n"
    "v_cvt_pk_bf16_f32 v59, v38, v39\n"
    "v_mfma_f32_32x32x16_bf16 v[80:95], v[156:159], v[52:55], v[80:95]\n"
    "buffer_load_dwordx4 v2, s[4:7], s44 offen offset:2048 sc1 lds\n"
    "v_exp_f32 v44, v44\n"
    "v_exp_f32 v45, v45\n"
    "v_exp_f32 v46, v46\n"
    "v_exp_f32 v47, v47\n"
    "v_cvt_pk_bf16_f32 v60, v40, v41\n"
    "v_cvt_pk_bf16_f32 v61, v42, v43\n"
    "v_cvt_pk_bf16_f32 v62, v44, v45\n"
    "v_cvt_pk_bf16_f32 v63, v46, v47\n"
    "v_mfma_f32_32x32x16_bf16 v[32:47], v[144:147], a[0:3], 0\n"
    "s_add_u32 s44, s55, 89088\n"
    "buffer_load_dwordx4 v[192:195], v0, s[4:7], s44 offen offset:0 sc1\n"
    "v_exp_f32 v16, v16\n"
    "v_exp_f32 v17, v17\n"
    "v_exp_f32 v18, v18\n"
    "v_exp_f32 v19, v19\n"
    "v_mfma_f32_32x32x16_bf16 v[32:47], v[148:151], a[4:7], v[32:47]\n"
    "v_max_u32 v248, v248, v150\n"
    "buffer_load_dwordx4 v[196:199], v0, s[4:7], s44 offen offset:1024 sc1\n"
    "s_mov_b32 s32, s42\n"
    "v_exp_f32 v20, v20\n"
    "v_exp_f32 v21, v21\n"
    "v_exp_f32 v22, v22\n"
    "v_exp_f32 v23, v23\n"
    "v_cvt_pk_bf16_f32 v48, v16, v17\n"
    "v_cvt_pk_bf16_f32 v49, v18, v19\n"
    "v_mfma_f32_32x32x16_bf16 v[96:111], v[152:155], v[56:59], v[96:111]\n"
    "buffer_load_dwordx4 v[200:203], v0, s[4:7], s44 offen offset:2048 sc1\n"
    "s_and_b32 s33, s43, 0xffff\n"
    "v_exp_f32 v24, v24\n"
    "v_exp_f32 v25, v25\n"
    "v_exp_f32 v26, v26\n"
    "v_exp_f32 v27, v27\n"
    "v_cvt_pk_bf16_f32 v50, v20, v21\n"
    "v_cvt_pk_bf16_f32 v51, v22, v23\n"
    "v_mfma_f32_32x32x16_bf16 v[96:111], v[156:159], v[60:63], v[96:111]\n"
    "s_waitcnt vmcnt(24)\n"
    "buffer_load_dwordx4 v[204:207], v0, s[4:7], s44 offen offset:3072 sc1\n"
    "v_exp_f32 v28, v28\n"
    "v_exp_f32 v29, v29\n"
    "v_exp_f32 v30, v30\n"
    "v_exp_f32 v31, v31\n"
    "v_cvt_pk_bf16_f32 v52, v24, v25\n"
    "v_cvt_pk_bf16_f32 v53, v26, v27\n"
    "v_cvt_pk_bf16_f32 v54, v28, v29\n"
    "v_cvt_pk_bf16_f32 v55, v30, v31\n"
    "v_mfma_f32_32x32x16_bf16 v[16:31], v[160:163], a[8:11], 0\n"
    "s_mov_b32 s34, 1835008\n"
    "v_exp_f32 v32, v32\n"
    "v_exp_f32 v33, v33\n"
    "v_exp_f32 v34, v34\n"
    "v_exp_f32 v35, v35\n"
    "v_mfma_f32_32x32x16_bf16 v[16:31], v[164:167], a[12:15], v[16:31]\n"
    "buffer_load_dwordx4 v[208:211], v2, s[4:7], s44 offen offset:0 sc1\n"
    "s_mov_b32 s35, 0x20000\n"
    "v_exp_f32 v36, v36\n"
    "v_exp_f32 v37, v37\n"
    "v_exp_f32 v38, v38\n"
    "v_exp_f32 v39, v39\n"
    "v_cvt_pk_bf16_f32 v56, v32, v33\n"
    "v_cvt_pk_bf16_f32 v57, v34, v35\n"
    "v_mfma_f32_32x32x16_bf16 v[112:127], v[152:155], v[48:51], v[112:127]\n"
    "buffer_load_dwordx4 v[212:215], v2, s[4:7], s44 offen offset:1024 sc1\n"
    "s_and_b32 s28, s18, 1\n"
    "v_exp_f32 v40, v40\n"
    "v_exp_f32 v41, v41\n"
    "v_exp_f32 v42, v42\n"
    "v_exp_f32 v43, v43\n"
    "v_cvt_pk_bf16_f32 v58, v36, v37\n"
    "v_cvt_pk_bf16_f32 v59, v38, v39\n"
    "v_mfma_f32_32x32x16_bf16 v[112:127], v[156:159], v[52:55], v[112:127]\n"
    "buffer_load_dwordx4 v[216:219], v2, s[4:7], s44 offen offset:2048 sc1\n"
    "s_lshl_b32 s28, s28, 7\n"
    "v_exp_f32 v44, v44\n"
    "v_exp_f32 v45, v45\n"
    "v_exp_f32 v46, v46\n"
    "v_exp_f32 v47, v47\n"
    "v_cvt_pk_bf16_f32 v60, v40, v41\n"
    "v_cvt_pk_bf16_f32 v61, v42, v43\n"
    "v_cvt_pk_bf16_f32 v62, v44, v45\n"
    "v_cvt_pk_bf16_f32 v63, v46, v47\n"
    "v_mfma_f32_32x32x16_bf16 v[32:47], v[160:163], a[16:19], 0\n"
    "s_add_u32 s44, s55, 118784\n"
    "buffer_load_dwordx4 v[220:223], v0, s[4:7], s44 offen offset:0 sc1\n"
    "v_exp_f32 v16, v16\n"
    "v_exp_f32 v17, v17\n"
    "v_exp_f32 v18, v18\n"
    "v_exp_f32 v19, v19\n"
    "v_mfma_f32_32x32x16_bf16 v[32:47], v[164:167], a[20:23], v[32:47]\n"
    "s_add_u32 s28, s28, 155136\n"
    "v_exp_f32 v20, v20\n"
    "v_exp_f32 v21, v21\n"
    "v_exp_f32 v22, v22\n"
    "v_exp_f32 v23, v23\n"
    "v_cvt_pk_bf16_f32 v48, v16, v17\n"
    "v_cvt_pk_bf16_f32 v49, v18, v19\n"
    "v_mfma_f32_32x32x16_bf16 v[64:79], v[152:155], v[56:59], v[64:79]\n"
    "buffer_load_dwordx4 v[224:227], v0, s[4:7], s44 offen offset:1024 sc1\n"
    "s_mul_i32 s29, s74, 56\n"
    "v_exp_f32 v24, v24\n"
    "v_exp_f32 v25, v25\n"
    "v_exp_f32 v26, v26\n"
    "v_exp_f32 v27, v27\n"
    "v_cvt_pk_bf16_f32 v50, v20, v21\n"
    "v_cvt_pk_bf16_f32 v51, v22, v23\n"
    "v_mfma_f32_32x32x16_bf16 v[64:79], v[156:159], v[60:63], v[64:79]\n"
    "buffer_load_dwordx4 v[228:231], v0, s[4:7], s44 offen offset:2048 sc1\n"
    "s_add_u32 s29, s29, 13344\n"
    "v_exp_f32 v28, v28\n"
    "v_exp_f32 v29, v29\n"
    "v_exp_f32 v30, v30\n"
    "v_exp_f32 v31, v31\n"
    "v_cvt_pk_bf16_f32 v52, v24, v25\n"
    "v_cvt_pk_bf16_f32 v53, v26, v27\n"
    "v_cvt_pk_bf16_f32 v54, v28, v29\n"
    "v_cvt_pk_bf16_f32 v55, v30, v31\n"
    "v_mfma_f32_32x32x16_bf16 v[16:31], v[160:163], a[24:27], 0\n"
    "buffer_load_dwordx4 v[232:235], v0, s[4:7], s44 offen offset:3072 sc1\n"
    "s_sub_u32 s96, s41, 16320\n"
    "v_exp_f32 v32, v32\n"
    "v_exp_f32 v33, v33\n"
    "v_exp_f32 v34, v34\n"
    "v_exp_f32 v35, v35\n"
    "v_mfma_f32_32x32x16_bf16 v[16:31], v[164:167], a[28:31], v[16:31]\n"
    "buffer_load_dwordx4 v[236:239], v2, s[4:7], s44 offen offset:0 sc1\n"
    "s_add_u32 s97, s41, 64\n"
    "v_exp_f32 v36, v36\n"
    "v_exp_f32 v37, v37\n"
    "v_exp_f32 v38, v38\n"
    "v_exp_f32 v39, v39\n"
    "v_cvt_pk_bf16_f32 v56, v32, v33\n"
    "v_cvt_pk_bf16_f32 v57, v34, v35\n"
    "v_mfma_f32_32x32x16_bf16 v[80:95], v[168:171], v[48:51], v[80:95]\n"
    "buffer_load_dwordx4 a[240:243], v2, s[4:7], s44 offen offset:1024 sc1\n"
    "s_mov_b32 s98, 32832\n"
    "v_exp_f32 v40, v40\n"
    "v_exp_f32 v41, v41\n"
    "v_exp_f32 v42, v42\n"
    "v_exp_f32 v43, v43\n"
    "v_cvt_pk_bf16_f32 v58, v36, v37\n"
    "v_cvt_pk_bf16_f32 v59, v38, v39\n"
    "v_mfma_f32_32x32x16_bf16 v[80:95], v[172:175], v[52:55], v[80:95]\n"
    "s_mov_b32 s99, 49216\n"
    "v_exp_f32 v44, v44\n"
    "v_exp_f32 v45, v45\n"
    "v_exp_f32 v46, v46\n"
    "v_exp_f32 v47, v47\n"
    "v_cvt_pk_bf16_f32 v60, v40, v41\n"
    "v_cvt_pk_bf16_f32 v61, v42, v43\n"
    "v_cvt_pk_bf16_f32 v62, v44, v45\n"
    "v_cvt_pk_bf16_f32 v63, v46, v47\n"
    "v_mfma_f32_32x32x16_bf16 v[32:47], v[160:163], a[0:3], 0\n"
    "buffer_load_dwordx4 a[244:247], v2, s[4:7], s44 offen offset:2048 sc1\n"
    "s_mov_b32 s50, 49152\n"
    "v_exp_f32 v16, v16\n"
    "v_exp_f32 v17, v17\n"
    "v_exp_f32 v18, v18\n"
    "v_exp_f32 v19, v19\n"
    "v_mfma_f32_32x32x16_bf16 v[32:47], v[164:167], a[4:7], v[32:47]\n"
    "v_max_u32 v248, v248, v166\n"
    "buffer_load_dwordx4 a[96:99], v0, s[4:7], s57 offen offset:0 sc1\n"
    "s_mov_b32 s31, 65536\n"
    "v_exp_f32 v20, v20\n"
    "v_exp_f32 v21, v21\n"
    "v_exp_f32 v22, v22\n"
    "v_exp_f32 v23, v23\n"
    "v_cvt_pk_bf16_f32 v48, v16, v17\n"
    "v_cvt_pk_bf16_f32 v49, v18, v19\n"
    "v_mfma_f32_32x32x16_bf16 v[96:111], v[168:171], v[56:59], v[96:111]\n"
    "buffer_load_dwordx4 a[100:103], v0, s[4:7], s58 offen offset:0 sc1\n"
    "s_xor_b32 s36, s18, 1\n"
    "v_exp_f32 v24, v24\n"
    "v_exp_f32 v25, v25\n"
    "v_exp_f32 v26, v26\n"
    "v_exp_f32 v27, v27\n"
    "v_cvt_pk_bf16_f32 v50, v20, v21\n"
    "v_cvt_pk_bf16_f32 v51, v22, v23\n"
    "v_mfma_f32_32x32x16_bf16 v[96:111], v[172:175], v[60:63], v[96:111]\n"
    "s_waitcnt vmcnt(24)\n"
    "buffer_load_dwordx4 a[104:107], v0, s[4:7], s57 offen offset:2048 sc1\n"
    "v_exp_f32 v28, v28\n"
    "v_exp_f32 v29, v29\n"
    "v_exp_f32 v30, v30\n"
    "v_exp_f32 v31, v31\n"
    "v_cvt_pk_bf16_f32 v52, v24, v25\n"
    "v_cvt_pk_bf16_f32 v53, v26, v27\n"
    "v_cvt_pk_bf16_f32 v54, v28, v29\n"
    "v_cvt_pk_bf16_f32 v55, v30, v31\n"
    "v_mfma_f32_32x32x16_bf16 v[16:31], v[176:179], a[8:11], 0\n"
    "s_mul_i32 s36, s36, 12288\n"
    "v_exp_f32 v32, v32\n"
    "v_exp_f32 v33, v33\n"
    "v_exp_f32 v34, v34\n"
    "v_exp_f32 v35, v35\n"
    "v_mfma_f32_32x32x16_bf16 v[16:31], v[180:183], a[12:15], v[16:31]\n"
    "buffer_load_dwordx4 a[108:111], v0, s[4:7], s58 offen offset:2048 sc1\n"
    "s_xor_b32 s37, s18, 2\n"
    "v_exp_f32 v36, v36\n"
    "v_exp_f32 v37, v37\n"
    "v_exp_f32 v38, v38\n"
    "v_exp_f32 v39, v39\n"
    "v_cvt_pk_bf16_f32 v56, v32, v33\n"
    "v_cvt_pk_bf16_f32 v57, v34, v35\n"
    "v_mfma_f32_32x32x16_bf16 v[112:127], v[168:171], v[48:51], v[112:127]\n"
    "buffer_load_dwordx4 a[112:115], v2, s[4:7], s57 offen offset:0 sc1\n"
    "s_mul_i32 s37, s37, 12288\n"
    "v_exp_f32 v40, v40\n"
    "v_exp_f32 v41, v41\n"
    "v_exp_f32 v42, v42\n"
    "v_exp_f32 v43, v43\n"
    "v_cvt_pk_bf16_f32 v58, v36, v37\n"
    "v_cvt_pk_bf16_f32 v59, v38, v39\n"
    "v_mfma_f32_32x32x16_bf16 v[112:127], v[172:175], v[52:55], v[112:127]\n"
    "buffer_load_dwordx4 a[116:119], v2, s[4:7], s58 offen offset:0 sc1\n"
    "s_add_u32 s37, s37, 4096\n"
    "v_exp_f32 v44, v44\n"
    "v_exp_f32 v45, v45\n"
    "v_exp_f32 v46, v46\n"
    "v_exp_f32 v47, v47\n"
    "v_cvt_pk_bf16_f32 v60, v40, v41\n"
    "v_cvt_pk_bf16_f32 v61, v42, v43\n"
    "v_cvt_pk_bf16_f32 v62, v44, v45\n"
    "v_cvt_pk_bf16_f32 v63, v46, v47\n"
    "v_mfma_f32_32x32x16_bf16 v[32:47], v[176:179], a[16:19], 0\n"
    "buffer_load_dwordx4 a[120:123], v2, s[4:7], s57 offen offset:2048 sc1\n"
    "s_xor_b32 s38, s18, 3\n"
    "v_exp_f32 v16, v16\n"
    "v_exp_f32 v17, v17\n"
    "v_exp_f32 v18, v18\n"
    "v_exp_f32 v19, v19\n"
    "v_mfma_f32_32x32x16_bf16 v[32:47], v[180:183], a[20:23], v[32:47]\n"
    "buffer_load_dwordx4 a[124:127], v2, s[4:7], s58 offen offset:2048 sc1\n"
    "s_mul_i32 s38, s38, 12288\n"
    "v_exp_f32 v20, v20\n"
    "v_exp_f32 v21, v21\n"
    "v_exp_f32 v22, v22\n"
    "v_exp_f32 v23, v23\n"
    "v_cvt_pk_bf16_f32 v48, v16, v17\n"
    "v_cvt_pk_bf16_f32 v49, v18, v19\n"
    "v_mfma_f32_32x32x16_bf16 v[64:79], v[168:171], v[56:59], v[64:79]\n"
    "s_add_u32 s38, s38, 8192\n"
    "v_exp_f32 v24, v24\n"
    "v_exp_f32 v25, v25\n"
    "v_exp_f32 v26, v26\n"
    "v_exp_f32 v27, v27\n"
    "v_cvt_pk_bf16_f32 v50, v20, v21\n"
    "v_cvt_pk_bf16_f32 v51, v22, v23\n"
    "v_mfma_f32_32x32x16_bf16 v[64:79], v[172:175], v[60:63], v[64:79]\n"
    "buffer_load_dwordx4 a[128:131], v3, s[4:7], s57 offen offset:0 sc1\n"
    "s_lshl_b32 s39, s18, 2\n"
    "v_exp_f32 v28, v28\n"
    "v_exp_f32 v29, v29\n"
    "v_exp_f32 v30, v30\n"
    "v_exp_f32 v31, v31\n"
    "v_cvt_pk_bf16_f32 v52, v24, v25\n"
    "v_cvt_pk_bf16_f32 v53, v26, v27\n"
    "v_cvt_pk_bf16_f32 v54, v28, v29\n"
    "v_cvt_pk_bf16_f32 v55, v30, v31\n"
    "v_mfma_f32_32x32x16_bf16 v[16:31], v[176:179], a[24:27], 0\n"
    "buffer_load_dwordx4 a[132:135], v3, s[4:7], s58 offen offset:0 sc1\n"
    "s_add_u32 s39, s39, 155904\n"
    "v_exp_f32 v32, v32\n"
    "v_exp_f32 v33, v33\n"
    "v_exp_f32 v34, v34\n"
    "v_exp_f32 v35, v35\n"
    "v_mfma_f32_32x32x16_bf16 v[16:31], v[180:183], a[28:31], v[16:31]\n"
    "buffer_load_dwordx4 a[136:139], v3, s[4:7], s57 offen offset:2048 sc1\n"
    "v_exp_f32 v36, v36\n"
    "v_exp_f32 v37, v37\n"
    "v_exp_f32 v38, v38\n"
    "v_exp_f32 v39, v39\n"
    "v_cvt_pk_bf16_f32 v56, v32, v33\n"
    "v_cvt_pk_bf16_f32 v57, v34, v35\n"
    "v_mfma_f32_32x32x16_bf16 v[80:95], v[184:187], v[48:51], v[80:95]\n"
    "buffer_load_dwordx4 a[140:143], v3, s[4:7], s58 offen offset:2048 sc1\n"
    "v_exp_f32 v40, v40\n"
    "v_exp_f32 v41, v41\n"
    "v_exp_f32 v42, v42\n"
    "v_exp_f32 v43, v43\n"
    "v_cvt_pk_bf16_f32 v58, v36, v37\n"
    "v_cvt_pk_bf16_f32 v59, v38, v39\n"
    "v_mfma_f32_32x32x16_bf16 v[80:95], v[188:191], v[52:55], v[80:95]\n"
    "v_exp_f32 v44, v44\n"
    "v_exp_f32 v45, v45\n"
    "v_exp_f32 v46, v46\n"
    "v_exp_f32 v47, v47\n"
    "v_cvt_pk_bf16_f32 v60, v40, v41\n"
    "v_cvt_pk_bf16_f32 v61, v42, v43\n"
    "v_cvt_pk_bf16_f32 v62, v44, v45\n"
    "v_cvt_pk_bf16_f32 v63, v46, v47\n"
    "v_mfma_f32_32x32x16_bf16 v[32:47], v[176:179], a[0:3], 0\n"
    "v_exp_f32 v16, v16\n"
    "v_exp_f32 v17, v17\n"
    "v_exp_f32 v18, v18\n"
    "v_exp_f32 v19, v19\n"
    "ds_write_b128 v251, v[80:83] offset:0\n"
    "v_mfma_f32_32x32x16_bf16 v[32:47], v[180:183], a[4:7], v[32:47]\n"
    "v_max_u32 v248, v248, v182\n"
    "v_exp_f32 v20, v20\n"
    "v_exp_f32 v21, v21\n"
    "v_exp_f32 v22, v22\n"
    "v_exp_f32 v23, v23\n"
    "v_cvt_pk_bf16_f32 v48, v16, v17\n"
    "v_cvt_pk_bf16_f32 v49, v18, v19\n"
    "ds_write_b128 v251, v[84:87] offset:1024\n"
    "v_mfma_f32_32x32x16_bf16 v[96:111], v[184:187], v[56:59], v[96:111]\n"
    "v_exp_f32 v24, v24\n"
    "v_exp_f32 v25, v25\n"
    "v_exp_f32 v26, v26\n"
    "v_exp_f32 v27, v27\n"
    "v_cvt_pk_bf16_f32 v50, v20, v21\n"
    "v_cvt_pk_bf16_f32 v51, v22, v23\n"
    "ds_write_b128 v251, v[88:91] offset:2048\n"
    "v_mfma_f32_32x32x16_bf16 v[96:111], v[188:191], v[60:63], v[96:111]\n"
    "v_exp_f32 v28, v28\n"
    "v_exp_f32 v29, v29\n"
    "v_exp_f32 v30, v30\n"
    "v_exp_f32 v31, v31\n"
    "v_cvt_pk_bf16_f32 v52, v24, v25\n"
    "v_cvt_pk_bf16_f32 v53, v26, v27\n"
    "ds_write_b128 v251, v[92:95] offset:3072\n"
    "v_cvt_pk_bf16_f32 v54, v28, v29\n"
    "v_cvt_pk_bf16_f32 v55, v30, v31\n"
    "v_exp_f32 v32, v32\n"
    "v_exp_f32 v33, v33\n"
    "v_exp_f32 v34, v34\n"
    "v_exp_f32 v35, v35\n"
    "ds_write_b128 v252, v[96:99] offset:0\n"
    "v_exp_f32 v36, v36\n"
    "v_exp_f32 v37, v37\n"
    "v_exp_f32 v38, v38\n"
    "v_exp_f32 v39, v39\n"
    "v_cvt_pk_bf16_f32 v56, v32, v33\n"
    "v_cvt_pk_bf16_f32 v57, v34, v35\n"
    "ds_write_b128 v252, v[100:103] offset:1024\n"
    "v_mfma_f32_32x32x16_bf16 v[112:127], v[184:187], v[48:51], v[112:127]\n"
    "v_exp_f32 v40, v40\n"
    "v_exp_f32 v41, v41\n"
    "v_exp_f32 v42, v42\n"
    "v_exp_f32 v43, v43\n"
    "v_cvt_pk_bf16_f32 v58, v36, v37\n"
    "v_cvt_pk_bf16_f32 v59, v38, v39\n"
    "ds_write_b128 v252, v[104:107] offset:2048\n"
    "v_mfma_f32_32x32x16_bf16 v[112:127], v[188:191], v[52:55], v[112:127]\n"
    "v_exp_f32 v44, v44\n"
    "v_exp_f32 v45, v45\n"
    "v_exp_f32 v46, v46\n"
    "v_exp_f32 v47, v47\n"
    "v_cvt_pk_bf16_f32 v60, v40, v41\n"
    "v_cvt_pk_bf16_f32 v61, v42, v43\n"
    "ds_write_b128 v252, v[108:111] offset:3072\n"
    "v_cvt_pk_bf16_f32 v62, v44, v45\n"
    "v_cvt_pk_bf16_f32 v63, v46, v47\n"
    "v_mfma_f32_32x32x16_bf16 v[64:79], v[184:187], v[56:59], v[64:79]\n"
    "v_accvgpr_read_b32 v8, a7\n"
    "v_accvgpr_read_b32 v9, a15\n"
    "v_accvgpr_read_b32 v10, a23\n"
    "v_accvgpr_read_b32 v11, a31\n"
    "v_and_b32 v12, 0xffff0000, v248\n"
    "v_mfma_f32_32x32x16_bf16 v[64:79], v[188:191], v[60:63], v[64:79]\n"
    "v_max_u32 v8, v8, v9\n"
    "v_max_u32 v10, v10, v11\n"
    "v_max_u32 v8, v8, v10\n"
    "v_lshlrev_b32 v8, 16, v8\n"
    "v_max_u32_dpp v12, v12, v12 quad_perm:[1,0,3,2] row_mask:0xf bank_mask:0xf\n"
    "ds_write_b128 v253, v[112:115] offset:0\n"
    "s_nop 0\n"
    "v_max_u32_dpp v12, v12, v12 quad_perm:[2,3,0,1] row_mask:0xf bank_mask:0xf\n"
    "ds_write_b128 v253, v[116:119] offset:1024\n"
    "s_nop 0\n"
    "v_max_u32_dpp v12, v12, v12 row_half_mirror row_mask:0xf bank_mask:0xf\n"
    "ds_write_b128 v253, v[120:123] offset:2048\n"
    "s_nop 0\n"
    "v_max_u32_dpp v12, v12, v12 row_mirror row_mask:0xf bank_mask:0xf\n"
    "ds_write_b128 v253, v[124:127] offset:3072\n"
    "v_mov_b32 v10, v12\n"
    "s_nop 1\n"
    "v_permlane16_swap_b32 v12, v10\n"
    "v_max_u32 v12, v12, v10\n"
    "v_mul_f32 v8, v8, v12\n"
    "v_cmp_nge_f32 vcc, 0x42200000, v8\n"
    "s_nop 0\n"
    "s_and_b64 vcc, vcc, s[78:79]\n"
    "s_cselect_b32 s52, 1, 0\n"
    "v_mov_b32 v8, s52\n"
    "v_mov_b32 v9, s39\n"
    "ds_write_b32 v9, v8\n"
    "v_mov_b32 v13, 155904\n"
    "s_waitcnt lgkmcnt(0)\n"
    "s_barrier\n"
    "ds_read_b128 v[8:11], v13\n"
    "Ljoin_19%=:\n"
    "ds_read_b128 v[16:19], v250 offset:0\n"
    "ds_read_b128 v[20:23], v250 offset:1024\n"
    "ds_read_b128 v[24:27], v250 offset:2048\n"
    "ds_read_b128 v[28:31], v250 offset:3072\n"
    "ds_read_b128 v[32:35], v250 offset:4096\n"
    "ds_read_b128 v[36:39], v250 offset:5120\n"
    "ds_read_b128 v[40:43], v250 offset:6144\n"
    "ds_read_b128 v[44:47], v250 offset:7168\n"
    "ds_read_b128 v[48:51], v250 offset:8192\n"
    "ds_read_b128 v[52:55], v250 offset:9216\n"
    "ds_read_b128 v[56:59], v250 offset:10240\n"
    "ds_read_b128 v[60:63], v250 offset:11264\n"
    "s_waitcnt lgkmcnt(12)\n"
    "v_or3_b32 v8, v8, v9, v10\n"
    "v_or_b32 v8, v8, v11\n"
    "s_nop 0\n"
    "v_readfirstlane_b32 s52, v8\n"
    "s_nop 0\n"
    "s_cmp_lg_u32 s52, 0\n"
    "s_cbranch_scc1 Lfallback_18%=\n"
    "s_waitcnt lgkmcnt(11)\n"
    "v_pk_add_f32 v[64:65], v[64:65], v[16:17]\n"
    "v_pk_add_f32 v[66:67], v[66:67], v[18:19]\n"
    "s_waitcnt lgkmcnt(10)\n"
    "v_pk_add_f32 v[68:69], v[68:69], v[20:21]\n"
    "v_pk_add_f32 v[70:71], v[70:71], v[22:23]\n"
    "s_waitcnt lgkmcnt(9)\n"
    "v_pk_add_f32 v[72:73], v[72:73], v[24:25]\n"
    "v_pk_add_f32 v[74:75], v[74:75], v[26:27]\n"
    "s_waitcnt lgkmcnt(8)\n"
    "v_pk_add_f32 v[76:77], v[76:77], v[28:29]\n"
    "v_pk_add_f32 v[78:79], v[78:79], v[30:31]\n"
    "s_waitcnt lgkmcnt(3)\n"
    "v_pk_add_f32 v[32:33], v[32:33], v[48:49]\n"
    "v_pk_add_f32 v[34:35], v[34:35], v[50:51]\n"
    "s_waitcnt lgkmcnt(2)\n"
    "v_pk_add_f32 v[36:37], v[36:37], v[52:53]\n"
    "v_pk_add_f32 v[38:39], v[38:39], v[54:55]\n"
    "s_waitcnt lgkmcnt(1)\n"
    "v_pk_add_f32 v[40:41], v[40:41], v[56:57]\n"
    "v_pk_add_f32 v[42:43], v[42:43], v[58:59]\n"
    "s_waitcnt lgkmcnt(0)\n"
    "v_pk_add_f32 v[44:45], v[44:45], v[60:61]\n"
    "v_pk_add_f32 v[46:47], v[46:47], v[62:63]\n"
    "v_pk_add_f32 v[64:65], v[64:65], v[32:33]\n"
    "v_pk_add_f32 v[66:67], v[66:67], v[34:35]\n"
    "v_pk_add_f32 v[68:69], v[68:69], v[36:37]\n"
    "v_pk_add_f32 v[70:71], v[70:71], v[38:39]\n"
    "v_pk_add_f32 v[72:73], v[72:73], v[40:41]\n"
    "v_pk_add_f32 v[74:75], v[74:75], v[42:43]\n"
    "v_pk_add_f32 v[76:77], v[76:77], v[44:45]\n"
    "v_pk_add_f32 v[78:79], v[78:79], v[46:47]\n"
    "v_mov_b32 v8, v76\n"
    "v_mov_b32 v9, v76\n"
    "s_nop 1\n"
    "v_permlane32_swap_b32 v8, v9\n"
    "v_rcp_f32 v8, v9\n"
    "v_cndmask_b32 v76, v76, 0, s[78:79]\n"
    "v_mov_b32 v9, v8\n"
    "v_pk_mul_f32 v[64:65], v[64:65], v[8:9]\n"
    "v_pk_mul_f32 v[66:67], v[66:67], v[8:9]\n"
    "v_pk_mul_f32 v[68:69], v[68:69], v[8:9]\n"
    "v_pk_mul_f32 v[70:71], v[70:71], v[8:9]\n"
    "v_pk_mul_f32 v[72:73], v[72:73], v[8:9]\n"
    "v_pk_mul_f32 v[74:75], v[74:75], v[8:9]\n"
    "v_pk_mul_f32 v[76:77], v[76:77], v[8:9]\n"
    "v_pk_mul_f32 v[78:79], v[78:79], v[8:9]\n"
    "v_cvt_pk_bf16_f32 v16, v64, v65\n"
    "v_cvt_pk_bf16_f32 v17, v66, v67\n"
    "v_cvt_pk_bf16_f32 v18, v68, v69\n"
    "v_cvt_pk_bf16_f32 v19, v70, v71\n"
    "buffer_store_dwordx4 v[16:19], v0, s[4:7], s64 offen sc1\n"
    "v_cvt_pk_bf16_f32 v20, v72, v73\n"
    "v_cvt_pk_bf16_f32 v21, v74, v75\n"
    "v_cvt_pk_bf16_f32 v22, v76, v77\n"
    "v_cvt_pk_bf16_f32 v23, v78, v79\n"
    "buffer_store_dwordx4 v[20:23], v0, s[4:7], s64 offen offset:1024 sc1\n"
    "v_add_u32 v251, s36, v0\n"
    "v_add_u32 v252, s37, v0\n"
    "v_add_u32 v253, s38, v0\n"
    "v_lshrrev_b32 v8, 5, v1\n"
    "v_lshlrev_b32 v9, 4, v8\n"
    "v_add_u32 v248, s28, v9\n"
    "v_mul_u32_u24 v8, 1792, v8\n"
    "v_add_u32 v6, v5, v8\n"
    "v_lshlrev_b32 v15, 3, v1\n"
    "v_min_u32 v15, 48, v15\n"
    "v_add_u32 v15, s29, v15\n"
    "v_add_u32 v7, s56, v0\n"
    "v_lshlrev_b32 v12, 3, v1\n"
    "s_add_u32 s44, s66, 32768\n"
    "v_add_u32 v12, s44, v12\n"
    "v_mov_b32 v254, s16\n"
    "v_mov_b32 v255, s17\n"
    "s_waitcnt vmcnt(0)\n"
    "s_mov_b64 exec, 1\n"
    "buffer_atomic_swap_x2 v[254:255], off, s[4:7], s3 sc1\n"
    "s_mov_b64 exec, -1\n"
    "s_barrier\n"
    "s_cmp_ge_u32 s20, 28\n"
    "s_cbranch_scc1 Lext_20%=\n"
    "ds_read_b128 v[16:19], v248 offset:0\n"
    "ds_read_b128 v[20:23], v248 offset:32\n"
    "ds_read_b128 v[24:27], v248 offset:64\n"
    "ds_read_b128 v[28:31], v248 offset:96\n"
    "ds_read_b128 v[32:35], v248 offset:256\n"
    "ds_read_b128 v[36:39], v248 offset:288\n"
    "ds_read_b128 v[40:43], v248 offset:320\n"
    "ds_read_b128 v[44:47], v248 offset:352\n"
    "v_lshlrev_b32 v48, 16, v240\n"
    "v_and_b32 v49, 0xffff0000, v240\n"
    "v_lshlrev_b32 v50, 16, v241\n"
    "v_and_b32 v51, 0xffff0000, v241\n"
    "v_lshlrev_b32 v52, 16, v242\n"
    "v_and_b32 v53, 0xffff0000, v242\n"
    "v_lshlrev_b32 v54, 16, v243\n"
    "v_and_b32 v55, 0xffff0000, v243\n"
    "v_lshlrev_b32 v56, 16, v244\n"
    "v_and_b32 v57, 0xffff0000, v244\n"
    "v_lshlrev_b32 v58, 16, v245\n"
    "v_and_b32 v59, 0xffff0000, v245\n"
    "v_lshlrev_b32 v60, 16, v246\n"
    "v_and_b32 v61, 0xffff0000, v246\n"
    "v_lshlrev_b32 v62, 16, v247\n"
    "v_and_b32 v63, 0xffff0000, v247\n"
    "s_mov_b32 s70, 0\n"
    "s_mov_b64 exec, 255\n"
    "s_nop 0\n"
    "Lpoll_22%=:\n"
    "v_mov_b32 v8, 0\n"
    "v_mov_b32 v9, 0\n"
    "buffer_atomic_or_x2 v[8:9], v12, s[4:7], 0 offen sc0 sc1\n"
    "s_waitcnt vmcnt(0)\n"
    "v_cmp_ne_u64 vcc, s[16:17], v[8:9]\n"
    "s_nop 0\n"
    "s_and_b64 exec, exec, vcc\n"
    "s_cbranch_scc0 Lpollok_23%=\n"
    "s_sleep 2\n"
    "s_add_u32 s70, s70, 1\n"
    "s_cmp_lt_u32 s70, 0x40000\n"
    "s_cbranch_scc1 Lpoll_22%=\n"
    "Lpollok_23%=:\n"
    "s_mov_b64 exec, -1\n"
    "buffer_load_dwordx4 v[128:131], v0, s[4:7], s60 offen offset:0 sc1\n"
    "buffer_load_dwordx4 v[132:135], v0, s[4:7], s60 offen offset:1024 sc1\n"
    "buffer_load_dwordx4 v[136:139], v0, s[4:7], s60 offen offset:2048 sc1\n"
    "buffer_load_dwordx4 v[140:143], v0, s[4:7], s60 offen offset:3072 sc1\n"
    "buffer_load_dwordx4 v[144:147], v2, s[4:7], s60 offen offset:0 sc1\n"
    "buffer_load_dwordx4 v[148:151], v2, s[4:7], s60 offen offset:1024 sc1\n"
    "buffer_load_dwordx4 v[152:155], v2, s[4:7], s60 offen offset:2048 sc1\n"
    "buffer_load_dwordx4 v[156:159], v2, s[4:7], s60 offen offset:3072 sc1\n"
    "buffer_load_dwordx4 v[160:163], v0, s[4:7], s61 offen offset:0 sc1\n"
    "buffer_load_dwordx4 v[164:167], v0, s[4:7], s61 offen offset:1024 sc1\n"
    "s_waitcnt vmcnt(9)\n"
    "v_mfma_f32_32x32x16_bf16 v[96:111], a[32:35], v[128:131], 0\n"
    "buffer_load_dwordx4 v[168:171], v0, s[4:7], s61 offen offset:2048 sc1\n"
    "v_mfma_f32_32x32x16_bf16 v[112:127], a[36:39], v[128:131], 0\n"
    "s_waitcnt vmcnt(9)\n"
    "v_mfma_f32_32x32x16_bf16 v[96:111], a[40:43], v[132:135], v[96:111]\n"
    "buffer_load_dwordx4 v[172:175], v0, s[4:7], s61 offen offset:3072 sc1\n"
    "v_mfma_f32_32x32x16_bf16 v[112:127], a[44:47], v[132:135], v[112:127]\n"
    "s_waitcnt vmcnt(9)\n"
    "v_mfma_f32_32x32x16_bf16 v[96:111], a[48:51], v[136:139], v[96:111]\n"
    "buffer_load_dwordx4 v[176:179], v2, s[4:7], s61 offen offset:0 sc1\n"
    "v_mfma_f32_32x32x16_bf16 v[112:127], a[52:55], v[136:139], v[112:127]\n"
    "s_waitcnt vmcnt(9)\n"
    "v_mfma_f32_32x32x16_bf16 v[96:111], a[56:59], v[140:143], v[96:111]\n"
    "buffer_load_dwordx4 v[180:183], v2, s[4:7], s61 offen offset:1024 sc1\n"
    "v_mfma_f32_32x32x16_bf16 v[112:127], a[60:63], v[140:143], v[112:127]\n"
    "s_waitcnt vmcnt(9)\n"
    "v_mfma_f32_32x32x16_bf16 v[96:111], a[64:67], v[144:147], v[96:111]\n"
    "buffer_load_dwordx4 v[184:187], v2, s[4:7], s61 offen offset:2048 sc1\n"
    "v_mfma_f32_32x32x16_bf16 v[112:127], a[68:71], v[144:147], v[112:127]\n"
    "s_waitcnt vmcnt(9)\n"
    "v_mfma_f32_32x32x16_bf16 v[96:111], a[72:75], v[148:151], v[96:111]\n"
    "buffer_load_dwordx4 v[188:191], v2, s[4:7], s61 offen offset:3072 sc1\n"
    "v_mfma_f32_32x32x16_bf16 v[112:127], a[76:79], v[148:151], v[112:127]\n"
    "s_waitcnt vmcnt(9)\n"
    "v_mfma_f32_32x32x16_bf16 v[96:111], a[80:83], v[152:155], v[96:111]\n"
    "v_mfma_f32_32x32x16_bf16 v[112:127], a[84:87], v[152:155], v[112:127]\n"
    "s_waitcnt vmcnt(8)\n"
    "v_mfma_f32_32x32x16_bf16 v[96:111], a[88:91], v[156:159], v[96:111]\n"
    "v_mfma_f32_32x32x16_bf16 v[112:127], a[92:95], v[156:159], v[112:127]\n"
    "s_waitcnt vmcnt(7)\n"
    "v_mfma_f32_32x32x16_bf16 v[80:95], a[36:39], v[160:163], 0\n"
    "v_mfma_f32_32x32x16_bf16 v[64:79], a[32:35], v[160:163], 0\n"
    "s_waitcnt vmcnt(6)\n"
    "v_mfma_f32_32x32x16_bf16 v[80:95], a[44:47], v[164:167], v[80:95]\n"
    "v_mfma_f32_32x32x16_bf16 v[64:79], a[40:43], v[164:167], v[64:79]\n"
    "s_waitcnt vmcnt(5)\n"
    "v_mfma_f32_32x32x16_bf16 v[80:95], a[52:55], v[168:171], v[80:95]\n"
    "s_nop 2\n"
    "ds_write_b128 v252, v[96:99] offset:0\n"
    "v_mfma_f32_32x32x16_bf16 v[64:79], a[48:51], v[168:171], v[64:79]\n"
    "s_waitcnt vmcnt(4)\n"
    "v_mfma_f32_32x32x16_bf16 v[80:95], a[60:63], v[172:175], v[80:95]\n"
    "ds_write_b128 v252, v[100:103] offset:1024\n"
    "v_mfma_f32_32x32x16_bf16 v[64:79], a[56:59], v[172:175], v[64:79]\n"
    "s_waitcnt vmcnt(3)\n"
    "v_mfma_f32_32x32x16_bf16 v[80:95], a[68:71], v[176:179], v[80:95]\n"
    "ds_write_b128 v252, v[104:107] offset:2048\n"
    "v_mfma_f32_32x32x16_bf16 v[64:79], a[64:67], v[176:179], v[64:79]\n"
    "s_waitcnt vmcnt(2)\n"
    "v_mfma_f32_32x32x16_bf16 v[80:95], a[76:79], v[180:183], v[80:95]\n"
    "ds_write_b128 v252, v[108:111] offset:3072\n"
    "v_mfma_f32_32x32x16_bf16 v[64:79], a[72:75], v[180:183], v[64:79]\n"
    "s_waitcnt vmcnt(1)\n"
    "v_mfma_f32_32x32x16_bf16 v[80:95], a[84:87], v[184:187], v[80:95]\n"
    "ds_write_b128 v253, v[112:115] offset:0\n"
    "v_mfma_f32_32x32x16_bf16 v[64:79], a[80:83], v[184:187], v[64:79]\n"
    "ds_write_b128 v253, v[116:119] offset:1024\n"
    "s_waitcnt vmcnt(0)\n"
    "v_mfma_f32_32x32x16_bf16 v[80:95], a[92:95], v[188:191], v[80:95]\n"
    "ds_write_b128 v253, v[120:123] offset:2048\n"
    "v_mfma_f32_32x32x16_bf16 v[64:79], a[88:91], v[188:191], v[64:79]\n"
    "ds_write_b128 v253, v[124:127] offset:3072\n"
    "s_nop 8\n"
    "ds_write_b128 v251, v[80:83] offset:0\n"
    "ds_write_b128 v251, v[84:87] offset:1024\n"
    "ds_write_b128 v251, v[88:91] offset:2048\n"
    "ds_write_b128 v251, v[92:95] offset:3072\n"
    "s_waitcnt lgkmcnt(0)\n"
    "s_barrier\n"
    "ds_read_b128 v[128:131], v250 offset:0\n"
    "ds_read_b128 v[132:135], v250 offset:1024\n"
    "ds_read_b128 v[136:139], v250 offset:2048\n"
    "ds_read_b128 v[140:143], v250 offset:3072\n"
    "ds_read_b128 v[144:147], v250 offset:4096\n"
    "ds_read_b128 v[148:151], v250 offset:5120\n"
    "ds_read_b128 v[152:155], v250 offset:6144\n"
    "ds_read_b128 v[156:159], v250 offset:7168\n"
    "ds_read_b128 v[160:163], v250 offset:8192\n"
    "ds_read_b128 v[164:167], v250 offset:9216\n"
    "ds_read_b128 v[168:171], v250 offset:10240\n"
    "ds_read_b128 v[172:175], v250 offset:11264\n"
    "s_waitcnt lgkmcnt(11)\n"
    "v_pk_add_f32 v[64:65], v[64:65], v[128:129]\n"
    "v_pk_add_f32 v[66:67], v[66:67], v[130:131]\n"
    "s_waitcnt lgkmcnt(10)\n"
    "v_pk_add_f32 v[68:69], v[68:69], v[132:133]\n"
    "v_pk_add_f32 v[70:71], v[70:71], v[134:135]\n"
    "s_waitcnt lgkmcnt(9)\n"
    "v_pk_add_f32 v[72:73], v[72:73], v[136:137]\n"
    "v_pk_add_f32 v[74:75], v[74:75], v[138:139]\n"
    "s_waitcnt lgkmcnt(8)\n"
    "v_pk_add_f32 v[76:77], v[76:77], v[140:141]\n"
    "v_pk_add_f32 v[78:79], v[78:79], v[142:143]\n"
    "s_waitcnt lgkmcnt(3)\n"
    "v_pk_add_f32 v[144:145], v[144:145], v[160:161]\n"
    "v_pk_add_f32 v[146:147], v[146:147], v[162:163]\n"
    "s_waitcnt lgkmcnt(2)\n"
    "v_pk_add_f32 v[148:149], v[148:149], v[164:165]\n"
    "v_pk_add_f32 v[150:151], v[150:151], v[166:167]\n"
    "s_waitcnt lgkmcnt(1)\n"
    "v_pk_add_f32 v[152:153], v[152:153], v[168:169]\n"
    "v_pk_add_f32 v[154:155], v[154:155], v[170:171]\n"
    "s_waitcnt lgkmcnt(0)\n"
    "v_pk_add_f32 v[156:157], v[156:157], v[172:173]\n"
    "v_pk_add_f32 v[158:159], v[158:159], v[174:175]\n"
    "v_pk_add_f32 v[64:65], v[64:65], v[144:145]\n"
    "v_pk_add_f32 v[66:67], v[66:67], v[146:147]\n"
    "v_pk_add_f32 v[68:69], v[68:69], v[148:149]\n"
    "v_pk_add_f32 v[70:71], v[70:71], v[150:151]\n"
    "v_pk_add_f32 v[72:73], v[72:73], v[152:153]\n"
    "v_pk_add_f32 v[74:75], v[74:75], v[154:155]\n"
    "v_pk_add_f32 v[76:77], v[76:77], v[156:157]\n"
    "v_pk_add_f32 v[78:79], v[78:79], v[158:159]\n"
    "v_pk_fma_f32 v[48:49], v[64:65], v[16:17], v[48:49]\n"
    "v_pk_fma_f32 v[50:51], v[66:67], v[18:19], v[50:51]\n"
    "v_pk_fma_f32 v[52:53], v[68:69], v[20:21], v[52:53]\n"
    "v_pk_fma_f32 v[54:55], v[70:71], v[22:23], v[54:55]\n"
    "v_pk_fma_f32 v[56:57], v[72:73], v[24:25], v[56:57]\n"
    "v_pk_fma_f32 v[58:59], v[74:75], v[26:27], v[58:59]\n"
    "v_pk_fma_f32 v[60:61], v[76:77], v[28:29], v[60:61]\n"
    "v_pk_fma_f32 v[62:63], v[78:79], v[30:31], v[62:63]\n"
    "v_pk_mul_f32 v[32:33], v[32:33], v[48:49]\n"
    "v_pk_mul_f32 v[34:35], v[34:35], v[50:51]\n"
    "v_pk_mul_f32 v[36:37], v[36:37], v[52:53]\n"
    "v_pk_mul_f32 v[38:39], v[38:39], v[54:55]\n"
    "v_pk_mul_f32 v[40:41], v[40:41], v[56:57]\n"
    "v_pk_mul_f32 v[42:43], v[42:43], v[58:59]\n"
    "v_pk_mul_f32 v[44:45], v[44:45], v[60:61]\n"
    "v_pk_mul_f32 v[46:47], v[46:47], v[62:63]\n"
    "v_cvt_pk_bf16_f32 v16, v32, v33\n"
    "v_cvt_pk_bf16_f32 v17, v34, v35\n"
    "v_cvt_pk_bf16_f32 v18, v36, v37\n"
    "v_cvt_pk_bf16_f32 v19, v38, v39\n"
    "buffer_store_dwordx4 v[16:19], v0, s[4:7], s63 offen sc1\n"
    "v_cvt_pk_bf16_f32 v20, v40, v41\n"
    "v_cvt_pk_bf16_f32 v21, v42, v43\n"
    "v_cvt_pk_bf16_f32 v22, v44, v45\n"
    "v_cvt_pk_bf16_f32 v23, v46, v47\n"
    "buffer_store_dwordx4 v[20:23], v0, s[4:7], s63 offen offset:1024 sc1\n"
    "v_pk_add_f32 v[8:9], v[48:49], v[50:51]\n"
    "v_pk_mul_f32 v[10:11], v[48:49], v[48:49]\n"
    "v_pk_fma_f32 v[10:11], v[50:51], v[50:51], v[10:11]\n"
    "v_pk_add_f32 v[8:9], v[8:9], v[52:53]\n"
    "v_pk_fma_f32 v[10:11], v[52:53], v[52:53], v[10:11]\n"
    "v_pk_add_f32 v[8:9], v[8:9], v[54:55]\n"
    "v_pk_fma_f32 v[10:11], v[54:55], v[54:55], v[10:11]\n"
    "v_pk_add_f32 v[8:9], v[8:9], v[56:57]\n"
    "v_pk_fma_f32 v[10:11], v[56:57], v[56:57], v[10:11]\n"
    "v_pk_add_f32 v[8:9], v[8:9], v[58:59]\n"
    "v_pk_fma_f32 v[10:11], v[58:59], v[58:59], v[10:11]\n"
    "v_pk_add_f32 v[8:9], v[8:9], v[60:61]\n"
    "v_pk_fma_f32 v[10:11], v[60:61], v[60:61], v[10:11]\n"
    "v_pk_add_f32 v[8:9], v[8:9], v[62:63]\n"
    "v_pk_fma_f32 v[10:11], v[62:63], v[62:63], v[10:11]\n"
    "v_add_f32 v8, v8, v9\n"
    "v_add_f32 v9, v10, v11\n"
    "v_mov_b32 v10, v8\n"
    "v_mov_b32 v11, v9\n"
    "s_nop 0\n"
    "v_permlane32_swap_b32 v8, v10\n"
    "v_permlane32_swap_b32 v9, v11\n"
    "v_pk_add_f32 v[8:9], v[8:9], v[10:11]\n"
    "buffer_store_dwordx2 v[8:9], v5, s[4:7], s62 offen sc1\n"
    "v_cvt_pk_bf16_f32 v240, v48, v49\n"
    "v_cvt_pk_bf16_f32 v241, v50, v51\n"
    "v_cvt_pk_bf16_f32 v242, v52, v53\n"
    "v_cvt_pk_bf16_f32 v243, v54, v55\n"
    "v_cvt_pk_bf16_f32 v244, v56, v57\n"
    "v_cvt_pk_bf16_f32 v245, v58, v59\n"
    "v_cvt_pk_bf16_f32 v246, v60, v61\n"
    "v_cvt_pk_bf16_f32 v247, v62, v63\n"
    "s_barrier\n"
    "ds_write_b128 v7, a[240:243] offset:33792\n"
    "ds_write_b128 v7, a[244:247] offset:34816\n"
    "ds_write_b128 v7, v[192:195] offset:0\n"
    "v_mov_b32 v254, s16\n"
    "v_mov_b32 v255, s17\n"
    "s_waitcnt vmcnt(0)\n"
    "s_mov_b64 exec, 1\n"
    "buffer_atomic_swap_x2 v[254:255], off, s[4:7], s40 sc1\n"
    "s_mov_b64 exec, -1\n"
    "s_branch Lp4_21%=\n"
    "Lext_20%=:\n"
    "buffer_load_dwordx4 a[32:35], v0, s[4:7], s53 offen offset:0 sc1\n"
    "buffer_load_dwordx4 a[36:39], v0, s[4:7], s53 offen offset:1024 sc1\n"
    "buffer_load_dwordx4 a[40:43], v0, s[4:7], s53 offen offset:2048 sc1\n"
    "buffer_load_dwordx4 a[44:47], v0, s[4:7], s53 offen offset:3072 sc1\n"
    "buffer_load_dwordx4 a[48:51], v2, s[4:7], s53 offen offset:0 sc1\n"
    "buffer_load_dwordx4 a[52:55], v2, s[4:7], s53 offen offset:1024 sc1\n"
    "buffer_load_dwordx4 a[56:59], v2, s[4:7], s53 offen offset:2048 sc1\n"
    "buffer_load_dwordx4 a[60:63], v2, s[4:7], s53 offen offset:3072 sc1\n"
    "buffer_load_dwordx4 a[64:67], v3, s[4:7], s53 offen offset:0 sc1\n"
    "buffer_load_dwordx4 a[68:71], v3, s[4:7], s53 offen offset:1024 sc1\n"
    "buffer_load_dwordx4 a[72:75], v3, s[4:7], s53 offen offset:2048 sc1\n"
    "buffer_load_dwordx4 a[76:79], v3, s[4:7], s53 offen offset:3072 sc1\n"
    "buffer_load_dwordx4 a[80:83], v4, s[4:7], s53 offen offset:0 sc1\n"
    "buffer_load_dwordx4 a[84:87], v4, s[4:7], s53 offen offset:1024 sc1\n"
    "buffer_load_dwordx4 a[88:91], v4, s[4:7], s53 offen offset:2048 sc1\n"
    "buffer_load_dwordx4 a[92:95], v4, s[4:7], s53 offen offset:3072 sc1\n"
    "buffer_load_dwordx4 a[96:99], v0, s[4:7], s54 offen offset:0 sc1\n"
    "buffer_load_dwordx4 a[100:103], v0, s[4:7], s54 offen offset:1024 sc1\n"
    "buffer_load_dwordx4 a[104:107], v0, s[4:7], s54 offen offset:2048 sc1\n"
    "buffer_load_dwordx4 a[108:111], v0, s[4:7], s54 offen offset:3072 sc1\n"
    "buffer_load_dwordx4 a[112:115], v2, s[4:7], s54 offen offset:0 sc1\n"
    "buffer_load_dwordx4 a[116:119], v2, s[4:7], s54 offen offset:1024 sc1\n"
    "buffer_load_dwordx4 a[120:123], v2, s[4:7], s54 offen offset:2048 sc1\n"
    "buffer_load_dwordx4 a[124:127], v2, s[4:7], s54 offen offset:3072 sc1\n"
    "buffer_load_dwordx4 a[128:131], v3, s[4:7], s54 offen offset:0 sc1\n"
    "buffer_load_dwordx4 a[132:135], v3, s[4:7], s54 offen offset:1024 sc1\n"
    "buffer_load_dwordx4 a[136:139], v3, s[4:7], s54 offen offset:2048 sc1\n"
    "buffer_load_dwordx4 a[140:143], v3, s[4:7], s54 offen offset:3072 sc1\n"
    "v_and_b32 v8, 1, v1\n"
    "v_xor_b32 v8, 1, v8\n"
    "v_mul_u32_u24 v8, 1792, v8\n"
    "v_lshrrev_b32 v9, 5, v1\n"
    "v_lshl_add_u32 v8, v9, 4, v8\n"
    "v_add_u32 v8, 155968, v8\n"
    "ds_read_b128 v[16:19], v8 offset:0\n"
    "ds_read_b128 v[20:23], v8 offset:32\n"
    "ds_read_b128 v[28:31], v8 offset:64\n"
    "ds_read_b128 v[32:35], v8 offset:96\n"
    "ds_read_b128 v[40:43], v8 offset:128\n"
    "ds_read_b128 v[44:47], v8 offset:160\n"
    "s_waitcnt lgkmcnt(5)\n"
    "v_cvt_pk_bf16_f32 v24, v16, v17\n"
    "v_cvt_pk_bf16_f32 v25, v18, v19\n"
    "s_waitcnt lgkmcnt(4)\n"
    "v_cvt_pk_bf16_f32 v26, v20, v21\n"
    "v_cvt_pk_bf16_f32 v27, v22, v23\n"
    "ds_read_b128 v[52:55], v8 offset:192\n"
    "ds_read_b128 v[56:59], v8 offset:224\n"
    "s_waitcnt vmcnt(27)\n"
    "v_mfma_f32_32x32x16_bf16 v[64:79], a[32:35], v[24:27], 0\n"
    "s_waitcnt lgkmcnt(5)\n"
    "v_cvt_pk_bf16_f32 v36, v28, v29\n"
    "v_cvt_pk_bf16_f32 v37, v30, v31\n"
    "s_waitcnt lgkmcnt(4)\n"
    "v_cvt_pk_bf16_f32 v38, v32, v33\n"
    "v_cvt_pk_bf16_f32 v39, v34, v35\n"
    "ds_read_b128 v[16:19], v8 offset:256\n"
    "ds_read_b128 v[20:23], v8 offset:288\n"
    "s_waitcnt vmcnt(26)\n"
    "v_mfma_f32_32x32x16_bf16 v[64:79], a[36:39], v[36:39], v[64:79]\n"
    "s_waitcnt lgkmcnt(5)\n"
    "v_cvt_pk_bf16_f32 v48, v40, v41\n"
    "v_cvt_pk_bf16_f32 v49, v42, v43\n"
    "s_waitcnt lgkmcnt(4)\n"
    "v_cvt_pk_bf16_f32 v50, v44, v45\n"
    "v_cvt_pk_bf16_f32 v51, v46, v47\n"
    "ds_read_b128 v[28:31], v8 offset:320\n"
    "ds_read_b128 v[32:35], v8 offset:352\n"
    "s_waitcnt vmcnt(25)\n"
    "v_mfma_f32_32x32x16_bf16 v[64:79], a[40:43], v[48:51], v[64:79]\n"
    "s_waitcnt lgkmcnt(5)\n"
    "v_cvt_pk_bf16_f32 v60, v52, v53\n"
    "v_cvt_pk_bf16_f32 v61, v54, v55\n"
    "s_waitcnt lgkmcnt(4)\n"
    "v_cvt_pk_bf16_f32 v62, v56, v57\n"
    "v_cvt_pk_bf16_f32 v63, v58, v59\n"
    "ds_read_b128 v[40:43], v8 offset:384\n"
    "ds_read_b128 v[44:47], v8 offset:416\n"
    "s_waitcnt vmcnt(24)\n"
    "v_mfma_f32_32x32x16_bf16 v[64:79], a[44:47], v[60:63], v[64:79]\n"
    "s_waitcnt lgkmcnt(5)\n"
    "v_cvt_pk_bf16_f32 v24, v16, v17\n"
    "v_cvt_pk_bf16_f32 v25, v18, v19\n"
    "s_waitcnt lgkmcnt(4)\n"
    "v_cvt_pk_bf16_f32 v26, v20, v21\n"
    "v_cvt_pk_bf16_f32 v27, v22, v23\n"
    "ds_read_b128 v[52:55], v8 offset:448\n"
    "ds_read_b128 v[56:59], v8 offset:480\n"
    "s_waitcnt vmcnt(23)\n"
    "v_mfma_f32_32x32x16_bf16 v[64:79], a[48:51], v[24:27], v[64:79]\n"
    "s_waitcnt lgkmcnt(5)\n"
    "v_cvt_pk_bf16_f32 v36, v28, v29\n"
    "v_cvt_pk_bf16_f32 v37, v30, v31\n"
    "s_waitcnt lgkmcnt(4)\n"
    "v_cvt_pk_bf16_f32 v38, v32, v33\n"
    "v_cvt_pk_bf16_f32 v39, v34, v35\n"
    "ds_read_b128 v[16:19], v8 offset:512\n"
    "ds_read_b128 v[20:23], v8 offset:544\n"
    "s_waitcnt vmcnt(22)\n"
    "v_mfma_f32_32x32x16_bf16 v[64:79], a[52:55], v[36:39], v[64:79]\n"
    "s_waitcnt lgkmcnt(5)\n"
    "v_cvt_pk_bf16_f32 v48, v40, v41\n"
    "v_cvt_pk_bf16_f32 v49, v42, v43\n"
    "s_waitcnt lgkmcnt(4)\n"
    "v_cvt_pk_bf16_f32 v50, v44, v45\n"
    "v_cvt_pk_bf16_f32 v51, v46, v47\n"
    "ds_read_b128 v[28:31], v8 offset:576\n"
    "ds_read_b128 v[32:35], v8 offset:608\n"
    "s_waitcnt vmcnt(21)\n"
    "v_mfma_f32_32x32x16_bf16 v[64:79], a[56:59], v[48:51], v[64:79]\n"
    "s_waitcnt lgkmcnt(5)\n"
    "v_cvt_pk_bf16_f32 v60, v52, v53\n"
    "v_cvt_pk_bf16_f32 v61, v54, v55\n"
    "s_waitcnt lgkmcnt(4)\n"
    "v_cvt_pk_bf16_f32 v62, v56, v57\n"
    "v_cvt_pk_bf16_f32 v63, v58, v59\n"
    "ds_read_b128 v[40:43], v8 offset:640\n"
    "ds_read_b128 v[44:47], v8 offset:672\n"
    "s_waitcnt vmcnt(20)\n"
    "v_mfma_f32_32x32x16_bf16 v[64:79], a[60:63], v[60:63], v[64:79]\n"
    "s_waitcnt lgkmcnt(5)\n"
    "v_cvt_pk_bf16_f32 v24, v16, v17\n"
    "v_cvt_pk_bf16_f32 v25, v18, v19\n"
    "s_waitcnt lgkmcnt(4)\n"
    "v_cvt_pk_bf16_f32 v26, v20, v21\n"
    "v_cvt_pk_bf16_f32 v27, v22, v23\n"
    "ds_read_b128 v[52:55], v8 offset:704\n"
    "ds_read_b128 v[56:59], v8 offset:736\n"
    "s_waitcnt vmcnt(19)\n"
    "v_mfma_f32_32x32x16_bf16 v[64:79], a[64:67], v[24:27], v[64:79]\n"
    "s_waitcnt lgkmcnt(5)\n"
    "v_cvt_pk_bf16_f32 v36, v28, v29\n"
    "v_cvt_pk_bf16_f32 v37, v30, v31\n"
    "s_waitcnt lgkmcnt(4)\n"
    "v_cvt_pk_bf16_f32 v38, v32, v33\n"
    "v_cvt_pk_bf16_f32 v39, v34, v35\n"
    "ds_read_b128 v[16:19], v8 offset:768\n"
    "ds_read_b128 v[20:23], v8 offset:800\n"
    "s_waitcnt vmcnt(18)\n"
    "v_mfma_f32_32x32x16_bf16 v[64:79], a[68:71], v[36:39], v[64:79]\n"
    "s_waitcnt lgkmcnt(5)\n"
    "v_cvt_pk_bf16_f32 v48, v40, v41\n"
    "v_cvt_pk_bf16_f32 v49, v42, v43\n"
    "s_waitcnt lgkmcnt(4)\n"
    "v_cvt_pk_bf16_f32 v50, v44, v45\n"
    "v_cvt_pk_bf16_f32 v51, v46, v47\n"
    "ds_read_b128 v[28:31], v8 offset:832\n"
    "ds_read_b128 v[32:35], v8 offset:864\n"
    "s_waitcnt vmcnt(17)\n"
    "v_mfma_f32_32x32x16_bf16 v[64:79], a[72:75], v[48:51], v[64:79]\n"
    "s_waitcnt lgkmcnt(5)\n"
    "v_cvt_pk_bf16_f32 v60, v52, v53\n"
    "v_cvt_pk_bf16_f32 v61, v54, v55\n"
    "s_waitcnt lgkmcnt(4)\n"
    "v_cvt_pk_bf16_f32 v62, v56, v57\n"
    "v_cvt_pk_bf16_f32 v63, v58, v59\n"
    "ds_read_b128 v[40:43], v8 offset:896\n"
    "ds_read_b128 v[44:47], v8 offset:928\n"
    "s_waitcnt vmcnt(16)\n"
    "v_mfma_f32_32x32x16_bf16 v[64:79], a[76:79], v[60:63], v[64:79]\n"
    "s_waitcnt lgkmcnt(5)\n"
    "v_cvt_pk_bf16_f32 v24, v16, v17\n"
    "v_cvt_pk_bf16_f32 v25, v18, v19\n"
    "s_waitcnt lgkmcnt(4)\n"
    "v_cvt_pk_bf16_f32 v26, v20, v21\n"
    "v_cvt_pk_bf16_f32 v27, v22, v23\n"
    "ds_read_b128 v[52:55], v8 offset:960\n"
    "ds_read_b128 v[56:59], v8 offset:992\n"
    "s_waitcnt vmcnt(15)\n"
    "v_mfma_f32_32x32x16_bf16 v[64:79], a[80:83], v[24:27], v[64:79]\n"
    "s_waitcnt lgkmcnt(5)\n"
    "v_cvt_pk_bf16_f32 v36, v28, v29\n"
    "v_cvt_pk_bf16_f32 v37, v30, v31\n"
    "s_waitcnt lgkmcnt(4)\n"
    "v_cvt_pk_bf16_f32 v38, v32, v33\n"
    "v_cvt_pk_bf16_f32 v39, v34, v35\n"
    "ds_read_b128 v[16:19], v8 offset:1024\n"
    "ds_read_b128 v[20:23], v8 offset:1056\n"
    "s_waitcnt vmcnt(14)\n"
    "v_mfma_f32_32x32x16_bf16 v[64:79], a[84:87], v[36:39], v[64:79]\n"
    "s_waitcnt lgkmcnt(5)\n"
    "v_cvt_pk_bf16_f32 v48, v40, v41\n"
    "v_cvt_pk_bf16_f32 v49, v42, v43\n"
    "s_waitcnt lgkmcnt(4)\n"
    "v_cvt_pk_bf16_f32 v50, v44, v45\n"
    "v_cvt_pk_bf16_f32 v51, v46, v47\n"
    "ds_read_b128 v[28:31], v8 offset:1088\n"
    "ds_read_b128 v[32:35], v8 offset:1120\n"
    "s_waitcnt vmcnt(13)\n"
    "v_mfma_f32_32x32x16_bf16 v[64:79], a[88:91], v[48:51], v[64:79]\n"
    "s_waitcnt lgkmcnt(5)\n"
    "v_cvt_pk_bf16_f32 v60, v52, v53\n"
    "v_cvt_pk_bf16_f32 v61, v54, v55\n"
    "s_waitcnt lgkmcnt(4)\n"
    "v_cvt_pk_bf16_f32 v62, v56, v57\n"
    "v_cvt_pk_bf16_f32 v63, v58, v59\n"
    "ds_read_b128 v[40:43], v8 offset:1152\n"
    "ds_read_b128 v[44:47], v8 offset:1184\n"
    "s_waitcnt vmcnt(12)\n"
    "v_mfma_f32_32x32x16_bf16 v[64:79], a[92:95], v[60:63], v[64:79]\n"
    "s_waitcnt lgkmcnt(5)\n"
    "v_cvt_pk_bf16_f32 v24, v16, v17\n"
    "v_cvt_pk_bf16_f32 v25, v18, v19\n"
    "s_waitcnt lgkmcnt(4)\n"
    "v_cvt_pk_bf16_f32 v26, v20, v21\n"
    "v_cvt_pk_bf16_f32 v27, v22, v23\n"
    "ds_read_b128 v[52:55], v8 offset:1216\n"
    "ds_read_b128 v[56:59], v8 offset:1248\n"
    "s_waitcnt vmcnt(11)\n"
    "v_mfma_f32_32x32x16_bf16 v[64:79], a[96:99], v[24:27], v[64:79]\n"
    "s_waitcnt lgkmcnt(5)\n"
    "v_cvt_pk_bf16_f32 v36, v28, v29\n"
    "v_cvt_pk_bf16_f32 v37, v30, v31\n"
    "s_waitcnt lgkmcnt(4)\n"
    "v_cvt_pk_bf16_f32 v38, v32, v33\n"
    "v_cvt_pk_bf16_f32 v39, v34, v35\n"
    "ds_read_b128 v[16:19], v8 offset:1280\n"
    "ds_read_b128 v[20:23], v8 offset:1312\n"
    "s_waitcnt vmcnt(10)\n"
    "v_mfma_f32_32x32x16_bf16 v[64:79], a[100:103], v[36:39], v[64:79]\n"
    "s_waitcnt lgkmcnt(5)\n"
    "v_cvt_pk_bf16_f32 v48, v40, v41\n"
    "v_cvt_pk_bf16_f32 v49, v42, v43\n"
    "s_waitcnt lgkmcnt(4)\n"
    "v_cvt_pk_bf16_f32 v50, v44, v45\n"
    "v_cvt_pk_bf16_f32 v51, v46, v47\n"
    "ds_read_b128 v[28:31], v8 offset:1344\n"
    "ds_read_b128 v[32:35], v8 offset:1376\n"
    "s_waitcnt vmcnt(9)\n"
    "v_mfma_f32_32x32x16_bf16 v[64:79], a[104:107], v[48:51], v[64:79]\n"
    "s_waitcnt lgkmcnt(5)\n"
    "v_cvt_pk_bf16_f32 v60, v52, v53\n"
    "v_cvt_pk_bf16_f32 v61, v54, v55\n"
    "s_waitcnt lgkmcnt(4)\n"
    "v_cvt_pk_bf16_f32 v62, v56, v57\n"
    "v_cvt_pk_bf16_f32 v63, v58, v59\n"
    "ds_read_b128 v[40:43], v8 offset:1408\n"
    "ds_read_b128 v[44:47], v8 offset:1440\n"
    "s_waitcnt vmcnt(8)\n"
    "v_mfma_f32_32x32x16_bf16 v[64:79], a[108:111], v[60:63], v[64:79]\n"
    "s_waitcnt lgkmcnt(5)\n"
    "v_cvt_pk_bf16_f32 v24, v16, v17\n"
    "v_cvt_pk_bf16_f32 v25, v18, v19\n"
    "s_waitcnt lgkmcnt(4)\n"
    "v_cvt_pk_bf16_f32 v26, v20, v21\n"
    "v_cvt_pk_bf16_f32 v27, v22, v23\n"
    "ds_read_b128 v[52:55], v8 offset:1472\n"
    "ds_read_b128 v[56:59], v8 offset:1504\n"
    "s_waitcnt vmcnt(7)\n"
    "v_mfma_f32_32x32x16_bf16 v[64:79], a[112:115], v[24:27], v[64:79]\n"
    "s_waitcnt lgkmcnt(5)\n"
    "v_cvt_pk_bf16_f32 v36, v28, v29\n"
    "v_cvt_pk_bf16_f32 v37, v30, v31\n"
    "s_waitcnt lgkmcnt(4)\n"
    "v_cvt_pk_bf16_f32 v38, v32, v33\n"
    "v_cvt_pk_bf16_f32 v39, v34, v35\n"
    "ds_read_b128 v[16:19], v8 offset:1536\n"
    "ds_read_b128 v[20:23], v8 offset:1568\n"
    "s_waitcnt vmcnt(6)\n"
    "v_mfma_f32_32x32x16_bf16 v[64:79], a[116:119], v[36:39], v[64:79]\n"
    "s_waitcnt lgkmcnt(5)\n"
    "v_cvt_pk_bf16_f32 v48, v40, v41\n"
    "v_cvt_pk_bf16_f32 v49, v42, v43\n"
    "s_waitcnt lgkmcnt(4)\n"
    "v_cvt_pk_bf16_f32 v50, v44, v45\n"
    "v_cvt_pk_bf16_f32 v51, v46, v47\n"
    "ds_read_b128 v[28:31], v8 offset:1600\n"
    "ds_read_b128 v[32:35], v8 offset:1632\n"
    "s_waitcnt vmcnt(5)\n"
    "v_mfma_f32_32x32x16_bf16 v[64:79], a[120:123], v[48:51], v[64:79]\n"
    "s_waitcnt lgkmcnt(5)\n"
    "v_cvt_pk_bf16_f32 v60, v52, v53\n"
    "v_cvt_pk_bf16_f32 v61, v54, v55\n"
    "s_waitcnt lgkmcnt(4)\n"
    "v_cvt_pk_bf16_f32 v62, v56, v57\n"
    "v_cvt_pk_bf16_f32 v63, v58, v59\n"
    "ds_read_b128 v[40:43], v8 offset:1664\n"
    "ds_read_b128 v[44:47], v8 offset:1696\n"
    "s_waitcnt vmcnt(4)\n"
    "v_mfma_f32_32x32x16_bf16 v[64:79], a[124:127], v[60:63], v[64:79]\n"
    "s_waitcnt lgkmcnt(5)\n"
    "v_cvt_pk_bf16_f32 v24, v16, v17\n"
    "v_cvt_pk_bf16_f32 v25, v18, v19\n"
    "s_waitcnt lgkmcnt(4)\n"
    "v_cvt_pk_bf16_f32 v26, v20, v21\n"
    "v_cvt_pk_bf16_f32 v27, v22, v23\n"
    "ds_read_b128 v[52:55], v8 offset:1728\n"
    "ds_read_b128 v[56:59], v8 offset:1760\n"
    "s_waitcnt vmcnt(3)\n"
    "v_mfma_f32_32x32x16_bf16 v[64:79], a[128:131], v[24:27], v[64:79]\n"
    "s_waitcnt lgkmcnt(5)\n"
    "v_cvt_pk_bf16_f32 v36, v28, v29\n"
    "v_cvt_pk_bf16_f32 v37, v30, v31\n"
    "s_waitcnt lgkmcnt(4)\n"
    "v_cvt_pk_bf16_f32 v38, v32, v33\n"
    "v_cvt_pk_bf16_f32 v39, v34, v35\n"
    "s_waitcnt vmcnt(2)\n"
    "s_nop 0\n"
    "v_mfma_f32_32x32x16_bf16 v[64:79], a[132:135], v[36:39], v[64:79]\n"
    "s_waitcnt lgkmcnt(3)\n"
    "v_cvt_pk_bf16_f32 v48, v40, v41\n"
    "v_cvt_pk_bf16_f32 v49, v42, v43\n"
    "s_waitcnt lgkmcnt(2)\n"
    "v_cvt_pk_bf16_f32 v50, v44, v45\n"
    "v_cvt_pk_bf16_f32 v51, v46, v47\n"
    "s_waitcnt vmcnt(1)\n"
    "s_nop 0\n"
    "v_mfma_f32_32x32x16_bf16 v[64:79], a[136:139], v[48:51], v[64:79]\n"
    "s_waitcnt lgkmcnt(1)\n"
    "v_cvt_pk_bf16_f32 v60, v52, v53\n"
    "v_cvt_pk_bf16_f32 v61, v54, v55\n"
    "s_waitcnt lgkmcnt(0)\n"
    "v_cvt_pk_bf16_f32 v62, v56, v57\n"
    "v_cvt_pk_bf16_f32 v63, v58, v59\n"
    "s_waitcnt vmcnt(0)\n"
    "s_nop 0\n"
    "v_mfma_f32_32x32x16_bf16 v[64:79], a[140:143], v[60:63], v[64:79]\n"
    "s_lshl_b32 s52, s18, 8\n"
    "s_add_u32 s52, s52, 143360\n"
    "v_and_b32 v8, 1, v1\n"
    "v_lshlrev_b32 v8, 7, v8\n"
    "v_lshl_add_u32 v8, v9, 4, v8\n"
    "v_add_u32 v8, s52, v8\n"
    "v_and_b32 v9, 31, v1\n"
    "v_lshlrev_b32 v9, 2, v9\n"
    "v_add_u32 v9, s52, v9\n"
    "s_mul_i32 s60, s59, 29696\n"
    "s_add_u32 s60, s60, 4222976\n"
    "s_nop 0\n"
    "ds_write_b128 v8, v[64:67] offset:0\n"
    "ds_write_b128 v8, v[68:71] offset:32\n"
    "ds_write_b128 v8, v[72:75] offset:64\n"
    "ds_write_b128 v8, v[76:79] offset:96\n"
    "s_waitcnt lgkmcnt(0)\n"
    "ds_read_b32 v10, v9\n"
    "ds_read_b32 v11, v9 offset:128\n"
    "s_waitcnt lgkmcnt(0)\n"
    "v_cvt_pk_bf16_f32 v12, v10, v11\n"
    "v_lshlrev_b32 v13, 16, v12\n"
    "v_and_b32 v14, 0xffff0000, v12\n"
    "v_sub_f32 v10, v10, v13\n"
    "v_sub_f32 v11, v11, v14\n"
    "v_cvt_pk_bf16_f32 v16, v13, v10\n"
    "v_mov_b32 v17, v12\n"
    "v_cvt_pk_bf16_f32 v18, v11, v14\n"
    "v_mov_b32 v19, 0\n"
    "v_cndmask_b32 v16, v16, 0, s[78:79]\n"
    "v_cndmask_b32 v17, v17, 0, s[78:79]\n"
    "v_cndmask_b32 v18, v18, 0, s[78:79]\n"
    "buffer_store_dwordx4 v[16:19], v0, s[4:7], s60 offen sc1\n"
    "s_lshl_b32 s52, s59, 3\n"
    "s_add_u32 s52, s52, 13344\n"
    "v_mov_b32 v254, s16\n"
    "v_mov_b32 v255, s17\n"
    "s_waitcnt vmcnt(0)\n"
    "s_mov_b64 exec, 1\n"
    "buffer_atomic_swap_x2 v[254:255], off, s[4:7], s52 sc1\n"
    "s_mov_b64 exec, -1\n"
    "ds_write_b128 v7, a[240:243] offset:33792\n"
    "ds_write_b128 v7, a[244:247] offset:34816\n"
    "ds_write_b128 v7, v[192:195] offset:0\n"
    "Lp4_21%=:\n"
    "ds_write_b128 v7, v[196:199] offset:1024\n"
    "ds_write_b128 v7, v[200:203] offset:2048\n"
    "ds_write_b128 v7, v[204:207] offset:3072\n"
    "ds_write_b128 v7, v[208:211] offset:4096\n"
    "ds_write_b128 v7, v[212:215] offset:5120\n"
    "ds_write_b128 v7, v[216:219] offset:6144\n"
    "ds_write_b128 v7, v[220:223] offset:28672\n"
    "ds_write_b128 v7, v[224:227] offset:29696\n"
    "ds_write_b128 v7, v[228:231] offset:30720\n"
    "ds_write_b128 v7, v[232:235] offset:31744\n"
    "ds_write_b128 v7, v[236:239] offset:32768\n"
    "v_add_u32 v5, 57344, v0\n"
    "v_add_u32 v190, 114688, v0\n"
    "s_add_u32 s3, s41, 16384\n"
    "s_lshl_b32 s44, s75, 7\n"
    "s_add_u32 s44, s44, 24576\n"
    "v_lshlrev_b32 v12, 3, v1\n"
    "v_add_u32 v12, s44, v12\n"
    "s_mov_b32 s70, 0\n"
    "s_mov_b64 exec, 16383\n"
    "s_nop 0\n"
    "Lpoll_24%=:\n"
    "v_mov_b32 v8, 0\n"
    "v_mov_b32 v9, 0\n"
    "buffer_atomic_or_x2 v[8:9], v12, s[4:7], 0 offen sc0 sc1\n"
    "s_waitcnt vmcnt(0)\n"
    "v_cmp_ne_u64 vcc, s[16:17], v[8:9]\n"
    "s_nop 0\n"
    "s_and_b64 exec, exec, vcc\n"
    "s_cbranch_scc0 Lpollok_25%=\n"
    "s_sleep 2\n"
    "s_add_u32 s70, s70, 1\n"
    "s_cmp_lt_u32 s70, 0x40000\n"
    "s_cbranch_scc1 Lpoll_24%=\n"
    "Lpollok_25%=:\n"
    "s_mov_b64 exec, -1\n"
    "buffer_load_dwordx4 a[0:3], v0, s[4:7], s46 offen offset:0 sc1\n"
    "buffer_load_dwordx4 a[4:7], v0, s[4:7], s46 offen offset:1024 sc1\n"
    "buffer_load_dwordx4 a[8:11], v0, s[4:7], s46 offen offset:2048 sc1\n"
    "buffer_load_dwordx4 a[12:15], v0, s[4:7], s46 offen offset:3072 sc1\n"
    "buffer_load_dwordx4 a[16:19], v2, s[4:7], s46 offen offset:0 sc1\n"
    "buffer_load_dwordx4 a[20:23], v2, s[4:7], s46 offen offset:1024 sc1\n"
    "buffer_load_dwordx4 a[24:27], v2, s[4:7], s46 offen offset:2048 sc1\n"
    "buffer_load_dwordx4 a[28:31], v2, s[4:7], s46 offen offset:3072 sc1\n"
    "buffer_load_dwordx4 a[32:35], v3, s[4:7], s46 offen offset:0 sc1\n"
    "buffer_load_dwordx4 a[36:39], v3, s[4:7], s46 offen offset:1024 sc1\n"
    "ds_read_b128 v[96:99], v5 offset:0\n"
    "ds_read_b128 v[100:103], v5 offset:28672\n"
    "ds_read_b128 v[104:107], v190 offset:0\n"
    "ds_read_b128 v[108:111], v5 offset:1024\n"
    "ds_read_b128 v[112:115], v5 offset:29696\n"
    "ds_read_b128 v[116:119], v190 offset:1024\n"
    "s_waitcnt vmcnt(9) lgkmcnt(5)\n"
    "v_mfma_f32_32x32x16_bf16 v[16:31], v[96:99], a[0:3], 0\n"
    "ds_read_b128 v[120:123], v5 offset:2048\n"
    "buffer_load_dwordx4 a[40:43], v3, s[4:7], s46 offen offset:2048 sc1\n"
    "s_waitcnt lgkmcnt(5)\n"
    "v_mfma_f32_32x32x16_bf16 v[32:47], v[100:103], a[0:3], 0\n"
    "ds_read_b128 v[124:127], v5 offset:30720\n"
    "buffer_load_dwordx4 a[44:47], v3, s[4:7], s46 offen offset:3072 sc1\n"
    "s_waitcnt lgkmcnt(5)\n"
    "v_mfma_f32_32x32x16_bf16 v[48:63], v[104:107], a[0:3], 0\n"
    "ds_read_b128 v[96:99], v190 offset:2048\n"
    "s_waitcnt vmcnt(10) lgkmcnt(5)\n"
    "v_mfma_f32_32x32x16_bf16 v[16:31], v[108:111], a[4:7], v[16:31]\n"
    "ds_read_b128 v[100:103], v5 offset:3072\n"
    "buffer_load_dwordx4 a[48:51], v4, s[4:7], s46 offen offset:0 sc1\n"
    "s_waitcnt lgkmcnt(5)\n"
    "v_mfma_f32_32x32x16_bf16 v[32:47], v[112:115], a[4:7], v[32:47]\n"
    "ds_read_b128 v[104:107], v5 offset:31744\n"
    "buffer_load_dwordx4 a[52:55], v4, s[4:7], s46 offen offset:1024 sc1\n"
    "s_waitcnt lgkmcnt(5)\n"
    "v_mfma_f32_32x32x16_bf16 v[48:63], v[116:119], a[4:7], v[48:63]\n"
    "ds_read_b128 v[108:111], v190 offset:3072\n"
    "s_waitcnt vmcnt(11) lgkmcnt(5)\n"
    "v_mfma_f32_32x32x16_bf16 v[16:31], v[120:123], a[8:11], v[16:31]\n"
    "ds_read_b128 v[112:115], v5 offset:4096\n"
    "buffer_load_dwordx4 a[56:59], v4, s[4:7], s46 offen offset:2048 sc1\n"
    "s_waitcnt lgkmcnt(5)\n"
    "v_mfma_f32_32x32x16_bf16 v[32:47], v[124:127], a[8:11], v[32:47]\n"
    "ds_read_b128 v[116:119], v5 offset:32768\n"
    "buffer_load_dwordx4 a[60:63], v4, s[4:7], s46 offen offset:3072 sc1\n"
    "s_waitcnt lgkmcnt(5)\n"
    "v_mfma_f32_32x32x16_bf16 v[48:63], v[96:99], a[8:11], v[48:63]\n"
    "ds_read_b128 v[120:123], v190 offset:4096\n"
    "s_waitcnt vmcnt(12) lgkmcnt(5)\n"
    "v_mfma_f32_32x32x16_bf16 v[16:31], v[100:103], a[12:15], v[16:31]\n"
    "ds_read_b128 v[124:127], v5 offset:5120\n"
    "buffer_load_dwordx4 a[64:67], v0, s[4:7], s47 offen offset:0 sc1\n"
    "s_waitcnt lgkmcnt(5)\n"
    "v_mfma_f32_32x32x16_bf16 v[32:47], v[104:107], a[12:15], v[32:47]\n"
    "ds_read_b128 v[96:99], v5 offset:33792\n"
    "buffer_load_dwordx4 a[68:71], v0, s[4:7], s47 offen offset:1024 sc1\n"
    "buffer_load_dwordx2 v[188:189], v15, s[4:7], 0 offen sc1\n"
    "s_waitcnt lgkmcnt(5)\n"
    "v_mfma_f32_32x32x16_bf16 v[48:63], v[108:111], a[12:15], v[48:63]\n"
    "ds_read_b128 v[100:103], v190 offset:5120\n"
    "s_waitcnt vmcnt(14) lgkmcnt(5)\n"
    "v_mfma_f32_32x32x16_bf16 v[16:31], v[112:115], a[16:19], v[16:31]\n"
    "ds_read_b128 v[104:107], v5 offset:6144\n"
    "buffer_load_dwordx4 a[72:75], v0, s[4:7], s47 offen offset:2048 sc1\n"
    "s_waitcnt lgkmcnt(5)\n"
    "v_mfma_f32_32x32x16_bf16 v[32:47], v[116:119], a[16:19], v[32:47]\n"
    "ds_read_b128 v[108:111], v5 offset:34816\n"
    "buffer_load_dwordx4 a[76:79], v0, s[4:7], s47 offen offset:3072 sc1\n"
    "s_waitcnt lgkmcnt(5)\n"
    "v_mfma_f32_32x32x16_bf16 v[48:63], v[120:123], a[16:19], v[48:63]\n"
    "ds_read_b128 v[112:115], v190 offset:6144\n"
    "s_waitcnt vmcnt(15) lgkmcnt(5)\n"
    "v_mfma_f32_32x32x16_bf16 v[16:31], v[124:127], a[20:23], v[16:31]\n"
    "ds_read_b128 v[116:119], v5 offset:7168\n"
    "buffer_load_dwordx4 a[80:83], v2, s[4:7], s47 offen offset:0 sc1\n"
    "s_waitcnt lgkmcnt(5)\n"
    "v_mfma_f32_32x32x16_bf16 v[32:47], v[96:99], a[20:23], v[32:47]\n"
    "ds_read_b128 v[120:123], v5 offset:35840\n"
    "buffer_load_dwordx4 a[84:87], v2, s[4:7], s47 offen offset:1024 sc1\n"
    "s_waitcnt lgkmcnt(5)\n"
    "v_mfma_f32_32x32x16_bf16 v[48:63], v[100:103], a[20:23], v[48:63]\n"
    "ds_read_b128 v[124:127], v190 offset:7168\n"
    "s_waitcnt vmcnt(16) lgkmcnt(5)\n"
    "v_mfma_f32_32x32x16_bf16 v[16:31], v[104:107], a[24:27], v[16:31]\n"
    "ds_read_b128 v[96:99], v5 offset:8192\n"
    "buffer_load_dwordx4 a[88:91], v2, s[4:7], s47 offen offset:2048 sc1\n"
    "s_waitcnt lgkmcnt(5)\n"
    "v_mfma_f32_32x32x16_bf16 v[32:47], v[108:111], a[24:27], v[32:47]\n"
    "ds_read_b128 v[100:103], v5 offset:36864\n"
    "buffer_load_dwordx4 a[92:95], v2, s[4:7], s47 offen offset:3072 sc1\n"
    "s_waitcnt lgkmcnt(5)\n"
    "v_mfma_f32_32x32x16_bf16 v[48:63], v[112:115], a[24:27], v[48:63]\n"
    "ds_read_b128 v[104:107], v190 offset:8192\n"
    "s_waitcnt vmcnt(17) lgkmcnt(5)\n"
    "v_mfma_f32_32x32x16_bf16 v[16:31], v[116:119], a[28:31], v[16:31]\n"
    "ds_read_b128 v[108:111], v5 offset:9216\n"
    "buffer_load_dwordx4 a[240:243], v3, s[4:7], s47 offen offset:0 sc1\n"
    "s_waitcnt lgkmcnt(5)\n"
    "v_mfma_f32_32x32x16_bf16 v[32:47], v[120:123], a[28:31], v[32:47]\n"
    "ds_read_b128 v[112:115], v5 offset:37888\n"
    "buffer_load_dwordx4 a[244:247], v3, s[4:7], s47 offen offset:1024 sc1\n"
    "s_waitcnt lgkmcnt(5)\n"
    "v_mfma_f32_32x32x16_bf16 v[48:63], v[124:127], a[28:31], v[48:63]\n"
    "ds_read_b128 v[116:119], v190 offset:9216\n"
    "s_waitcnt vmcnt(18) lgkmcnt(5)\n"
    "v_mfma_f32_32x32x16_bf16 v[16:31], v[96:99], a[32:35], v[16:31]\n"
    "ds_read_b128 v[120:123], v5 offset:10240\n"
    "buffer_load_dwordx4 a[248:251], v3, s[4:7], s47 offen offset:2048 sc1\n"
    "s_waitcnt lgkmcnt(5)\n"
    "v_mfma_f32_32x32x16_bf16 v[32:47], v[100:103], a[32:35], v[32:47]\n"
    "ds_read_b128 v[124:127], v5 offset:38912\n"
    "buffer_load_dwordx4 a[252:255], v3, s[4:7], s47 offen offset:3072 sc1\n"
    "s_waitcnt lgkmcnt(5)\n"
    "v_mfma_f32_32x32x16_bf16 v[48:63], v[104:107], a[32:35], v[48:63]\n"
    "ds_read_b128 v[96:99], v190 offset:10240\n"
    "s_waitcnt vmcnt(19) lgkmcnt(5)\n"
    "v_mfma_f32_32x32x16_bf16 v[16:31], v[108:111], a[36:39], v[16:31]\n"
    "ds_read_b128 v[100:103], v5 offset:11264\n"
    "buffer_load_dwordx2 v[168:169], v6, s[4:7], s48 offen offset:0 sc1\n"
    "s_waitcnt lgkmcnt(5)\n"
    "v_mfma_f32_32x32x16_bf16 v[32:47], v[112:115], a[36:39], v[32:47]\n"
    "ds_read_b128 v[104:107], v5 offset:39936\n"
    "buffer_load_dwordx2 v[170:171], v6, s[4:7], s48 offen offset:256 sc1\n"
    "s_waitcnt lgkmcnt(5)\n"
    "v_mfma_f32_32x32x16_bf16 v[48:63], v[116:119], a[36:39], v[48:63]\n"
    "ds_read_b128 v[108:111], v190 offset:11264\n"
    "s_waitcnt vmcnt(20) lgkmcnt(5)\n"
    "v_mfma_f32_32x32x16_bf16 v[16:31], v[120:123], a[40:43], v[16:31]\n"
    "ds_read_b128 v[112:115], v5 offset:12288\n"
    "buffer_load_dwordx2 v[172:173], v6, s[4:7], s48 offen offset:512 sc1\n"
    "buffer_load_dwordx4 a[144:147], v4, s[4:7], s57 offen offset:0 sc1\n"
    "s_waitcnt lgkmcnt(5)\n"
    "v_mfma_f32_32x32x16_bf16 v[32:47], v[124:127], a[40:43], v[32:47]\n"
    "ds_read_b128 v[116:119], v5 offset:40960\n"
    "buffer_load_dwordx2 v[174:175], v6, s[4:7], s48 offen offset:768 sc1\n"
    "s_waitcnt lgkmcnt(5)\n"
    "v_mfma_f32_32x32x16_bf16 v[48:63], v[96:99], a[40:43], v[48:63]\n"
    "ds_read_b128 v[120:123], v190 offset:12288\n"
    "buffer_load_dwordx4 a[148:151], v4, s[4:7], s58 offen offset:0 sc1\n"
    "s_waitcnt vmcnt(23) lgkmcnt(5)\n"
    "v_mfma_f32_32x32x16_bf16 v[16:31], v[100:103], a[44:47], v[16:31]\n"
    "ds_read_b128 v[124:127], v5 offset:13312\n"
    "buffer_load_dwordx2 v[176:177], v6, s[4:7], s48 offen offset:1024 sc1\n"
    "s_waitcnt lgkmcnt(5)\n"
    "v_mfma_f32_32x32x16_bf16 v[32:47], v[104:107], a[44:47], v[32:47]\n"
    "ds_read_b128 v[96:99], v5 offset:41984\n"
    "buffer_load_dwordx2 v[178:179], v6, s[4:7], s48 offen offset:1280 sc1\n"
    "buffer_load_dwordx4 a[152:155], v4, s[4:7], s57 offen offset:2048 sc1\n"
    "s_waitcnt lgkmcnt(5)\n"
    "v_mfma_f32_32x32x16_bf16 v[48:63], v[108:111], a[44:47], v[48:63]\n"
    "ds_read_b128 v[100:103], v190 offset:13312\n"
    "s_waitcnt vmcnt(25) lgkmcnt(5)\n"
    "v_mfma_f32_32x32x16_bf16 v[16:31], v[112:115], a[48:51], v[16:31]\n"
    "ds_read_b128 v[104:107], v5 offset:14336\n"
    "buffer_load_dwordx2 v[180:181], v6, s[4:7], s48 offen offset:1536 sc1\n"
    "buffer_load_dwordx4 a[156:159], v4, s[4:7], s58 offen offset:2048 sc1\n"
    "s_waitcnt lgkmcnt(5)\n"
    "v_mfma_f32_32x32x16_bf16 v[32:47], v[116:119], a[48:51], v[32:47]\n"
    "ds_read_b128 v[108:111], v5 offset:43008\n"
    "s_waitcnt lgkmcnt(5)\n"
    "v_mfma_f32_32x32x16_bf16 v[48:63], v[120:123], a[48:51], v[48:63]\n"
    "ds_read_b128 v[112:115], v190 offset:14336\n"
    "s_add_u32 s57, s57, 16384\n"
    "s_add_u32 s58, s58, 16384\n"
    "buffer_load_dwordx4 a[160:163], v0, s[4:7], s57 offen offset:0 sc1\n"
    "s_waitcnt vmcnt(27) lgkmcnt(5)\n"
    "v_mfma_f32_32x32x16_bf16 v[16:31], v[124:127], a[52:55], v[16:31]\n"
    "ds_read_b128 v[116:119], v5 offset:15360\n"
    "s_waitcnt lgkmcnt(5)\n"
    "v_mfma_f32_32x32x16_bf16 v[32:47], v[96:99], a[52:55], v[32:47]\n"
    "ds_read_b128 v[120:123], v5 offset:44032\n"
    "buffer_load_dwordx4 a[164:167], v0, s[4:7], s58 offen offset:0 sc1\n"
    "s_waitcnt lgkmcnt(5)\n"
    "v_mfma_f32_32x32x16_bf16 v[48:63], v[100:103], a[52:55], v[48:63]\n"
    "ds_read_b128 v[124:127], v190 offset:15360\n"
    "s_waitcnt vmcnt(27) lgkmcnt(5)\n"
    "v_mfma_f32_32x32x16_bf16 v[16:31], v[104:107], a[56:59], v[16:31]\n"
    "s_waitcnt lgkmcnt(0)\n"
    "s_barrier\n"
    "buffer_load_dwordx4 a[168:171], v0, s[4:7], s57 offen offset:2048 sc1\n"
    "ds_read_b128 v[96:99], v5 offset:16384\n"
    "v_mfma_f32_32x32x16_bf16 v[32:47], v[108:111], a[56:59], v[32:47]\n"
    "ds_read_b128 v[100:103], v5 offset:45056\n"
    "v_mfma_f32_32x32x16_bf16 v[48:63], v[112:115], a[56:59], v[48:63]\n"
    "ds_read_b128 v[104:107], v190 offset:16384\n"
    "buffer_load_dwordx4 a[172:175], v0, s[4:7], s58 offen offset:2048 sc1\n"
    "s_waitcnt vmcnt(28)\n"
    "v_mfma_f32_32x32x16_bf16 v[16:31], v[116:119], a[60:63], v[16:31]\n"
    "ds_read_b128 v[108:111], v5 offset:17408\n"
    "v_mfma_f32_32x32x16_bf16 v[32:47], v[120:123], a[60:63], v[32:47]\n"
    "ds_read_b128 v[112:115], v5 offset:46080\n"
    "buffer_load_dwordx4 a[176:179], v2, s[4:7], s57 offen offset:0 sc1\n"
    "v_mfma_f32_32x32x16_bf16 v[48:63], v[124:127], a[60:63], v[48:63]\n"
    "ds_read_b128 v[116:119], v190 offset:17408\n"
    "s_waitcnt vmcnt(28) lgkmcnt(5)\n"
    "v_mfma_f32_32x32x16_bf16 v[16:31], v[96:99], a[64:67], v[16:31]\n"
    "ds_read_b128 v[120:123], v5 offset:18432\n"
    "buffer_load_dwordx4 a[180:183], v2, s[4:7], s58 offen offset:0 sc1\n"
    "s_waitcnt lgkmcnt(5)\n"
    "v_mfma_f32_32x32x16_bf16 v[32:47], v[100:103], a[64:67], v[32:47]\n"
    "ds_read_b128 v[124:127], v5 offset:47104\n"
    "s_waitcnt lgkmcnt(5)\n"
    "v_mfma_f32_32x32x16_bf16 v[48:63], v[104:107], a[64:67], v[48:63]\n"
    "ds_read_b128 v[96:99], v190 offset:18432\n"
    "s_waitcnt vmcnt(27)\n"
    "v_cmp_ne_u64 vcc, s[16:17], v[188:189]\n"
    "s_cbranch_vccnz Lchk_26%=\n"
    "Lchkb_27%=:\n"
    "buffer_load_dwordx4 v[140:143], v0, s[4:7], s49 offen offset:0 sc1\n"
    "s_waitcnt lgkmcnt(5)\n"
    "v_mfma_f32_32x32x16_bf16 v[16:31], v[108:111], a[68:71], v[16:31]\n"
    "ds_read_b128 v[100:103], v5 offset:19456\n"
    "s_add_u32 s49, s49, 29696\n"
    "buffer_load_dwordx4 v[144:147], v0, s[4:7], s49 offen offset:0 sc1\n"
    "s_waitcnt lgkmcnt(5)\n"
    "v_mfma_f32_32x32x16_bf16 v[32:47], v[112:115], a[68:71], v[32:47]\n"
    "ds_read_b128 v[104:107], v5 offset:48128\n"
    "s_add_u32 s49, s49, 29696\n"
    "buffer_load_dwordx4 v[148:151], v0, s[4:7], s49 offen offset:0 sc1\n"
    "s_waitcnt lgkmcnt(5)\n"
    "v_mfma_f32_32x32x16_bf16 v[48:63], v[116:119], a[68:71], v[48:63]\n"
    "ds_read_b128 v[108:111], v190 offset:19456\n"
    "s_add_u32 s49, s49, 29696\n"
    "buffer_load_dwordx4 v[152:155], v0, s[4:7], s49 offen offset:0 sc1\n"
    "s_waitcnt vmcnt(30) lgkmcnt(5)\n"
    "v_mfma_f32_32x32x16_bf16 v[16:31], v[120:123], a[72:75], v[16:31]\n"
    "ds_read_b128 v[112:115], v5 offset:20480\n"
    "s_add_u32 s49, s49, 29696\n"
    "buffer_load_dwordx4 v[156:159], v0, s[4:7], s49 offen offset:0 sc1\n"
    "s_waitcnt lgkmcnt(5)\n"
    "v_mfma_f32_32x32x16_bf16 v[32:47], v[124:127], a[72:75], v[32:47]\n"
    "ds_read_b128 v[116:119], v5 offset:49152\n"
    "s_add_u32 s49, s49, 29696\n"
    "buffer_load_dwordx4 v[160:163], v0, s[4:7], s49 offen offset:0 sc1\n"
    "s_waitcnt lgkmcnt(5)\n"
    "v_mfma_f32_32x32x16_bf16 v[48:63], v[96:99], a[72:75], v[48:63]\n"
    "ds_read_b128 v[120:123], v190 offset:20480\n"
    "s_add_u32 s49, s49, 29696\n"
    "buffer_load_dwordx4 v[164:167], v0, s[4:7], s49 offen offset:0 sc1\n"
    "s_waitcnt vmcnt(32) lgkmcnt(5)\n"
    "v_mfma_f32_32x32x16_bf16 v[16:31], v[100:103], a[76:79], v[16:31]\n"
    "ds_read_b128 v[124:127], v5 offset:21504\n"
    "s_waitcnt lgkmcnt(5)\n"
    "v_mfma_f32_32x32x16_bf16 v[32:47], v[104:107], a[76:79], v[32:47]\n"
    "ds_read_b128 v[96:99], v5 offset:50176\n"
    "buffer_load_dwordx4 a[184:187], v2, s[4:7], s57 offen offset:2048 sc1\n"
    "s_waitcnt lgkmcnt(5)\n"
    "v_mfma_f32_32x32x16_bf16 v[48:63], v[108:111], a[76:79], v[48:63]\n"
    "ds_read_b128 v[100:103], v190 offset:21504\n"
    "s_waitcnt vmcnt(32) lgkmcnt(5)\n"
    "v_mfma_f32_32x32x16_bf16 v[16:31], v[112:115], a[80:83], v[16:31]\n"
    "ds_read_b128 v[104:107], v5 offset:22528\n"
    "buffer_load_dwordx4 a[188:191], v2, s[4:7], s58 offen offset:2048 sc1\n"
    "s_waitcnt lgkmcnt(5)\n"
    "v_mfma_f32_32x32x16_bf16 v[32:47], v[116:119], a[80:83], v[32:47]\n"
    "ds_read_b128 v[108:111], v5 offset:51200\n"
    "s_waitcnt lgkmcnt(5)\n"
    "v_mfma_f32_32x32x16_bf16 v[48:63], v[120:123], a[80:83], v[48:63]\n"
    "ds_read_b128 v[112:115], v190 offset:22528\n"
    "buffer_load_dwordx4 a[192:195], v3, s[4:7], s57 offen offset:0 sc1\n"
    "s_waitcnt vmcnt(33) lgkmcnt(5)\n"
    "v_mfma_f32_32x32x16_bf16 v[16:31], v[124:127], a[84:87], v[16:31]\n"
    "ds_read_b128 v[116:119], v5 offset:23552\n"
    "s_waitcnt lgkmcnt(5)\n"
    "v_mfma_f32_32x32x16_bf16 v[32:47], v[96:99], a[84:87], v[32:47]\n"
    "ds_read_b128 v[120:123], v5 offset:52224\n"
    "buffer_load_dwordx4 a[196:199], v3, s[4:7], s58 offen offset:0 sc1\n"
    "s_waitcnt lgkmcnt(5)\n"
    "v_mfma_f32_32x32x16_bf16 v[48:63], v[100:103], a[84:87], v[48:63]\n"
    "ds_read_b128 v[124:127], v190 offset:23552\n"
    "s_waitcnt vmcnt(33) lgkmcnt(5)\n"
    "v_mfma_f32_32x32x16_bf16 v[16:31], v[104:107], a[88:91], v[16:31]\n"
    "ds_read_b128 v[96:99], v5 offset:24576\n"
    "buffer_load_dwordx4 a[200:203], v3, s[4:7], s57 offen offset:2048 sc1\n"
    "s_waitcnt vmcnt(27)\n"
    "v_add_f32 v8, v168, v170\n"
    "v_add_f32 v9, v169, v171\n"
    "s_waitcnt vmcnt(26)\n"
    "v_add_f32 v8, v8, v172\n"
    "v_add_f32 v9, v9, v173\n"
    "s_waitcnt lgkmcnt(5)\n"
    "v_mfma_f32_32x32x16_bf16 v[32:47], v[108:111], a[88:91], v[32:47]\n"
    "ds_read_b128 v[100:103], v5 offset:53248\n"
    "s_waitcnt vmcnt(24)\n"
    "v_add_f32 v8, v8, v174\n"
    "v_add_f32 v9, v9, v175\n"
    "s_waitcnt vmcnt(22)\n"
    "v_add_f32 v8, v8, v176\n"
    "v_add_f32 v9, v9, v177\n"
    "s_waitcnt lgkmcnt(5)\n"
    "v_mfma_f32_32x32x16_bf16 v[48:63], v[112:115], a[88:91], v[48:63]\n"
    "ds_read_b128 v[104:107], v190 offset:24576\n"
    "buffer_load_dwordx4 a[204:207], v3, s[4:7], s58 offen offset:2048 sc1\n"
    "s_waitcnt vmcnt(22)\n"
    "v_add_f32 v8, v8, v178\n"
    "v_add_f32 v9, v9, v179\n"
    "s_waitcnt vmcnt(20)\n"
    "v_add_f32 v8, v8, v180\n"
    "v_add_f32 v9, v9, v181\n"
    "s_waitcnt lgkmcnt(5)\n"
    "v_mfma_f32_32x32x16_bf16 v[16:31], v[116:119], a[92:95], v[16:31]\n"
    "ds_read_b128 v[108:111], v5 offset:25600\n"
    "v_mov_b32 v10, v8\n"
    "v_mov_b32 v11, v9\n"
    "s_nop 0\n"
    "v_permlane32_swap_b32 v8, v10\n"
    "v_permlane32_swap_b32 v9, v11\n"
    "s_waitcnt lgkmcnt(5)\n"
    "v_mfma_f32_32x32x16_bf16 v[32:47], v[120:123], a[92:95], v[32:47]\n"
    "ds_read_b128 v[112:115], v5 offset:54272\n"
    "buffer_load_dwordx4 a[208:211], v4, s[4:7], s57 offen offset:0 sc1\n"
    "v_add_f32 v8, v8, v10\n"
    "v_add_f32 v9, v9, v11\n"
    "v_mul_f32 v8, 0xbb124925, v8\n"
    "v_mul_f32 v9, 0x3b124925, v9\n"
    "s_waitcnt lgkmcnt(5)\n"
    "v_mfma_f32_32x32x16_bf16 v[48:63], v[124:127], a[92:95], v[48:63]\n"
    "ds_read_b128 v[116:119], v190 offset:25600\n"
    "v_fma_f32 v9, -v8, v8, v9\n"
    "v_max_f32 v9, 0, v9\n"
    "v_add_f32 v13, 0x3727c5ac, v9\n"
    "v_rsq_f32 v12, v13\n"
    "s_waitcnt lgkmcnt(5)\n"
    "v_mfma_f32_32x32x16_bf16 v[16:31], v[96:99], a[240:243], v[16:31]\n"
    "ds_read_b128 v[120:123], v5 offset:26624\n"
    "buffer_load_dwordx4 a[212:215], v4, s[4:7], s58 offen offset:0 sc1\n"
    "v_sqrt_f32 v9, v13\n"
    "v_rcp_f32 v14, v13\n"
    "v_cvt_pk_bf16_f32 v10, v8, v9\n"
    "v_mul_f32 v14, 0xbdd2d3e8, v14\n"
    "s_waitcnt lgkmcnt(5)\n"
    "v_mfma_f32_32x32x16_bf16 v[32:47], v[100:103], a[240:243], v[32:47]\n"
    "ds_read_b128 v[124:127], v5 offset:55296\n"
    "v_lshlrev_b32 v11, 16, v10\n"
    "v_and_b32 v10, 0xffff0000, v10\n"
    "v_sub_f32 v8, v8, v11\n"
    "v_sub_f32 v9, v9, v10\n"
    "s_waitcnt lgkmcnt(5)\n"
    "v_mfma_f32_32x32x16_bf16 v[48:63], v[104:107], a[240:243], v[48:63]\n"
    "ds_read_b128 v[96:99], v190 offset:26624\n"
    "buffer_load_dwordx4 a[216:219], v4, s[4:7], s57 offen offset:2048 sc1\n"
    "v_mul_f32 v14, v14, v12\n"
    "v_mul_f32 v12, 0xc0135761, v12\n"
    "v_cvt_pk_bf16_f32 v184, v11, v11\n"
    "v_cvt_pk_bf16_f32 v185, v8, v10\n"
    "s_waitcnt lgkmcnt(5)\n"
    "v_mfma_f32_32x32x16_bf16 v[16:31], v[108:111], a[244:247], v[16:31]\n"
    "ds_read_b128 v[100:103], v5 offset:27648\n"
    "v_cvt_pk_bf16_f32 v186, v10, v9\n"
    "v_mov_b32 v187, 0\n"
    "v_cndmask_b32 v184, v184, 0, s[78:79]\n"
    "v_cndmask_b32 v185, v185, 0, s[78:79]\n"
    "s_waitcnt lgkmcnt(5)\n"
    "v_mfma_f32_32x32x16_bf16 v[32:47], v[112:115], a[244:247], v[32:47]\n"
    "ds_read_b128 v[104:107], v5 offset:56320\n"
    "buffer_load_dwordx4 a[220:223], v4, s[4:7], s58 offen offset:2048 sc1\n"
    "v_cndmask_b32 v186, v186, 0, s[78:79]\n"
    "s_waitcnt lgkmcnt(5)\n"
    "v_mfma_f32_32x32x16_bf16 v[48:63], v[116:119], a[244:247], v[48:63]\n"
    "ds_read_b128 v[108:111], v190 offset:27648\n"
    "s_waitcnt lgkmcnt(5)\n"
    "v_mfma_f32_32x32x16_bf16 v[16:31], v[120:123], a[248:251], v[16:31]\n"
    "ds_read_b128 v[112:115], v0 offset:0\n"
    "s_add_u32 s57, s57, 16384\n"
    "s_add_u32 s58, s58, 16384\n"
    "buffer_load_dwordx4 a[224:227], v0, s[4:7], s57 offen offset:0 sc1\n"
    "s_waitcnt lgkmcnt(5)\n"
    "v_mfma_f32_32x32x16_bf16 v[32:47], v[124:127], a[248:251], v[32:47]\n"
    "ds_read_b128 v[116:119], v0 offset:1024\n"
    "s_waitcnt lgkmcnt(5)\n"
    "v_mfma_f32_32x32x16_bf16 v[48:63], v[96:99], a[248:251], v[48:63]\n"
    "ds_read_b128 v[120:123], v0 offset:2048\n"
    "buffer_load_dwordx4 a[228:231], v0, s[4:7], s58 offen offset:0 sc1\n"
    "s_waitcnt lgkmcnt(5)\n"
    "v_mfma_f32_32x32x16_bf16 v[16:31], v[100:103], a[252:255], v[16:31]\n"
    "ds_read_b128 v[124:127], v0 offset:3072\n"
    "s_waitcnt lgkmcnt(5)\n"
    "v_mfma_f32_32x32x16_bf16 v[32:47], v[104:107], a[252:255], v[32:47]\n"
    "ds_read_b128 v[96:99], v0 offset:4096\n"
    "buffer_load_dwordx4 a[232:235], v0, s[4:7], s57 offen offset:2048 sc1\n"
    "s_waitcnt lgkmcnt(5)\n"
    "v_mfma_f32_32x32x16_bf16 v[48:63], v[108:111], a[252:255], v[48:63]\n"
    "ds_read_b128 v[100:103], v0 offset:5120\n"
    "s_waitcnt vmcnt(19)\n"
    "v_mfma_f32_32x32x16_bf16 v[16:31], v[140:143], v[184:187], v[16:31]\n"
    "buffer_load_dwordx4 a[236:239], v0, s[4:7], s58 offen offset:2048 sc1\n"
    "s_waitcnt vmcnt(19)\n"
    "v_mfma_f32_32x32x16_bf16 v[32:47], v[144:147], v[184:187], v[32:47]\n"
    "s_waitcnt vmcnt(18)\n"
    "v_mfma_f32_32x32x16_bf16 v[48:63], v[148:151], v[184:187], v[48:63]\n"
    "s_waitcnt lgkmcnt(4)\n"
    "v_mfma_f32_32x32x16_bf16 v[64:79], v[112:115], a[0:3], 0\n"
    "ds_read_b128 v[104:107], v0 offset:6144\n"
    "s_nop 3\n"
    "v_mul_f32 v128, v16, v16\n"
    "v_mul_f32 v129, v17, v17\n"
    "v_mul_f32 v130, v18, v18\n"
    "v_mul_f32 v131, v19, v19\n"
    "v_fma_f32 v128, v128, v14, v12\n"
    "v_mfma_f32_32x32x16_bf16 v[64:79], v[116:119], a[4:7], v[64:79]\n"
    "ds_read_b128 v[108:111], v0 offset:7168\n"
    "s_waitcnt lgkmcnt(4)\n"
    "v_fma_f32 v129, v129, v14, v12\n"
    "v_fma_f32 v130, v130, v14, v12\n"
    "v_fma_f32 v131, v131, v14, v12\n"
    "v_mul_f32 v128, v128, v16\n"
    "v_mul_f32 v129, v129, v17\n"
    "v_mfma_f32_32x32x16_bf16 v[64:79], v[120:123], a[8:11], v[64:79]\n"
    "ds_read_b128 v[112:115], v0 offset:8192\n"
    "v_mul_f32 v130, v130, v18\n"
    "v_mul_f32 v131, v131, v19\n"
    "v_exp_f32 v128, v128\n"
    "v_mul_f32 v16, v16, v24\n"
    "v_mfma_f32_32x32x16_bf16 v[64:79], v[124:127], a[12:15], v[64:79]\n"
    "ds_read_b128 v[116:119], v0 offset:9216\n"
    "s_waitcnt lgkmcnt(4)\n"
    "v_exp_f32 v129, v129\n"
    "v_mul_f32 v17, v17, v25\n"
    "v_exp_f32 v130, v130\n"
    "v_mfma_f32_32x32x16_bf16 v[64:79], v[96:99], a[16:19], v[64:79]\n"
    "ds_read_b128 v[120:123], v0 offset:10240\n"
    "v_mul_f32 v18, v18, v26\n"
    "v_exp_f32 v131, v131\n"
    "v_mul_f32 v19, v19, v27\n"
    "v_fma_f32 v128, v128, v13, v13\n"
    "v_mfma_f32_32x32x16_bf16 v[64:79], v[100:103], a[20:23], v[64:79]\n"
    "ds_read_b128 v[124:127], v0 offset:11264\n"
    "s_waitcnt lgkmcnt(4)\n"
    "v_fma_f32 v129, v129, v13, v13\n"
    "v_fma_f32 v130, v130, v13, v13\n"
    "v_fma_f32 v131, v131, v13, v13\n"
    "v_rcp_f32 v128, v128\n"
    "v_mfma_f32_32x32x16_bf16 v[64:79], v[104:107], a[24:27], v[64:79]\n"
    "ds_read_b128 v[96:99], v0 offset:12288\n"
    "v_rcp_f32 v129, v129\n"
    "v_rcp_f32 v130, v130\n"
    "v_mfma_f32_32x32x16_bf16 v[64:79], v[108:111], a[28:31], v[64:79]\n"
    "s_waitcnt lgkmcnt(0)\n"
    "s_barrier\n"
    "ds_read_b128 v[100:103], v0 offset:13312\n"
    "v_rcp_f32 v131, v131\n"
    "v_mul_f32 v16, v16, v128\n"
    "v_mfma_f32_32x32x16_bf16 v[64:79], v[112:115], a[32:35], v[64:79]\n"
    "ds_read_b128 v[104:107], v0 offset:14336\n"
    "s_add_u32 s44, s55, 148480\n"
    "s_add_u32 m0, s56, 57344\n"
    "v_mul_f32 v17, v17, v129\n"
    "v_mul_f32 v18, v18, v130\n"
    "v_mul_f32 v19, v19, v131\n"
    "v_cvt_pk_bf16_f32 v132, v16, v17\n"
    "v_mfma_f32_32x32x16_bf16 v[64:79], v[116:119], a[36:39], v[64:79]\n"
    "ds_read_b128 v[108:111], v0 offset:15360\n"
    "buffer_load_dwordx4 v0, s[4:7], s44 offen offset:0 sc1 lds\n"
    "v_cvt_pk_bf16_f32 v133, v18, v19\n"
    "v_mul_f32 v128, v20, v20\n"
    "v_mul_f32 v129, v21, v21\n"
    "v_mul_f32 v130, v22, v22\n"
    "v_mfma_f32_32x32x16_bf16 v[64:79], v[120:123], a[40:43], v[64:79]\n"
    "ds_read_b128 v[112:115], v0 offset:16384\n"
    "buffer_load_dwordx4 v0, s[4:7], s44 offen offset:1024 sc1 lds\n"
    "v_mul_f32 v131, v23, v23\n"
    "v_fma_f32 v128, v128, v14, v12\n"
    "v_fma_f32 v129, v129, v14, v12\n"
    "v_fma_f32 v130, v130, v14, v12\n"
    "v_mfma_f32_32x32x16_bf16 v[64:79], v[124:127], a[44:47], v[64:79]\n"
    "ds_read_b128 v[116:119], v0 offset:17408\n"
    "buffer_load_dwordx4 v0, s[4:7], s44 offen offset:2048 sc1 lds\n"
    "s_waitcnt lgkmcnt(4)\n"
    "v_fma_f32 v131, v131, v14, v12\n"
    "v_mul_f32 v128, v128, v20\n"
    "v_mul_f32 v129, v129, v21\n"
    "v_mul_f32 v130, v130, v22\n"
    "v_mfma_f32_32x32x16_bf16 v[64:79], v[96:99], a[48:51], v[64:79]\n"
    "ds_read_b128 v[120:123], v0 offset:18432\n"
    "buffer_load_dwordx4 v0, s[4:7], s44 offen offset:3072 sc1 lds\n"
    "v_mul_f32 v131, v131, v23\n"
    "v_exp_f32 v128, v128\n"
    "v_mul_f32 v20, v20, v28\n"
    "v_mfma_f32_32x32x16_bf16 v[64:79], v[100:103], a[52:55], v[64:79]\n"
    "ds_read_b128 v[124:127], v0 offset:19456\n"
    "s_add_u32 m0, s56, 61440\n"
    "s_nop 0\n"
    "buffer_load_dwordx4 v2, s[4:7], s44 offen offset:0 sc1 lds\n"
    "s_waitcnt lgkmcnt(4)\n"
    "v_exp_f32 v129, v129\n"
    "v_mul_f32 v21, v21, v29\n"
    "v_mfma_f32_32x32x16_bf16 v[64:79], v[104:107], a[56:59], v[64:79]\n"
    "ds_read_b128 v[96:99], v0 offset:20480\n"
    "buffer_load_dwordx4 v2, s[4:7], s44 offen offset:1024 sc1 lds\n"
    "v_exp_f32 v130, v130\n"
    "v_mul_f32 v22, v22, v30\n"
    "v_mfma_f32_32x32x16_bf16 v[64:79], v[108:111], a[60:63], v[64:79]\n"
    "ds_read_b128 v[100:103], v0 offset:21504\n"
    "buffer_load_dwordx4 v2, s[4:7], s44 offen offset:2048 sc1 lds\n"
    "s_waitcnt lgkmcnt(4)\n"
    "v_exp_f32 v131, v131\n"
    "v_mul_f32 v23, v23, v31\n"
    "v_fma_f32 v128, v128, v13, v13\n"
    "v_mfma_f32_32x32x16_bf16 v[64:79], v[112:115], a[64:67], v[64:79]\n"
    "ds_read_b128 v[104:107], v0 offset:22528\n"
    "s_add_u32 s44, s55, 178176\n"
    "s_add_u32 m0, s56, 86016\n"
    "v_fma_f32 v129, v129, v13, v13\n"
    "v_fma_f32 v130, v130, v13, v13\n"
    "v_fma_f32 v131, v131, v13, v13\n"
    "v_mfma_f32_32x32x16_bf16 v[64:79], v[116:119], a[68:71], v[64:79]\n"
    "ds_read_b128 v[108:111], v0 offset:23552\n"
    "buffer_load_dwordx4 v0, s[4:7], s44 offen offset:0 sc1 lds\n"
    "s_waitcnt lgkmcnt(4)\n"
    "v_rcp_f32 v128, v128\n"
    "v_rcp_f32 v129, v129\n"
    "v_mfma_f32_32x32x16_bf16 v[64:79], v[120:123], a[72:75], v[64:79]\n"
    "ds_read_b128 v[112:115], v0 offset:24576\n"
    "buffer_load_dwordx4 v0, s[4:7], s44 offen offset:1024 sc1 lds\n"
    "v_rcp_f32 v130, v130\n"
    "v_rcp_f32 v131, v131\n"
    "v_mfma_f32_32x32x16_bf16 v[64:79], v[124:127], a[76:79], v[64:79]\n"
    "ds_read_b128 v[116:119], v0 offset:25600\n"
    "buffer_load_dwordx4 v0, s[4:7], s44 offen offset:2048 sc1 lds\n"
    "s_waitcnt lgkmcnt(4)\n"
    "v_mul_f32 v20, v20, v128\n"
    "v_mul_f32 v21, v21, v129\n"
    "v_mul_f32 v22, v22, v130\n"
    "v_mul_f32 v23, v23, v131\n"
    "v_mfma_f32_32x32x16_bf16 v[64:79], v[96:99], a[80:83], v[64:79]\n"
    "ds_read_b128 v[120:123], v0 offset:26624\n"
    "buffer_load_dwordx4 v0, s[4:7], s44 offen offset:3072 sc1 lds\n"
    "v_cvt_pk_bf16_f32 v134, v20, v21\n"
    "v_cvt_pk_bf16_f32 v135, v22, v23\n"
    "v_mul_f32 v128, v32, v32\n"
    "v_mul_f32 v129, v33, v33\n"
    "v_mfma_f32_32x32x16_bf16 v[64:79], v[100:103], a[84:87], v[64:79]\n"
    "ds_read_b128 v[124:127], v0 offset:27648\n"
    "s_add_u32 m0, s56, 90112\n"
    "s_nop 0\n"
    "buffer_load_dwordx4 v2, s[4:7], s44 offen offset:0 sc1 lds\n"
    "buffer_store_dwordx4 v[132:135], v0, s[4:7], s67 offen offset:0 sc1\n"
    "s_waitcnt lgkmcnt(4)\n"
    "v_mul_f32 v130, v34, v34\n"
    "v_mul_f32 v131, v35, v35\n"
    "v_mfma_f32_32x32x16_bf16 v[64:79], v[104:107], a[88:91], v[64:79]\n"
    "ds_read_b128 v[96:99], v0 offset:28672\n"
    "buffer_load_dwordx4 v2, s[4:7], s44 offen offset:1024 sc1 lds\n"
    "v_fma_f32 v128, v128, v14, v12\n"
    "v_fma_f32 v129, v129, v14, v12\n"
    "v_fma_f32 v130, v130, v14, v12\n"
    "v_fma_f32 v131, v131, v14, v12\n"
    "v_mfma_f32_32x32x16_bf16 v[64:79], v[108:111], a[92:95], v[64:79]\n"
    "ds_read_b128 v[100:103], v0 offset:29696\n"
    "buffer_load_dwordx4 v2, s[4:7], s44 offen offset:2048 sc1 lds\n"
    "s_waitcnt lgkmcnt(4)\n"
    "v_mul_f32 v128, v128, v32\n"
    "v_mul_f32 v129, v129, v33\n"
    "v_mul_f32 v130, v130, v34\n"
    "v_mul_f32 v131, v131, v35\n"
    "v_mfma_f32_32x32x16_bf16 v[64:79], v[112:115], a[240:243], v[64:79]\n"
    "ds_read_b128 v[104:107], v0 offset:30720\n"
    "v_exp_f32 v128, v128\n"
    "v_mul_f32 v32, v32, v40\n"
    "v_exp_f32 v129, v129\n"
    "v_mfma_f32_32x32x16_bf16 v[64:79], v[116:119], a[244:247], v[64:79]\n"
    "ds_read_b128 v[108:111], v0 offset:31744\n"
    "s_waitcnt lgkmcnt(4)\n"
    "v_mul_f32 v33, v33, v41\n"
    "v_exp_f32 v130, v130\n"
    "v_mul_f32 v34, v34, v42\n"
    "v_mfma_f32_32x32x16_bf16 v[64:79], v[120:123], a[248:251], v[64:79]\n"
    "ds_read_b128 v[112:115], v0 offset:32768\n"
    "v_exp_f32 v131, v131\n"
    "v_mul_f32 v35, v35, v43\n"
    "v_fma_f32 v128, v128, v13, v13\n"
    "v_fma_f32 v129, v129, v13, v13\n"
    "v_mfma_f32_32x32x16_bf16 v[64:79], v[124:127], a[252:255], v[64:79]\n"
    "ds_read_b128 v[116:119], v0 offset:33792\n"
    "s_waitcnt vmcnt(32) lgkmcnt(5)\n"
    "v_fma_f32 v130, v130, v13, v13\n"
    "v_fma_f32 v131, v131, v13, v13\n"
    "v_rcp_f32 v128, v128\n"
    "v_mfma_f32_32x32x16_bf16 v[64:79], v[152:155], v[184:187], v[64:79]\n"
    "v_rcp_f32 v129, v129\n"
    "v_rcp_f32 v130, v130\n"
    "v_mfma_f32_32x32x16_bf16 v[80:95], v[96:99], a[0:3], 0\n"
    "ds_read_b128 v[120:123], v0 offset:34816\n"
    "s_waitcnt lgkmcnt(4)\n"
    "v_rcp_f32 v131, v131\n"
    "v_mul_f32 v32, v32, v128\n"
    "v_mul_f32 v33, v33, v129\n"
    "v_mul_f32 v34, v34, v130\n"
    "v_mfma_f32_32x32x16_bf16 v[80:95], v[100:103], a[4:7], v[80:95]\n"
    "ds_read_b128 v[124:127], v0 offset:35840\n"
    "v_mul_f32 v35, v35, v131\n"
    "v_cvt_pk_bf16_f32 v136, v32, v33\n"
    "v_cvt_pk_bf16_f32 v137, v34, v35\n"
    "v_mul_f32 v128, v36, v36\n"
    "v_mul_f32 v129, v37, v37\n"
    "v_mfma_f32_32x32x16_bf16 v[80:95], v[104:107], a[8:11], v[80:95]\n"
    "ds_read_b128 v[96:99], v0 offset:36864\n"
    "s_waitcnt lgkmcnt(4)\n"
    "v_mul_f32 v130, v38, v38\n"
    "v_mul_f32 v131, v39, v39\n"
    "v_fma_f32 v128, v128, v14, v12\n"
    "v_fma_f32 v129, v129, v14, v12\n"
    "v_fma_f32 v130, v130, v14, v12\n"
    "v_mfma_f32_32x32x16_bf16 v[80:95], v[108:111], a[12:15], v[80:95]\n"
    "ds_read_b128 v[100:103], v0 offset:37888\n"
    "v_fma_f32 v131, v131, v14, v12\n"
    "v_mul_f32 v128, v128, v36\n"
    "v_mul_f32 v129, v129, v37\n"
    "v_mul_f32 v130, v130, v38\n"
    "v_mul_f32 v131, v131, v39\n"
    "v_mfma_f32_32x32x16_bf16 v[80:95], v[112:115], a[16:19], v[80:95]\n"
    "ds_read_b128 v[104:107], v0 offset:38912\n"
    "s_waitcnt lgkmcnt(4)\n"
    "v_exp_f32 v128, v128\n"
    "v_mul_f32 v36, v36, v44\n"
    "v_exp_f32 v129, v129\n"
    "v_mfma_f32_32x32x16_bf16 v[80:95], v[116:119], a[20:23], v[80:95]\n"
    "ds_read_b128 v[108:111], v0 offset:39936\n"
    "v_mul_f32 v37, v37, v45\n"
    "v_exp_f32 v130, v130\n"
    "v_mul_f32 v38, v38, v46\n"
    "v_mfma_f32_32x32x16_bf16 v[80:95], v[120:123], a[24:27], v[80:95]\n"
    "ds_read_b128 v[112:115], v0 offset:40960\n"
    "buffer_load_dwordx4 v[192:195], v2, s[4:7], s57 offen offset:0 sc1\n"
    "s_waitcnt lgkmcnt(4)\n"
    "v_exp_f32 v131, v131\n"
    "v_mul_f32 v39, v39, v47\n"
    "v_fma_f32 v128, v128, v13, v13\n"
    "v_mfma_f32_32x32x16_bf16 v[80:95], v[124:127], a[28:31], v[80:95]\n"
    "ds_read_b128 v[116:119], v0 offset:41984\n"
    "v_fma_f32 v129, v129, v13, v13\n"
    "v_fma_f32 v130, v130, v13, v13\n"
    "v_fma_f32 v131, v131, v13, v13\n"
    "v_rcp_f32 v128, v128\n"
    "v_mfma_f32_32x32x16_bf16 v[80:95], v[96:99], a[32:35], v[80:95]\n"
    "ds_read_b128 v[120:123], v0 offset:43008\n"
    "s_waitcnt lgkmcnt(4)\n"
    "v_rcp_f32 v129, v129\n"
    "v_rcp_f32 v130, v130\n"
    "v_mfma_f32_32x32x16_bf16 v[80:95], v[100:103], a[36:39], v[80:95]\n"
    "ds_read_b128 v[124:127], v0 offset:44032\n"
    "buffer_load_dwordx4 v[196:199], v2, s[4:7], s58 offen offset:0 sc1\n"
    "v_rcp_f32 v131, v131\n"
    "v_mul_f32 v36, v36, v128\n"
    "v_mul_f32 v37, v37, v129\n"
    "v_mfma_f32_32x32x16_bf16 v[80:95], v[104:107], a[40:43], v[80:95]\n"
    "ds_read_b128 v[96:99], v0 offset:45056\n"
    "s_waitcnt lgkmcnt(4)\n"
    "v_mul_f32 v38, v38, v130\n"
    "v_mul_f32 v39, v39, v131\n"
    "v_cvt_pk_bf16_f32 v138, v36, v37\n"
    "v_cvt_pk_bf16_f32 v139, v38, v39\n"
    "v_mul_f32 v128, v48, v48\n"
    "v_mfma_f32_32x32x16_bf16 v[80:95], v[108:111], a[44:47], v[80:95]\n"
    "ds_read_b128 v[100:103], v0 offset:46080\n"
    "buffer_store_dwordx4 v[136:139], v0, s[4:7], s67 offen offset:1024 sc1\n"
    "v_mul_f32 v129, v49, v49\n"
    "v_mul_f32 v130, v50, v50\n"
    "v_mul_f32 v131, v51, v51\n"
    "v_fma_f32 v128, v128, v14, v12\n"
    "v_mfma_f32_32x32x16_bf16 v[80:95], v[112:115], a[48:51], v[80:95]\n"
    "ds_read_b128 v[104:107], v0 offset:47104\n"
    "buffer_load_dwordx4 v[200:203], v2, s[4:7], s57 offen offset:2048 sc1\n"
    "s_waitcnt lgkmcnt(4)\n"
    "v_fma_f32 v129, v129, v14, v12\n"
    "v_fma_f32 v130, v130, v14, v12\n"
    "v_fma_f32 v131, v131, v14, v12\n"
    "v_mul_f32 v128, v128, v48\n"
    "v_mfma_f32_32x32x16_bf16 v[80:95], v[116:119], a[52:55], v[80:95]\n"
    "ds_read_b128 v[108:111], v0 offset:48128\n"
    "v_mul_f32 v129, v129, v49\n"
    "v_mul_f32 v130, v130, v50\n"
    "v_mul_f32 v131, v131, v51\n"
    "v_exp_f32 v128, v128\n"
    "v_mfma_f32_32x32x16_bf16 v[80:95], v[120:123], a[56:59], v[80:95]\n"
    "ds_read_b128 v[112:115], v0 offset:49152\n"
    "s_waitcnt lgkmcnt(4)\n"
    "v_mul_f32 v48, v48, v56\n"
    "v_exp_f32 v129, v129\n"
    "v_mul_f32 v49, v49, v57\n"
    "v_mfma_f32_32x32x16_bf16 v[80:95], v[124:127], a[60:63], v[80:95]\n"
    "ds_read_b128 v[116:119], v0 offset:50176\n"
    "buffer_load_dwordx4 v[204:207], v2, s[4:7], s58 offen offset:2048 sc1\n"
    "v_exp_f32 v130, v130\n"
    "v_mul_f32 v50, v50, v58\n"
    "v_mfma_f32_32x32x16_bf16 v[80:95], v[96:99], a[64:67], v[80:95]\n"
    "ds_read_b128 v[120:123], v0 offset:51200\n"
    "s_waitcnt lgkmcnt(4)\n"
    "v_exp_f32 v131, v131\n"
    "v_mul_f32 v51, v51, v59\n"
    "v_fma_f32 v128, v128, v13, v13\n"
    "v_fma_f32 v129, v129, v13, v13\n"
    "v_mfma_f32_32x32x16_bf16 v[80:95], v[100:103], a[68:71], v[80:95]\n"
    "ds_read_b128 v[124:127], v0 offset:52224\n"
    "v_fma_f32 v130, v130, v13, v13\n"
    "v_fma_f32 v131, v131, v13, v13\n"
    "v_rcp_f32 v128, v128\n"
    "v_mfma_f32_32x32x16_bf16 v[80:95], v[104:107], a[72:75], v[80:95]\n"
    "ds_read_b128 v[96:99], v0 offset:53248\n"
    "buffer_load_dwordx4 v[208:211], v3, s[4:7], s57 offen offset:0 sc1\n"
    "s_waitcnt lgkmcnt(4)\n"
    "v_rcp_f32 v129, v129\n"
    "v_rcp_f32 v130, v130\n"
    "v_mfma_f32_32x32x16_bf16 v[80:95], v[108:111], a[76:79], v[80:95]\n"
    "ds_read_b128 v[100:103], v0 offset:54272\n"
    "v_rcp_f32 v131, v131\n"
    "v_mul_f32 v48, v48, v128\n"
    "v_mul_f32 v49, v49, v129\n"
    "v_mul_f32 v50, v50, v130\n"
    "v_mfma_f32_32x32x16_bf16 v[80:95], v[112:115], a[80:83], v[80:95]\n"
    "ds_read_b128 v[104:107], v0 offset:55296\n"
    "s_waitcnt lgkmcnt(4)\n"
    "v_mul_f32 v51, v51, v131\n"
    "v_cvt_pk_bf16_f32 v132, v48, v49\n"
    "v_cvt_pk_bf16_f32 v133, v50, v51\n"
    "v_mul_f32 v128, v52, v52\n"
    "v_mul_f32 v129, v53, v53\n"
    "v_mfma_f32_32x32x16_bf16 v[80:95], v[116:119], a[84:87], v[80:95]\n"
    "s_waitcnt vmcnt(14)\n"
    "s_waitcnt lgkmcnt(0)\n"
    "s_barrier\n"
    "buffer_load_dwordx4 v[212:215], v3, s[4:7], s58 offen offset:0 sc1\n"
    "ds_read_b128 v[108:111], v0 offset:56320\n"
    "v_mul_f32 v130, v54, v54\n"
    "v_mul_f32 v131, v55, v55\n"
    "v_mfma_f32_32x32x16_bf16 v[80:95], v[120:123], a[88:91], v[80:95]\n"
    "ds_read_b128 v[112:115], v5 offset:0\n"
    "v_fma_f32 v128, v128, v14, v12\n"
    "v_fma_f32 v129, v129, v14, v12\n"
    "v_fma_f32 v130, v130, v14, v12\n"
    "v_fma_f32 v131, v131, v14, v12\n"
    "v_mul_f32 v128, v128, v52\n"
    "v_mfma_f32_32x32x16_bf16 v[80:95], v[124:127], a[92:95], v[80:95]\n"
    "ds_read_b128 v[116:119], v5 offset:1024\n"
    "v_mul_f32 v129, v129, v53\n"
    "v_mul_f32 v130, v130, v54\n"
    "v_mul_f32 v131, v131, v55\n"
    "v_exp_f32 v128, v128\n"
    "v_mfma_f32_32x32x16_bf16 v[80:95], v[96:99], a[240:243], v[80:95]\n"
    "ds_read_b128 v[120:123], v5 offset:2048\n"
    "buffer_load_dwordx4 v[216:219], v3, s[4:7], s57 offen offset:2048 sc1\n"
    "v_mul_f32 v52, v52, v60\n"
    "v_exp_f32 v129, v129\n"
    "v_mul_f32 v53, v53, v61\n"
    "v_mfma_f32_32x32x16_bf16 v[80:95], v[100:103], a[244:247], v[80:95]\n"
    "ds_read_b128 v[124:127], v5 offset:3072\n"
    "v_exp_f32 v130, v130\n"
    "v_mul_f32 v54, v54, v62\n"
    "v_exp_f32 v131, v131\n"
    "v_mfma_f32_32x32x16_bf16 v[80:95], v[104:107], a[248:251], v[80:95]\n"
    "ds_read_b128 v[96:99], v5 offset:4096\n"
    "s_waitcnt lgkmcnt(5)\n"
    "v_mul_f32 v55, v55, v63\n"
    "v_fma_f32 v128, v128, v13, v13\n"
    "v_fma_f32 v129, v129, v13, v13\n"
    "v_fma_f32 v130, v130, v13, v13\n"
    "v_fma_f32 v131, v131, v13, v13\n"
    "v_mfma_f32_32x32x16_bf16 v[80:95], v[108:111], a[252:255], v[80:95]\n"
    "ds_read_b128 v[100:103], v5 offset:5120\n"
    "buffer_load_dwordx4 v[220:223], v3, s[4:7], s58 offen offset:2048 sc1\n"
    "v_rcp_f32 v128, v128\n"
    "v_rcp_f32 v129, v129\n"
    "v_mfma_f32_32x32x16_bf16 v[80:95], v[156:159], v[184:187], v[80:95]\n"
    "s_waitcnt lgkmcnt(4)\n"
    "v_rcp_f32 v130, v130\n"
    "v_rcp_f32 v131, v131\n"
    "v_mul_f32 v52, v52, v128\n"
    "v_mfma_f32_32x32x16_bf16 v[16:31], v[112:115], a[0:3], 0\n"
    "ds_read_b128 v[104:107], v5 offset:6144\n"
    "v_mul_f32 v53, v53, v129\n"
    "v_mul_f32 v54, v54, v130\n"
    "v_mul_f32 v55, v55, v131\n"
    "v_cvt_pk_bf16_f32 v134, v52, v53\n"
    "v_cvt_pk_bf16_f32 v135, v54, v55\n"
    "v_mfma_f32_32x32x16_bf16 v[16:31], v[116:119], a[4:7], v[16:31]\n"
    "ds_read_b128 v[108:111], v5 offset:7168\n"
    "buffer_load_dwordx4 v[224:227], v4, s[4:7], s57 offen offset:0 sc1\n"
    "buffer_store_dwordx4 v[132:135], v0, s[4:7], s67 offen offset:2048 sc1\n"
    "s_waitcnt lgkmcnt(4)\n"
    "v_mul_f32 v128, v64, v64\n"
    "v_mul_f32 v129, v65, v65\n"
    "v_mul_f32 v130, v66, v66\n"
    "v_mfma_f32_32x32x16_bf16 v[16:31], v[120:123], a[8:11], v[16:31]\n"
    "ds_read_b128 v[112:115], v5 offset:8192\n"
    "v_mul_f32 v131, v67, v67\n"
    "v_fma_f32 v128, v128, v14, v12\n"
    "v_fma_f32 v129, v129, v14, v12\n"
    "v_fma_f32 v130, v130, v14, v12\n"
    "v_fma_f32 v131, v131, v14, v12\n"
    "v_mfma_f32_32x32x16_bf16 v[16:31], v[124:127], a[12:15], v[16:31]\n"
    "ds_read_b128 v[116:119], v5 offset:9216\n"
    "s_waitcnt lgkmcnt(4)\n"
    "v_mul_f32 v128, v128, v64\n"
    "v_mul_f32 v129, v129, v65\n"
    "v_mul_f32 v130, v130, v66\n"
    "v_mul_f32 v131, v131, v67\n"
    "v_mfma_f32_32x32x16_bf16 v[16:31], v[96:99], a[16:19], v[16:31]\n"
    "ds_read_b128 v[120:123], v5 offset:10240\n"
    "buffer_load_dwordx4 v[228:231], v4, s[4:7], s58 offen offset:0 sc1\n"
    "v_exp_f32 v128, v128\n"
    "v_mul_f32 v64, v64, v72\n"
    "v_mfma_f32_32x32x16_bf16 v[16:31], v[100:103], a[20:23], v[16:31]\n"
    "ds_read_b128 v[124:127], v5 offset:11264\n"
    "s_waitcnt lgkmcnt(4)\n"
    "v_exp_f32 v129, v129\n"
    "v_mul_f32 v65, v65, v73\n"
    "v_exp_f32 v130, v130\n"
    "v_mfma_f32_32x32x16_bf16 v[16:31], v[104:107], a[24:27], v[16:31]\n"
    "ds_read_b128 v[96:99], v5 offset:12288\n"
    "v_mul_f32 v66, v66, v74\n"
    "v_exp_f32 v131, v131\n"
    "v_mul_f32 v67, v67, v75\n"
    "v_fma_f32 v128, v128, v13, v13\n"
    "v_mfma_f32_32x32x16_bf16 v[16:31], v[108:111], a[28:31], v[16:31]\n"
    "ds_read_b128 v[100:103], v5 offset:13312\n"
    "buffer_load_dwordx4 v[232:235], v4, s[4:7], s57 offen offset:2048 sc1\n"
    "s_waitcnt lgkmcnt(4)\n"
    "v_fma_f32 v129, v129, v13, v13\n"
    "v_fma_f32 v130, v130, v13, v13\n"
    "v_fma_f32 v131, v131, v13, v13\n"
    "v_mfma_f32_32x32x16_bf16 v[16:31], v[112:115], a[32:35], v[16:31]\n"
    "ds_read_b128 v[104:107], v5 offset:14336\n"
    "v_rcp_f32 v128, v128\n"
    "v_rcp_f32 v129, v129\n"
    "v_mfma_f32_32x32x16_bf16 v[16:31], v[116:119], a[36:39], v[16:31]\n"
    "ds_read_b128 v[108:111], v5 offset:15360\n"
    "s_waitcnt lgkmcnt(4)\n"
    "v_rcp_f32 v130, v130\n"
    "v_rcp_f32 v131, v131\n"
    "v_mul_f32 v64, v64, v128\n"
    "v_mfma_f32_32x32x16_bf16 v[16:31], v[120:123], a[40:43], v[16:31]\n"
    "ds_read_b128 v[112:115], v5 offset:16384\n"
    "buffer_load_dwordx4 v[236:239], v4, s[4:7], s58 offen offset:2048 sc1\n"
    "v_mul_f32 v65, v65, v129\n"
    "v_mul_f32 v66, v66, v130\n"
    "v_mul_f32 v67, v67, v131\n"
    "v_cvt_pk_bf16_f32 v136, v64, v65\n"
    "v_mfma_f32_32x32x16_bf16 v[16:31], v[124:127], a[44:47], v[16:31]\n"
    "ds_read_b128 v[116:119], v5 offset:17408\n"
    "s_waitcnt lgkmcnt(4)\n"
    "v_cvt_pk_bf16_f32 v137, v66, v67\n"
    "v_mul_f32 v128, v68, v68\n"
    "v_mul_f32 v129, v69, v69\n"
    "v_mul_f32 v130, v70, v70\n"
    "v_mul_f32 v131, v71, v71\n"
    "v_mfma_f32_32x32x16_bf16 v[16:31], v[96:99], a[48:51], v[16:31]\n"
    "ds_read_b128 v[120:123], v5 offset:18432\n"
    "v_fma_f32 v128, v128, v14, v12\n"
    "v_fma_f32 v129, v129, v14, v12\n"
    "v_fma_f32 v130, v130, v14, v12\n"
    "v_fma_f32 v131, v131, v14, v12\n"
    "v_mul_f32 v128, v128, v68\n"
    "v_mfma_f32_32x32x16_bf16 v[16:31], v[100:103], a[52:55], v[16:31]\n"
    "ds_read_b128 v[124:127], v5 offset:19456\n"
    "s_add_u32 s57, s57, 16384\n"
    "s_add_u32 s58, s58, 16384\n"
    "buffer_load_dwordx4 v[168:171], v0, s[4:7], s57 offen offset:0 sc1\n"
    "s_waitcnt lgkmcnt(4)\n"
    "v_mul_f32 v129, v129, v69\n"
    "v_mul_f32 v130, v130, v70\n"
    "v_mul_f32 v131, v131, v71\n"
    "v_mfma_f32_32x32x16_bf16 v[16:31], v[104:107], a[56:59], v[16:31]\n"
    "ds_read_b128 v[96:99], v5 offset:20480\n"
    "v_exp_f32 v128, v128\n"
    "v_mul_f32 v68, v68, v76\n"
    "v_exp_f32 v129, v129\n"
    "v_mfma_f32_32x32x16_bf16 v[16:31], v[108:111], a[60:63], v[16:31]\n"
    "ds_read_b128 v[100:103], v5 offset:21504\n"
    "s_waitcnt lgkmcnt(4)\n"
    "v_mul_f32 v69, v69, v77\n"
    "v_exp_f32 v130, v130\n"
    "v_mul_f32 v70, v70, v78\n"
    "v_mfma_f32_32x32x16_bf16 v[16:31], v[112:115], a[64:67], v[16:31]\n"
    "ds_read_b128 v[104:107], v5 offset:22528\n"
    "buffer_load_dwordx4 v[172:175], v0, s[4:7], s58 offen offset:0 sc1\n"
    "v_exp_f32 v131, v131\n"
    "v_mul_f32 v71, v71, v79\n"
    "v_fma_f32 v128, v128, v13, v13\n"
    "v_mfma_f32_32x32x16_bf16 v[16:31], v[116:119], a[68:71], v[16:31]\n"
    "ds_read_b128 v[108:111], v5 offset:23552\n"
    "s_waitcnt lgkmcnt(4)\n"
    "v_fma_f32 v129, v129, v13, v13\n"
    "v_fma_f32 v130, v130, v13, v13\n"
    "v_fma_f32 v131, v131, v13, v13\n"
    "v_rcp_f32 v128, v128\n"
    "v_mfma_f32_32x32x16_bf16 v[16:31], v[120:123], a[72:75], v[16:31]\n"
    "ds_read_b128 v[112:115], v5 offset:24576\n"
    "v_rcp_f32 v129, v129\n"
    "v_rcp_f32 v130, v130\n"
    "v_mfma_f32_32x32x16_bf16 v[16:31], v[124:127], a[76:79], v[16:31]\n"
    "ds_read_b128 v[116:119], v5 offset:25600\n"
    "buffer_load_dwordx4 v[176:179], v0, s[4:7], s57 offen offset:2048 sc1\n"
    "s_waitcnt lgkmcnt(4)\n"
    "v_rcp_f32 v131, v131\n"
    "v_mul_f32 v68, v68, v128\n"
    "v_mul_f32 v69, v69, v129\n"
    "v_mfma_f32_32x32x16_bf16 v[16:31], v[96:99], a[80:83], v[16:31]\n"
    "ds_read_b128 v[120:123], v5 offset:26624\n"
    "v_mul_f32 v70, v70, v130\n"
    "v_mul_f32 v71, v71, v131\n"
    "v_cvt_pk_bf16_f32 v138, v68, v69\n"
    "v_cvt_pk_bf16_f32 v139, v70, v71\n"
    "v_mul_f32 v128, v80, v80\n"
    "v_mfma_f32_32x32x16_bf16 v[16:31], v[100:103], a[84:87], v[16:31]\n"
    "s_waitcnt vmcnt(17)\n"
    "s_waitcnt lgkmcnt(0)\n"
    "s_barrier\n"
    "buffer_store_dwordx4 v[136:139], v0, s[4:7], s67 offen offset:3072 sc1\n"
    "ds_read_b128 v[124:127], v5 offset:27648\n"
    "v_mul_f32 v129, v81, v81\n"
    "v_mul_f32 v130, v82, v82\n"
    "v_mfma_f32_32x32x16_bf16 v[16:31], v[104:107], a[88:91], v[16:31]\n"
    "ds_read_b128 v[96:99], v5 offset:28672\n"
    "buffer_load_dwordx4 v[180:183], v0, s[4:7], s58 offen offset:2048 sc1\n"
    "v_mul_f32 v131, v83, v83\n"
    "v_fma_f32 v128, v128, v14, v12\n"
    "v_fma_f32 v129, v129, v14, v12\n"
    "v_fma_f32 v130, v130, v14, v12\n"
    "v_mfma_f32_32x32x16_bf16 v[16:31], v[108:111], a[92:95], v[16:31]\n"
    "ds_read_b128 v[100:103], v5 offset:29696\n"
    "v_fma_f32 v131, v131, v14, v12\n"
    "v_mul_f32 v128, v128, v80\n"
    "v_mul_f32 v129, v129, v81\n"
    "v_mul_f32 v130, v130, v82\n"
    "v_mul_f32 v131, v131, v83\n"
    "v_mfma_f32_32x32x16_bf16 v[16:31], v[112:115], a[240:243], v[16:31]\n"
    "ds_read_b128 v[104:107], v5 offset:30720\n"
    "v_exp_f32 v128, v128\n"
    "v_mul_f32 v80, v80, v88\n"
    "v_exp_f32 v129, v129\n"
    "v_mfma_f32_32x32x16_bf16 v[16:31], v[116:119], a[244:247], v[16:31]\n"
    "ds_read_b128 v[108:111], v5 offset:31744\n"
    "buffer_load_dwordx4 v[140:143], v2, s[4:7], s57 offen offset:0 sc1\n"
    "s_waitcnt lgkmcnt(4)\n"
    "v_mul_f32 v81, v81, v89\n"
    "v_exp_f32 v130, v130\n"
    "v_mul_f32 v82, v82, v90\n"
    "v_mfma_f32_32x32x16_bf16 v[16:31], v[120:123], a[248:251], v[16:31]\n"
    "ds_read_b128 v[112:115], v5 offset:32768\n"
    "v_exp_f32 v131, v131\n"
    "v_mul_f32 v83, v83, v91\n"
    "v_fma_f32 v128, v128, v13, v13\n"
    "v_fma_f32 v129, v129, v13, v13\n"
    "v_mfma_f32_32x32x16_bf16 v[16:31], v[124:127], a[252:255], v[16:31]\n"
    "ds_read_b128 v[116:119], v5 offset:33792\n"
    "s_waitcnt lgkmcnt(5)\n"
    "v_fma_f32 v130, v130, v13, v13\n"
    "v_fma_f32 v131, v131, v13, v13\n"
    "v_rcp_f32 v128, v128\n"
    "v_mfma_f32_32x32x16_bf16 v[16:31], v[160:163], v[184:187], v[16:31]\n"
    "buffer_load_dwordx4 v[144:147], v2, s[4:7], s58 offen offset:0 sc1\n"
    "v_rcp_f32 v129, v129\n"
    "v_rcp_f32 v130, v130\n"
    "v_mfma_f32_32x32x16_bf16 v[32:47], v[96:99], a[0:3], 0\n"
    "ds_read_b128 v[120:123], v5 offset:34816\n"
    "s_waitcnt lgkmcnt(4)\n"
    "v_rcp_f32 v131, v131\n"
    "v_mul_f32 v80, v80, v128\n"
    "v_mul_f32 v81, v81, v129\n"
    "v_mul_f32 v82, v82, v130\n"
    "v_mfma_f32_32x32x16_bf16 v[32:47], v[100:103], a[4:7], v[32:47]\n"
    "ds_read_b128 v[124:127], v5 offset:35840\n"
    "v_mul_f32 v83, v83, v131\n"
    "v_cvt_pk_bf16_f32 v132, v80, v81\n"
    "v_cvt_pk_bf16_f32 v133, v82, v83\n"
    "v_mul_f32 v128, v84, v84\n"
    "v_mul_f32 v129, v85, v85\n"
    "v_mfma_f32_32x32x16_bf16 v[32:47], v[104:107], a[8:11], v[32:47]\n"
    "ds_read_b128 v[96:99], v5 offset:36864\n"
    "buffer_load_dwordx4 v[148:151], v2, s[4:7], s57 offen offset:2048 sc1\n"
    "s_waitcnt lgkmcnt(4)\n"
    "v_mul_f32 v130, v86, v86\n"
    "v_mul_f32 v131, v87, v87\n"
    "v_fma_f32 v128, v128, v14, v12\n"
    "v_fma_f32 v129, v129, v14, v12\n"
    "v_mfma_f32_32x32x16_bf16 v[32:47], v[108:111], a[12:15], v[32:47]\n"
    "ds_read_b128 v[100:103], v5 offset:37888\n"
    "v_fma_f32 v130, v130, v14, v12\n"
    "v_fma_f32 v131, v131, v14, v12\n"
    "v_mul_f32 v128, v128, v84\n"
    "v_mul_f32 v129, v129, v85\n"
    "v_mul_f32 v130, v130, v86\n"
    "v_mfma_f32_32x32x16_bf16 v[32:47], v[112:115], a[16:19], v[32:47]\n"
    "ds_read_b128 v[104:107], v5 offset:38912\n"
    "s_waitcnt lgkmcnt(4)\n"
    "v_mul_f32 v131, v131, v87\n"
    "v_exp_f32 v128, v128\n"
    "v_mul_f32 v84, v84, v92\n"
    "v_mfma_f32_32x32x16_bf16 v[32:47], v[116:119], a[20:23], v[32:47]\n"
    "ds_read_b128 v[108:111], v5 offset:39936\n"
    "buffer_load_dwordx4 v[8:11], v2, s[4:7], s58 offen offset:2048 sc1\n"
    "v_exp_f32 v129, v129\n"
    "v_mul_f32 v85, v85, v93\n"
    "v_mfma_f32_32x32x16_bf16 v[32:47], v[120:123], a[24:27], v[32:47]\n"
    "ds_read_b128 v[112:115], v5 offset:40960\n"
    "s_waitcnt lgkmcnt(4)\n"
    "v_exp_f32 v130, v130\n"
    "v_mul_f32 v86, v86, v94\n"
    "v_exp_f32 v131, v131\n"
    "v_mfma_f32_32x32x16_bf16 v[32:47], v[124:127], a[28:31], v[32:47]\n"
    "ds_read_b128 v[116:119], v5 offset:41984\n"
    "v_mul_f32 v87, v87, v95\n"
    "v_fma_f32 v128, v128, v13, v13\n"
    "v_fma_f32 v129, v129, v13, v13\n"
    "v_fma_f32 v130, v130, v13, v13\n"
    "v_fma_f32 v131, v131, v13, v13\n"
    "v_mfma_f32_32x32x16_bf16 v[32:47], v[96:99], a[32:35], v[32:47]\n"
    "ds_read_b128 v[120:123], v5 offset:43008\n"
    "s_waitcnt lgkmcnt(4)\n"
    "v_rcp_f32 v128, v128\n"
    "v_rcp_f32 v129, v129\n"
    "v_mfma_f32_32x32x16_bf16 v[32:47], v[100:103], a[36:39], v[32:47]\n"
    "ds_read_b128 v[124:127], v5 offset:44032\n"
    "v_rcp_f32 v130, v130\n"
    "v_rcp_f32 v131, v131\n"
    "v_mul_f32 v84, v84, v128\n"
    "v_mfma_f32_32x32x16_bf16 v[32:47], v[104:107], a[40:43], v[32:47]\n"
    "ds_read_b128 v[96:99], v5 offset:45056\n"
    "s_waitcnt lgkmcnt(4)\n"
    "v_mul_f32 v85, v85, v129\n"
    "v_mul_f32 v86, v86, v130\n"
    "v_mul_f32 v87, v87, v131\n"
    "v_cvt_pk_bf16_f32 v134, v84, v85\n"
    "v_cvt_pk_bf16_f32 v135, v86, v87\n"
    "v_mfma_f32_32x32x16_bf16 v[32:47], v[108:111], a[44:47], v[32:47]\n"
    "ds_read_b128 v[100:103], v5 offset:46080\n"
    "buffer_store_dwordx4 v[132:135], v2, s[4:7], s67 offen offset:0 sc1\n"
    "v_mul_f32 v128, v16, v16\n"
    "v_mul_f32 v129, v17, v17\n"
    "v_mul_f32 v130, v18, v18\n"
    "v_mul_f32 v131, v19, v19\n"
    "v_mfma_f32_32x32x16_bf16 v[32:47], v[112:115], a[48:51], v[32:47]\n"
    "ds_read_b128 v[104:107], v5 offset:47104\n"
    "s_waitcnt lgkmcnt(4)\n"
    "v_fma_f32 v128, v128, v14, v12\n"
    "v_fma_f32 v129, v129, v14, v12\n"
    "v_fma_f32 v130, v130, v14, v12\n"
    "v_fma_f32 v131, v131, v14, v12\n"
    "v_mul_f32 v128, v128, v16\n"
    "v_mfma_f32_32x32x16_bf16 v[32:47], v[116:119], a[52:55], v[32:47]\n"
    "ds_read_b128 v[108:111], v5 offset:48128\n"
    "v_mul_f32 v129, v129, v17\n"
    "v_mul_f32 v130, v130, v18\n"
    "v_mul_f32 v131, v131, v19\n"
    "v_exp_f32 v128, v128\n"
    "v_mfma_f32_32x32x16_bf16 v[32:47], v[120:123], a[56:59], v[32:47]\n"
    "ds_read_b128 v[112:115], v5 offset:49152\n"
    "s_waitcnt lgkmcnt(4)\n"
    "v_mul_f32 v16, v16, v24\n"
    "v_exp_f32 v129, v129\n"
    "v_mul_f32 v17, v17, v25\n"
    "v_mfma_f32_32x32x16_bf16 v[32:47], v[124:127], a[60:63], v[32:47]\n"
    "ds_read_b128 v[116:119], v5 offset:50176\n"
    "v_exp_f32 v130, v130\n"
    "v_mul_f32 v18, v18, v26\n"
    "v_exp_f32 v131, v131\n"
    "v_mfma_f32_32x32x16_bf16 v[32:47], v[96:99], a[64:67], v[32:47]\n"
    "ds_read_b128 v[120:123], v5 offset:51200\n"
    "s_waitcnt lgkmcnt(4)\n"
    "v_mul_f32 v19, v19, v27\n"
    "v_fma_f32 v128, v128, v13, v13\n"
    "v_fma_f32 v129, v129, v13, v13\n"
    "v_fma_f32 v130, v130, v13, v13\n"
    "v_fma_f32 v131, v131, v13, v13\n"
    "v_mfma_f32_32x32x16_bf16 v[32:47], v[100:103], a[68:71], v[32:47]\n"
    "ds_read_b128 v[124:127], v5 offset:52224\n"
    "v_rcp_f32 v128, v128\n"
    "v_rcp_f32 v129, v129\n"
    "v_mfma_f32_32x32x16_bf16 v[32:47], v[104:107], a[72:75], v[32:47]\n"
    "ds_read_b128 v[96:99], v5 offset:53248\n"
    "s_waitcnt lgkmcnt(4)\n"
    "v_rcp_f32 v130, v130\n"
    "v_rcp_f32 v131, v131\n"
    "v_mul_f32 v16, v16, v128\n"
    "v_mfma_f32_32x32x16_bf16 v[32:47], v[108:111], a[76:79], v[32:47]\n"
    "ds_read_b128 v[100:103], v5 offset:54272\n"
    "v_mul_f32 v17, v17, v129\n"
    "v_mul_f32 v18, v18, v130\n"
    "v_mul_f32 v19, v19, v131\n"
    "v_cvt_pk_bf16_f32 v136, v16, v17\n"
    "v_cvt_pk_bf16_f32 v137, v18, v19\n"
    "v_mfma_f32_32x32x16_bf16 v[32:47], v[112:115], a[80:83], v[32:47]\n"
    "ds_read_b128 v[104:107], v5 offset:55296\n"
    "s_waitcnt lgkmcnt(4)\n"
    "v_mul_f32 v128, v20, v20\n"
    "v_mul_f32 v129, v21, v21\n"
    "v_mul_f32 v130, v22, v22\n"
    "v_mul_f32 v131, v23, v23\n"
    "v_fma_f32 v128, v128, v14, v12\n"
    "v_mfma_f32_32x32x16_bf16 v[32:47], v[116:119], a[84:87], v[32:47]\n"
    "ds_read_b128 v[108:111], v5 offset:56320\n"
    "v_fma_f32 v129, v129, v14, v12\n"
    "v_fma_f32 v130, v130, v14, v12\n"
    "v_fma_f32 v131, v131, v14, v12\n"
    "v_mul_f32 v128, v128, v20\n"
    "v_mul_f32 v129, v129, v21\n"
    "v_mfma_f32_32x32x16_bf16 v[32:47], v[120:123], a[88:91], v[32:47]\n"
    "s_waitcnt lgkmcnt(3)\n"
    "v_mul_f32 v130, v130, v22\n"
    "v_mul_f32 v131, v131, v23\n"
    "v_exp_f32 v128, v128\n"
    "v_mul_f32 v20, v20, v28\n"
    "v_mfma_f32_32x32x16_bf16 v[32:47], v[124:127], a[92:95], v[32:47]\n"
    "v_exp_f32 v129, v129\n"
    "v_mul_f32 v21, v21, v29\n"
    "v_exp_f32 v130, v130\n"
    "v_mfma_f32_32x32x16_bf16 v[32:47], v[96:99], a[240:243], v[32:47]\n"
    "s_waitcnt lgkmcnt(1)\n"
    "v_mul_f32 v22, v22, v30\n"
    "v_exp_f32 v131, v131\n"
    "v_mul_f32 v23, v23, v31\n"
    "v_fma_f32 v128, v128, v13, v13\n"
    "v_mfma_f32_32x32x16_bf16 v[32:47], v[100:103], a[244:247], v[32:47]\n"
    "v_fma_f32 v129, v129, v13, v13\n"
    "v_fma_f32 v130, v130, v13, v13\n"
    "v_fma_f32 v131, v131, v13, v13\n"
    "v_rcp_f32 v128, v128\n"
    "v_mfma_f32_32x32x16_bf16 v[32:47], v[104:107], a[248:251], v[32:47]\n"
    "s_waitcnt lgkmcnt(0)\n"
    "v_rcp_f32 v129, v129\n"
    "v_rcp_f32 v130, v130\n"
    "v_mfma_f32_32x32x16_bf16 v[32:47], v[108:111], a[252:255], v[32:47]\n"
    "v_rcp_f32 v131, v131\n"
    "v_mul_f32 v20, v20, v128\n"
    "v_mul_f32 v21, v21, v129\n"
    "v_mul_f32 v22, v22, v130\n"
    "v_mfma_f32_32x32x16_bf16 v[32:47], v[164:167], v[184:187], v[32:47]\n"
    "v_mul_f32 v23, v23, v131\n"
    "v_cvt_pk_bf16_f32 v138, v20, v21\n"
    "v_cvt_pk_bf16_f32 v139, v22, v23\n"
    "buffer_store_dwordx4 v[136:139], v2, s[4:7], s67 offen offset:1024 sc1\n"
    "s_nop 7\n"
    "v_mul_f32 v128, v32, v32\n"
    "v_mul_f32 v129, v33, v33\n"
    "v_mul_f32 v130, v34, v34\n"
    "v_mul_f32 v131, v35, v35\n"
    "v_fma_f32 v128, v128, v14, v12\n"
    "v_fma_f32 v129, v129, v14, v12\n"
    "v_fma_f32 v130, v130, v14, v12\n"
    "v_fma_f32 v131, v131, v14, v12\n"
    "v_mul_f32 v128, v128, v32\n"
    "v_mul_f32 v129, v129, v33\n"
    "v_mul_f32 v130, v130, v34\n"
    "v_mul_f32 v131, v131, v35\n"
    "v_exp_f32 v128, v128\n"
    "v_mul_f32 v32, v32, v40\n"
    "v_exp_f32 v129, v129\n"
    "v_mul_f32 v33, v33, v41\n"
    "v_exp_f32 v130, v130\n"
    "v_mul_f32 v34, v34, v42\n"
    "v_exp_f32 v131, v131\n"
    "v_mul_f32 v35, v35, v43\n"
    "v_fma_f32 v128, v128, v13, v13\n"
    "v_fma_f32 v129, v129, v13, v13\n"
    "v_fma_f32 v130, v130, v13, v13\n"
    "v_fma_f32 v131, v131, v13, v13\n"
    "v_rcp_f32 v128, v128\n"
    "v_rcp_f32 v129, v129\n"
    "v_rcp_f32 v130, v130\n"
    "v_rcp_f32 v131, v131\n"
    "v_mul_f32 v32, v32, v128\n"
    "v_mul_f32 v33, v33, v129\n"
    "v_mul_f32 v34, v34, v130\n"
    "v_mul_f32 v35, v35, v131\n"
    "v_cvt_pk_bf16_f32 v132, v32, v33\n"
    "v_cvt_pk_bf16_f32 v133, v34, v35\n"
    "v_mul_f32 v128, v36, v36\n"
    "v_mul_f32 v129, v37, v37\n"
    "v_mul_f32 v130, v38, v38\n"
    "v_mul_f32 v131, v39, v39\n"
    "v_fma_f32 v128, v128, v14, v12\n"
    "v_fma_f32 v129, v129, v14, v12\n"
    "v_fma_f32 v130, v130, v14, v12\n"
    "v_fma_f32 v131, v131, v14, v12\n"
    "v_mul_f32 v128, v128, v36\n"
    "v_mul_f32 v129, v129, v37\n"
    "v_mul_f32 v130, v130, v38\n"
    "v_mul_f32 v131, v131, v39\n"
    "v_exp_f32 v128, v128\n"
    "v_mul_f32 v36, v36, v44\n"
    "v_exp_f32 v129, v129\n"
    "v_mul_f32 v37, v37, v45\n"
    "v_exp_f32 v130, v130\n"
    "v_mul_f32 v38, v38, v46\n"
    "v_exp_f32 v131, v131\n"
    "v_mul_f32 v39, v39, v47\n"
    "v_fma_f32 v128, v128, v13, v13\n"
    "v_fma_f32 v129, v129, v13, v13\n"
    "v_fma_f32 v130, v130, v13, v13\n"
    "v_fma_f32 v131, v131, v13, v13\n"
    "v_rcp_f32 v128, v128\n"
    "v_rcp_f32 v129, v129\n"
    "v_rcp_f32 v130, v130\n"
    "v_rcp_f32 v131, v131\n"
    "v_mul_f32 v36, v36, v128\n"
    "v_mul_f32 v37, v37, v129\n"
    "v_mul_f32 v38, v38, v130\n"
    "v_mul_f32 v39, v39, v131\n"
    "v_cvt_pk_bf16_f32 v134, v36, v37\n"
    "v_cvt_pk_bf16_f32 v135, v38, v39\n"
    "buffer_store_dwordx4 v[132:135], v2, s[4:7], s67 offen offset:2048 sc1\n"
    "s_add_u32 s44, s66, 49152\n"
    "v_lshlrev_b32 v6, 3, v1\n"
    "v_add_u32 v6, s44, v6\n"
    "v_mov_b32 v254, s16\n"
    "v_mov_b32 v255, s17\n"
    "s_waitcnt vmcnt(0)\n"
    "s_mov_b64 exec, 1\n"
    "buffer_atomic_swap_x2 v[254:255], off, s[4:7], s41 sc1\n"
    "s_mov_b64 exec, -1\n"
    "s_cmp_ge_u32 s20, 28\n"
    "s_cbranch_scc1 Lend_28%=\n"
    "s_mov_b32 s70, 0\n"
    "s_mov_b64 exec, 255\n"
    "Lpoll_30%=:\n"
    "v_mov_b32 v184, 0\n"
    "v_mov_b32 v185, 0\n"
    "buffer_atomic_or_x2 v[184:185], v6, s[4:7], 0 offen sc0 sc1\n"
    "s_waitcnt vmcnt(0)\n"
    "v_cmp_ne_u64 vcc, s[16:17], v[184:185]\n"
    "s_nop 0\n"
    "s_and_b64 exec, exec, vcc\n"
    "s_cbranch_scc0 Lpollok_31%=\n"
    "s_sleep 2\n"
    "s_add_u32 s70, s70, 1\n"
    "s_cmp_lt_u32 s70, 0x40000\n"
    "s_cbranch_scc1 Lpoll_30%=\n"
    "Lpollok_31%=:\n"
    "s_mov_b64 exec, -1\n"
    "buffer_load_dwordx4 v[48:51], v0, s[4:7], s26 offen offset:0 sc1\n"
    "buffer_load_dwordx4 v[52:55], v2, s[4:7], s26 offen offset:3072 sc1\n"
    "buffer_load_dwordx4 v[56:59], v4, s[4:7], s26 offen offset:2048 sc1\n"
    "buffer_load_dwordx4 v[60:63], v2, s[4:7], s27 offen offset:1024 sc1\n"
    "buffer_load_dwordx4 v[64:67], v0, s[4:7], s26 offen offset:1024 sc1\n"
    "buffer_load_dwordx4 v[68:71], v3, s[4:7], s26 offen offset:0 sc1\n"
    "buffer_load_dwordx4 v[72:75], v4, s[4:7], s26 offen offset:3072 sc1\n"
    "buffer_load_dwordx4 v[76:79], v2, s[4:7], s27 offen offset:2048 sc1\n"
    "buffer_load_dwordx4 v[80:83], v0, s[4:7], s26 offen offset:2048 sc1\n"
    "buffer_load_dwordx4 v[84:87], v3, s[4:7], s26 offen offset:1024 sc1\n"
    "s_waitcnt vmcnt(9)\n"
    "v_mfma_f32_32x32x16_bf16 v[16:31], a[96:99], v[48:51], 0\n"
    "buffer_load_dwordx4 v[88:91], v0, s[4:7], s27 offen offset:0 sc1\n"
    "v_mfma_f32_32x32x16_bf16 v[32:47], a[100:103], v[48:51], 0\n"
    "s_waitcnt vmcnt(9)\n"
    "v_mfma_f32_32x32x16_bf16 v[16:31], a[152:155], v[52:55], v[16:31]\n"
    "buffer_load_dwordx4 v[92:95], v2, s[4:7], s27 offen offset:3072 sc1\n"
    "v_mfma_f32_32x32x16_bf16 v[32:47], a[156:159], v[52:55], v[32:47]\n"
    "s_waitcnt vmcnt(9)\n"
    "v_mfma_f32_32x32x16_bf16 v[16:31], a[208:211], v[56:59], v[16:31]\n"
    "buffer_load_dwordx4 a[0:3], v0, s[4:7], s26 offen offset:3072 sc1\n"
    "v_mfma_f32_32x32x16_bf16 v[32:47], a[212:215], v[56:59], v[32:47]\n"
    "s_waitcnt vmcnt(9)\n"
    "v_mfma_f32_32x32x16_bf16 v[16:31], v[216:219], v[60:63], v[16:31]\n"
    "buffer_load_dwordx4 a[4:7], v3, s[4:7], s26 offen offset:2048 sc1\n"
    "v_mfma_f32_32x32x16_bf16 v[32:47], v[220:223], v[60:63], v[32:47]\n"
    "s_waitcnt vmcnt(9)\n"
    "v_mfma_f32_32x32x16_bf16 v[16:31], a[104:107], v[64:67], v[16:31]\n"
    "buffer_load_dwordx4 a[8:11], v0, s[4:7], s27 offen offset:1024 sc1\n"
    "v_mfma_f32_32x32x16_bf16 v[32:47], a[108:111], v[64:67], v[32:47]\n"
    "s_waitcnt vmcnt(9)\n"
    "v_mfma_f32_32x32x16_bf16 v[16:31], a[160:163], v[68:71], v[16:31]\n"
    "buffer_load_dwordx4 a[12:15], v3, s[4:7], s27 offen offset:0 sc1\n"
    "v_mfma_f32_32x32x16_bf16 v[32:47], a[164:167], v[68:71], v[32:47]\n"
    "s_waitcnt vmcnt(9)\n"
    "v_mfma_f32_32x32x16_bf16 v[16:31], a[216:219], v[72:75], v[16:31]\n"
    "buffer_load_dwordx4 a[16:19], v2, s[4:7], s26 offen offset:0 sc1\n"
    "v_mfma_f32_32x32x16_bf16 v[32:47], a[220:223], v[72:75], v[32:47]\n"
    "s_waitcnt vmcnt(9)\n"
    "v_mfma_f32_32x32x16_bf16 v[16:31], v[224:227], v[76:79], v[16:31]\n"
    "buffer_load_dwordx4 a[20:23], v3, s[4:7], s26 offen offset:3072 sc1\n"
    "v_mfma_f32_32x32x16_bf16 v[32:47], v[228:231], v[76:79], v[32:47]\n"
    "s_waitcnt vmcnt(9)\n"
    "v_mfma_f32_32x32x16_bf16 v[16:31], a[112:115], v[80:83], v[16:31]\n"
    "buffer_load_dwordx4 a[24:27], v0, s[4:7], s27 offen offset:2048 sc1\n"
    "v_mfma_f32_32x32x16_bf16 v[32:47], a[116:119], v[80:83], v[32:47]\n"
    "s_waitcnt vmcnt(9)\n"
    "v_mfma_f32_32x32x16_bf16 v[16:31], a[168:171], v[84:87], v[16:31]\n"
    "buffer_load_dwordx4 a[28:31], v3, s[4:7], s27 offen offset:1024 sc1\n"
    "v_mfma_f32_32x32x16_bf16 v[32:47], a[172:175], v[84:87], v[32:47]\n"
    "s_waitcnt vmcnt(9)\n"
    "v_mfma_f32_32x32x16_bf16 v[16:31], a[224:227], v[88:91], v[16:31]\n"
    "buffer_load_dwordx4 a[32:35], v2, s[4:7], s26 offen offset:1024 sc1\n"
    "v_mfma_f32_32x32x16_bf16 v[32:47], a[228:231], v[88:91], v[32:47]\n"
    "s_waitcnt vmcnt(9)\n"
    "v_mfma_f32_32x32x16_bf16 v[16:31], v[232:235], v[92:95], v[16:31]\n"
    "buffer_load_dwordx4 a[36:39], v4, s[4:7], s26 offen offset:0 sc1\n"
    "v_mfma_f32_32x32x16_bf16 v[32:47], v[236:239], v[92:95], v[32:47]\n"
    "s_waitcnt vmcnt(9)\n"
    "v_mfma_f32_32x32x16_bf16 v[16:31], a[120:123], a[0:3], v[16:31]\n"
    "buffer_load_dwordx4 a[40:43], v0, s[4:7], s27 offen offset:3072 sc1\n"
    "v_mfma_f32_32x32x16_bf16 v[32:47], a[124:127], a[0:3], v[32:47]\n"
    "s_waitcnt vmcnt(9)\n"
    "v_mfma_f32_32x32x16_bf16 v[16:31], a[176:179], a[4:7], v[16:31]\n"
    "buffer_load_dwordx4 a[44:47], v3, s[4:7], s27 offen offset:2048 sc1\n"
    "v_mfma_f32_32x32x16_bf16 v[32:47], a[180:183], a[4:7], v[32:47]\n"
    "s_waitcnt vmcnt(9)\n"
    "v_mfma_f32_32x32x16_bf16 v[16:31], a[232:235], a[8:11], v[16:31]\n"
    "buffer_load_dwordx4 a[48:51], v2, s[4:7], s26 offen offset:2048 sc1\n"
    "v_mfma_f32_32x32x16_bf16 v[32:47], a[236:239], a[8:11], v[32:47]\n"
    "s_waitcnt vmcnt(9)\n"
    "v_mfma_f32_32x32x16_bf16 v[16:31], v[168:171], a[12:15], v[16:31]\n"
    "buffer_load_dwordx4 a[52:55], v4, s[4:7], s26 offen offset:1024 sc1\n"
    "v_mfma_f32_32x32x16_bf16 v[32:47], v[172:175], a[12:15], v[32:47]\n"
    "s_waitcnt vmcnt(9)\n"
    "v_mfma_f32_32x32x16_bf16 v[16:31], a[128:131], a[16:19], v[16:31]\n"
    "buffer_load_dwordx4 a[56:59], v2, s[4:7], s27 offen offset:0 sc1\n"
    "v_mfma_f32_32x32x16_bf16 v[32:47], a[132:135], a[16:19], v[32:47]\n"
    "s_waitcnt vmcnt(9)\n"
    "v_mfma_f32_32x32x16_bf16 v[16:31], a[184:187], a[20:23], v[16:31]\n"
    "buffer_load_dwordx4 a[60:63], v3, s[4:7], s27 offen offset:3072 sc1\n"
    "v_mfma_f32_32x32x16_bf16 v[32:47], a[188:191], a[20:23], v[32:47]\n"
    "s_waitcnt vmcnt(9)\n"
    "v_mfma_f32_32x32x16_bf16 v[16:31], v[192:195], a[24:27], v[16:31]\n"
    "buffer_load_dwordx4 a[64:67], v0, s[4:7], s24 offen offset:0 sc1\n"
    "v_mfma_f32_32x32x16_bf16 v[32:47], v[196:199], a[24:27], v[32:47]\n"
    "s_waitcnt vmcnt(9)\n"
    "v_mfma_f32_32x32x16_bf16 v[16:31], v[176:179], a[28:31], v[16:31]\n"
    "buffer_load_dwordx4 a[68:71], v2, s[4:7], s24 offen offset:3072 sc1\n"
    "v_mfma_f32_32x32x16_bf16 v[32:47], v[180:183], a[28:31], v[32:47]\n"
    "s_waitcnt vmcnt(9)\n"
    "v_mfma_f32_32x32x16_bf16 v[16:31], a[136:139], a[32:35], v[16:31]\n"
    "buffer_load_dwordx4 a[72:75], v4, s[4:7], s24 offen offset:2048 sc1\n"
    "v_mfma_f32_32x32x16_bf16 v[32:47], a[140:143], a[32:35], v[32:47]\n"
    "s_waitcnt vmcnt(9)\n"
    "v_mfma_f32_32x32x16_bf16 v[16:31], a[192:195], a[36:39], v[16:31]\n"
    "buffer_load_dwordx4 a[76:79], v2, s[4:7], s25 offen offset:1024 sc1\n"
    "v_mfma_f32_32x32x16_bf16 v[32:47], a[196:199], a[36:39], v[32:47]\n"
    "s_waitcnt vmcnt(9)\n"
    "v_mfma_f32_32x32x16_bf16 v[16:31], v[200:203], a[40:43], v[16:31]\n"
    "buffer_load_dwordx4 a[80:83], v0, s[4:7], s24 offen offset:1024 sc1\n"
    "v_mfma_f32_32x32x16_bf16 v[32:47], v[204:207], a[40:43], v[32:47]\n"
    "s_waitcnt vmcnt(9)\n"
    "v_mfma_f32_32x32x16_bf16 v[16:31], v[140:143], a[44:47], v[16:31]\n"
    "buffer_load_dwordx4 a[84:87], v3, s[4:7], s24 offen offset:0 sc1\n"
    "v_mfma_f32_32x32x16_bf16 v[32:47], v[144:147], a[44:47], v[32:47]\n"
    "s_waitcnt vmcnt(9)\n"
    "v_mfma_f32_32x32x16_bf16 v[16:31], a[144:147], a[48:51], v[16:31]\n"
    "buffer_load_dwordx4 a[88:91], v4, s[4:7], s24 offen offset:3072 sc1\n"
    "v_mfma_f32_32x32x16_bf16 v[32:47], a[148:151], a[48:51], v[32:47]\n"
    "s_waitcnt vmcnt(9)\n"
    "v_mfma_f32_32x32x16_bf16 v[16:31], a[200:203], a[52:55], v[16:31]\n"
    "buffer_load_dwordx4 a[92:95], v2, s[4:7], s25 offen offset:2048 sc1\n"
    "v_mfma_f32_32x32x16_bf16 v[32:47], a[204:207], a[52:55], v[32:47]\n"
    "s_waitcnt vmcnt(9)\n"
    "v_mfma_f32_32x32x16_bf16 v[16:31], v[208:211], a[56:59], v[16:31]\n"
    "buffer_load_dwordx4 a[240:243], v0, s[4:7], s24 offen offset:2048 sc1\n"
    "v_mfma_f32_32x32x16_bf16 v[32:47], v[212:215], a[56:59], v[32:47]\n"
    "s_waitcnt vmcnt(9)\n"
    "v_mfma_f32_32x32x16_bf16 v[16:31], v[148:151], a[60:63], v[16:31]\n"
    "buffer_load_dwordx4 a[244:247], v3, s[4:7], s24 offen offset:1024 sc1\n"
    "v_mfma_f32_32x32x16_bf16 v[32:47], v[8:11], a[60:63], v[32:47]\n"
    "s_waitcnt vmcnt(9)\n"
    "v_mfma_f32_32x32x16_bf16 v[64:79], a[100:103], a[64:67], 0\n"
    "buffer_load_dwordx4 a[248:251], v0, s[4:7], s25 offen offset:0 sc1\n"
    "v_mfma_f32_32x32x16_bf16 v[48:63], a[96:99], a[64:67], 0\n"
    "s_waitcnt vmcnt(9)\n"
    "v_mfma_f32_32x32x16_bf16 v[64:79], a[156:159], a[68:71], v[64:79]\n"
    "buffer_load_dwordx4 a[252:255], v2, s[4:7], s25 offen offset:3072 sc1\n"
    "v_mfma_f32_32x32x16_bf16 v[48:63], a[152:155], a[68:71], v[48:63]\n"
    "s_waitcnt vmcnt(9)\n"
    "v_mfma_f32_32x32x16_bf16 v[64:79], a[212:215], a[72:75], v[64:79]\n"
    "buffer_load_dwordx4 v[96:99], v0, s[4:7], s24 offen offset:3072 sc1\n"
    "v_mfma_f32_32x32x16_bf16 v[48:63], a[208:211], a[72:75], v[48:63]\n"
    "ds_write_b128 v252, v[16:19] offset:0\n"
    "s_or_b32 s52, s2, s18\n"
    "s_cmp_eq_u32 s52, 0\n"
    "s_cselect_b32 s52, 0, 24723456\n"
    "s_mov_b64 exec, 1\n"
    "buffer_atomic_swap_x2 v[254:255], off, s[4:7], s52 sc1\n"
    "s_mov_b64 exec, -1\n"
    "s_waitcnt vmcnt(10)\n"
    "v_mfma_f32_32x32x16_bf16 v[64:79], v[220:223], a[76:79], v[64:79]\n"
    "buffer_load_dwordx4 v[100:103], v3, s[4:7], s24 offen offset:2048 sc1\n"
    "v_mfma_f32_32x32x16_bf16 v[48:63], v[216:219], a[76:79], v[48:63]\n"
    "ds_write_b128 v252, v[20:23] offset:1024\n"
    "s_waitcnt vmcnt(10)\n"
    "v_mfma_f32_32x32x16_bf16 v[64:79], a[108:111], a[80:83], v[64:79]\n"
    "buffer_load_dwordx4 v[104:107], v0, s[4:7], s25 offen offset:1024 sc1\n"
    "v_mfma_f32_32x32x16_bf16 v[48:63], a[104:107], a[80:83], v[48:63]\n"
    "ds_write_b128 v252, v[24:27] offset:2048\n"
    "s_waitcnt vmcnt(10)\n"
    "v_mfma_f32_32x32x16_bf16 v[64:79], a[164:167], a[84:87], v[64:79]\n"
    "buffer_load_dwordx4 v[108:111], v3, s[4:7], s25 offen offset:0 sc1\n"
    "v_mfma_f32_32x32x16_bf16 v[48:63], a[160:163], a[84:87], v[48:63]\n"
    "ds_write_b128 v252, v[28:31] offset:3072\n"
    "s_waitcnt vmcnt(10)\n"
    "v_mfma_f32_32x32x16_bf16 v[64:79], a[220:223], a[88:91], v[64:79]\n"
    "buffer_load_dwordx4 v[112:115], v2, s[4:7], s24 offen offset:0 sc1\n"
    "v_mfma_f32_32x32x16_bf16 v[48:63], a[216:219], a[88:91], v[48:63]\n"
    "ds_write_b128 v253, v[32:35] offset:0\n"
    "s_waitcnt vmcnt(10)\n"
    "v_mfma_f32_32x32x16_bf16 v[64:79], v[228:231], a[92:95], v[64:79]\n"
    "buffer_load_dwordx4 v[116:119], v3, s[4:7], s24 offen offset:3072 sc1\n"
    "v_mfma_f32_32x32x16_bf16 v[48:63], v[224:227], a[92:95], v[48:63]\n"
    "ds_write_b128 v253, v[36:39] offset:1024\n"
    "s_waitcnt vmcnt(10)\n"
    "v_mfma_f32_32x32x16_bf16 v[64:79], a[116:119], a[240:243], v[64:79]\n"
    "buffer_load_dwordx4 v[120:123], v0, s[4:7], s25 offen offset:2048 sc1\n"
    "v_mfma_f32_32x32x16_bf16 v[48:63], a[112:115], a[240:243], v[48:63]\n"
    "ds_write_b128 v253, v[40:43] offset:2048\n"
    "s_waitcnt vmcnt(10)\n"
    "v_mfma_f32_32x32x16_bf16 v[64:79], a[172:175], a[244:247], v[64:79]\n"
    "buffer_load_dwordx4 v[124:127], v3, s[4:7], s25 offen offset:1024 sc1\n"
    "v_mfma_f32_32x32x16_bf16 v[48:63], a[168:171], a[244:247], v[48:63]\n"
    "ds_write_b128 v253, v[44:47] offset:3072\n"
    "s_waitcnt vmcnt(10)\n"
    "v_mfma_f32_32x32x16_bf16 v[64:79], a[228:231], a[248:251], v[64:79]\n"
    "buffer_load_dwordx4 v[128:131], v2, s[4:7], s24 offen offset:1024 sc1\n"
    "v_mfma_f32_32x32x16_bf16 v[48:63], a[224:227], a[248:251], v[48:63]\n"
    "s_waitcnt vmcnt(10)\n"
    "v_mfma_f32_32x32x16_bf16 v[64:79], v[236:239], a[252:255], v[64:79]\n"
    "buffer_load_dwordx4 v[132:135], v4, s[4:7], s24 offen offset:0 sc1\n"
    "v_mfma_f32_32x32x16_bf16 v[48:63], v[232:235], a[252:255], v[48:63]\n"
    "s_waitcnt vmcnt(10)\n"
    "v_mfma_f32_32x32x16_bf16 v[64:79], a[124:127], v[96:99], v[64:79]\n"
    "buffer_load_dwordx4 v[136:139], v0, s[4:7], s25 offen offset:3072 sc1\n"
    "v_mfma_f32_32x32x16_bf16 v[48:63], a[120:123], v[96:99], v[48:63]\n"
    "s_waitcnt vmcnt(9)\n"
    "v_mfma_f32_32x32x16_bf16 v[64:79], a[180:183], v[100:103], v[64:79]\n"
    "buffer_load_dwordx4 v[152:155], v3, s[4:7], s25 offen offset:2048 sc1\n"
    "v_mfma_f32_32x32x16_bf16 v[48:63], a[176:179], v[100:103], v[48:63]\n"
    "s_waitcnt vmcnt(9)\n"
    "v_mfma_f32_32x32x16_bf16 v[64:79], a[236:239], v[104:107], v[64:79]\n"
    "buffer_load_dwordx4 v[156:159], v2, s[4:7], s24 offen offset:2048 sc1\n"
    "v_mfma_f32_32x32x16_bf16 v[48:63], a[232:235], v[104:107], v[48:63]\n"
    "s_waitcnt vmcnt(9)\n"
    "v_mfma_f32_32x32x16_bf16 v[64:79], v[172:175], v[108:111], v[64:79]\n"
    "buffer_load_dwordx4 v[160:163], v4, s[4:7], s24 offen offset:1024 sc1\n"
    "v_mfma_f32_32x32x16_bf16 v[48:63], v[168:171], v[108:111], v[48:63]\n"
    "s_waitcnt vmcnt(9)\n"
    "v_mfma_f32_32x32x16_bf16 v[64:79], a[132:135], v[112:115], v[64:79]\n"
    "buffer_load_dwordx4 v[164:167], v2, s[4:7], s25 offen offset:0 sc1\n"
    "v_mfma_f32_32x32x16_bf16 v[48:63], a[128:131], v[112:115], v[48:63]\n"
    "s_waitcnt vmcnt(9)\n"
    "v_mfma_f32_32x32x16_bf16 v[64:79], a[188:191], v[116:119], v[64:79]\n"
    "buffer_load_dwordx4 v[12:15], v3, s[4:7], s25 offen offset:3072 sc1\n"
    "v_mfma_f32_32x32x16_bf16 v[48:63], a[184:187], v[116:119], v[48:63]\n"
    "s_waitcnt vmcnt(9)\n"
    "v_mfma_f32_32x32x16_bf16 v[64:79], v[196:199], v[120:123], v[64:79]\n"
    "v_mfma_f32_32x32x16_bf16 v[48:63], v[192:195], v[120:123], v[48:63]\n"
    "s_waitcnt vmcnt(8)\n"
    "v_mfma_f32_32x32x16_bf16 v[64:79], v[180:183], v[124:127], v[64:79]\n"
    "v_mfma_f32_32x32x16_bf16 v[48:63], v[176:179], v[124:127], v[48:63]\n"
    "s_waitcnt vmcnt(7)\n"
    "v_mfma_f32_32x32x16_bf16 v[64:79], a[140:143], v[128:131], v[64:79]\n"
    "v_mfma_f32_32x32x16_bf16 v[48:63], a[136:139], v[128:131], v[48:63]\n"
    "s_waitcnt vmcnt(6)\n"
    "v_mfma_f32_32x32x16_bf16 v[64:79], a[196:199], v[132:135], v[64:79]\n"
    "v_mfma_f32_32x32x16_bf16 v[48:63], a[192:195], v[132:135], v[48:63]\n"
    "s_waitcnt vmcnt(5)\n"
    "v_mfma_f32_32x32x16_bf16 v[64:79], v[204:207], v[136:139], v[64:79]\n"
    "v_mfma_f32_32x32x16_bf16 v[48:63], v[200:203], v[136:139], v[48:63]\n"
    "s_waitcnt vmcnt(4)\n"
    "v_mfma_f32_32x32x16_bf16 v[64:79], v[144:147], v[152:155], v[64:79]\n"
    "v_mfma_f32_32x32x16_bf16 v[48:63], v[140:143], v[152:155], v[48:63]\n"
    "s_waitcnt vmcnt(3)\n"
    "v_mfma_f32_32x32x16_bf16 v[64:79], a[148:151], v[156:159], v[64:79]\n"
    "v_mfma_f32_32x32x16_bf16 v[48:63], a[144:147], v[156:159], v[48:63]\n"
    "s_waitcnt vmcnt(2)\n"
    "v_mfma_f32_32x32x16_bf16 v[64:79], a[204:207], v[160:163], v[64:79]\n"
    "v_mfma_f32_32x32x16_bf16 v[48:63], a[200:203], v[160:163], v[48:63]\n"
    "s_waitcnt vmcnt(1)\n"
    "v_mfma_f32_32x32x16_bf16 v[64:79], v[212:215], v[164:167], v[64:79]\n"
    "v_mfma_f32_32x32x16_bf16 v[48:63], v[208:211], v[164:167], v[48:63]\n"
    "s_waitcnt vmcnt(0)\n"
    "v_mfma_f32_32x32x16_bf16 v[64:79], v[8:11], v[12:15], v[64:79]\n"
    "v_mfma_f32_32x32x16_bf16 v[48:63], v[148:151], v[12:15], v[48:63]\n"
    "s_nop 10\n"
    "ds_write_b128 v251, v[64:67] offset:0\n"
    "ds_write_b128 v251, v[68:71] offset:1024\n"
    "ds_write_b128 v251, v[72:75] offset:2048\n"
    "ds_write_b128 v251, v[76:79] offset:3072\n"
    "ds_read_b128 v[128:131], v248 offset:512\n"
    "ds_read_b128 v[132:135], v248 offset:544\n"
    "ds_read_b128 v[136:139], v248 offset:576\n"
    "ds_read_b128 v[140:143], v248 offset:608\n"
    "v_lshlrev_b32 v144, 16, v240\n"
    "v_and_b32 v145, 0xffff0000, v240\n"
    "v_lshlrev_b32 v146, 16, v241\n"
    "v_and_b32 v147, 0xffff0000, v241\n"
    "v_lshlrev_b32 v148, 16, v242\n"
    "v_and_b32 v149, 0xffff0000, v242\n"
    "v_lshlrev_b32 v150, 16, v243\n"
    "v_and_b32 v151, 0xffff0000, v243\n"
    "v_lshlrev_b32 v152, 16, v244\n"
    "v_and_b32 v153, 0xffff0000, v244\n"
    "v_lshlrev_b32 v154, 16, v245\n"
    "v_and_b32 v155, 0xffff0000, v245\n"
    "v_lshlrev_b32 v156, 16, v246\n"
    "v_and_b32 v157, 0xffff0000, v246\n"
    "v_lshlrev_b32 v158, 16, v247\n"
    "v_and_b32 v159, 0xffff0000, v247\n"
    "v_lshrrev_b32 v8, 5, v1\n"
    "v_lshl_add_u32 v249, v8, 3, v249\n"
    "s_waitcnt lgkmcnt(0)\n"
    "s_barrier\n"
    "ds_read_b128 v[80:83], v250 offset:0\n"
    "ds_read_b128 v[84:87], v250 offset:1024\n"
    "ds_read_b128 v[88:91], v250 offset:2048\n"
    "ds_read_b128 v[92:95], v250 offset:3072\n"
    "ds_read_b128 v[96:99], v250 offset:4096\n"
    "ds_read_b128 v[100:103], v250 offset:5120\n"
    "ds_read_b128 v[104:107], v250 offset:6144\n"
    "ds_read_b128 v[108:111], v250 offset:7168\n"
    "ds_read_b128 v[112:115], v250 offset:8192\n"
    "ds_read_b128 v[116:119], v250 offset:9216\n"
    "ds_read_b128 v[120:123], v250 offset:10240\n"
    "ds_read_b128 v[124:127], v250 offset:11264\n"
    "s_waitcnt lgkmcnt(11)\n"
    "v_pk_add_f32 v[48:49], v[48:49], v[80:81]\n"
    "v_pk_add_f32 v[50:51], v[50:51], v[82:83]\n"
    "s_waitcnt lgkmcnt(10)\n"
    "v_pk_add_f32 v[52:53], v[52:53], v[84:85]\n"
    "v_pk_add_f32 v[54:55], v[54:55], v[86:87]\n"
    "s_waitcnt lgkmcnt(9)\n"
    "v_pk_add_f32 v[56:57], v[56:57], v[88:89]\n"
    "v_pk_add_f32 v[58:59], v[58:59], v[90:91]\n"
    "s_waitcnt lgkmcnt(8)\n"
    "v_pk_add_f32 v[60:61], v[60:61], v[92:93]\n"
    "v_pk_add_f32 v[62:63], v[62:63], v[94:95]\n"
    "s_waitcnt lgkmcnt(3)\n"
    "v_pk_add_f32 v[96:97], v[96:97], v[112:113]\n"
    "v_pk_add_f32 v[98:99], v[98:99], v[114:115]\n"
    "s_waitcnt lgkmcnt(2)\n"
    "v_pk_add_f32 v[100:101], v[100:101], v[116:117]\n"
    "v_pk_add_f32 v[102:103], v[102:103], v[118:119]\n"
    "s_waitcnt lgkmcnt(1)\n"
    "v_pk_add_f32 v[104:105], v[104:105], v[120:121]\n"
    "v_pk_add_f32 v[106:107], v[106:107], v[122:123]\n"
    "s_waitcnt lgkmcnt(0)\n"
    "v_pk_add_f32 v[108:109], v[108:109], v[124:125]\n"
    "v_pk_add_f32 v[110:111], v[110:111], v[126:127]\n"
    "v_pk_add_f32 v[48:49], v[48:49], v[96:97]\n"
    "v_pk_add_f32 v[50:51], v[50:51], v[98:99]\n"
    "v_pk_add_f32 v[52:53], v[52:53], v[100:101]\n"
    "v_pk_add_f32 v[54:55], v[54:55], v[102:103]\n"
    "v_pk_add_f32 v[56:57], v[56:57], v[104:105]\n"
    "v_pk_add_f32 v[58:59], v[58:59], v[106:107]\n"
    "v_pk_add_f32 v[60:61], v[60:61], v[108:109]\n"
    "v_pk_add_f32 v[62:63], v[62:63], v[110:111]\n"
    "v_pk_fma_f32 v[144:145], v[48:49], v[128:129], v[144:145]\n"
    "v_pk_fma_f32 v[146:147], v[50:51], v[130:131], v[146:147]\n"
    "v_pk_fma_f32 v[148:149], v[52:53], v[132:133], v[148:149]\n"
    "v_pk_fma_f32 v[150:151], v[54:55], v[134:135], v[150:151]\n"
    "v_pk_fma_f32 v[152:153], v[56:57], v[136:137], v[152:153]\n"
    "v_pk_fma_f32 v[154:155], v[58:59], v[138:139], v[154:155]\n"
    "v_pk_fma_f32 v[156:157], v[60:61], v[140:141], v[156:157]\n"
    "v_pk_fma_f32 v[158:159], v[62:63], v[142:143], v[158:159]\n"
    "v_cvt_pk_bf16_f32 v160, v144, v145\n"
    "v_cvt_pk_bf16_f32 v161, v146, v147\n"
    "v_cvt_pk_bf16_f32 v162, v148, v149\n"
    "v_cvt_pk_bf16_f32 v163, v150, v151\n"
    "v_cvt_pk_bf16_f32 v164, v152, v153\n"
    "v_cvt_pk_bf16_f32 v165, v154, v155\n"
    "v_cvt_pk_bf16_f32 v166, v156, v157\n"
    "v_cvt_pk_bf16_f32 v167, v158, v159\n"
    "v_permlane32_swap_b32 v160, v162\n"
    "v_permlane32_swap_b32 v161, v163\n"
    "v_permlane32_swap_b32 v164, v166\n"
    "v_permlane32_swap_b32 v165, v167\n"
    "buffer_store_dwordx4 v[160:163], v249, s[32:35], s77 offen\n"
    "buffer_store_dwordx4 v[164:167], v249, s[32:35], s77 offen offset:32\n"
    "s_nop 0\n"
    "s_branch Lend_28%=\n"
    "Lchk_10%=:\n"
    "s_nop 11\n"
    "s_mov_b32 s70, 0\n"
    "s_mov_b64 exec, 65535\n"
    "Lpoll_32%=:\n"
    "buffer_load_dwordx2 v[242:243], v240, s[4:7], 0 offen sc1\n"
    "s_waitcnt vmcnt(0)\n"
    "v_cmp_ne_u64 vcc, s[16:17], v[242:243]\n"
    "s_nop 0\n"
    "s_and_b64 exec, exec, vcc\n"
    "s_cbranch_scc0 Lpollok_33%=\n"
    "s_sleep 12\n"
    "s_add_u32 s70, s70, 1\n"
    "s_cmp_lt_u32 s70, 0x40000\n"
    "s_cbranch_scc1 Lpoll_32%=\n"
    "Lpollok_33%=:\n"
    "s_mov_b64 exec, -1\n"
    "s_branch Lchkb_11%=\n"
    "Lchk_14%=:\n"
    "s_nop 11\n"
    "s_mov_b32 s70, 0\n"
    "s_mov_b32 exec_hi, 16777215\n"
    "Lpoll_34%=:\n"
    "buffer_load_dwordx2 v[8:9], v6, s[4:7], 0 offen sc1\n"
    "s_waitcnt vmcnt(0)\n"
    "v_cmp_ne_u64 vcc, s[16:17], v[8:9]\n"
    "s_nop 0\n"
    "s_and_b64 exec, exec, vcc\n"
    "s_cbranch_scc0 Lpollok_35%=\n"
    "s_sleep 12\n"
    "s_add_u32 s70, s70, 1\n"
    "s_cmp_lt_u32 s70, 0x40000\n"
    "s_cbranch_scc1 Lpoll_34%=\n"
    "Lpollok_35%=:\n"
    "s_mov_b64 exec, -1\n"
    "s_branch Lchkb_15%=\n"
    "Lchk_16%=:\n"
    "s_nop 11\n"
    "s_mov_b32 s70, 0\n"
    "s_mov_b64 exec, 127\n"
    "Lpoll_36%=:\n"
    "buffer_load_dwordx2 v[10:11], v7, s[4:7], 0 offen sc1\n"
    "s_waitcnt vmcnt(0)\n"
    "v_cmp_ne_u64 vcc, s[16:17], v[10:11]\n"
    "s_nop 0\n"
    "s_and_b64 exec, exec, vcc\n"
    "s_cbranch_scc0 Lpollok_37%=\n"
    "s_sleep 12\n"
    "s_add_u32 s70, s70, 1\n"
    "s_cmp_lt_u32 s70, 0x40000\n"
    "s_cbranch_scc1 Lpoll_36%=\n"
    "Lpollok_37%=:\n"
    "s_mov_b64 exec, -1\n"
    "s_branch Lchkb_17%=\n"
    "Lchk_26%=:\n"
    "s_nop 11\n"
    "s_mov_b32 s70, 0\n"
    "s_mov_b64 exec, 127\n"
    "Lpoll_38%=:\n"
    "buffer_load_dwordx2 v[188:189], v15, s[4:7], 0 offen sc1\n"
    "s_waitcnt vmcnt(0)\n"
    "v_cmp_ne_u64 vcc, s[16:17], v[188:189]\n"
    "s_nop 0\n"
    "s_and_b64 exec, exec, vcc\n"
    "s_cbranch_scc0 Lpollok_39%=\n"
    "s_sleep 12\n"
    "s_add_u32 s70, s70, 1\n"
    "s_cmp_lt_u32 s70, 0x40000\n"
    "s_cbranch_scc1 Lpoll_38%=\n"
    "Lpollok_39%=:\n"
    "s_mov_b64 exec, -1\n"
    "s_branch Lchkb_27%=\n"
    "Lfallback_18%=:\n"
    "s_waitcnt vmcnt(0) lgkmcnt(0)\n"
    "s_barrier\n"
    "v_accvgpr_read_b32 v8, a6\n"
    "v_cndmask_b32 v8, v8, 0, s[78:79]\n"
    "v_accvgpr_write_b32 a6, v8\n"
    "v_mov_b32 v12, 0xfeffffff\n"
    "v_mov_b32 v64, 0\n"
    "v_mov_b32 v65, 0\n"
    "v_mov_b32 v66, 0\n"
    "v_mov_b32 v67, 0\n"
    "v_mov_b32 v68, 0\n"
    "v_mov_b32 v69, 0\n"
    "v_mov_b32 v70, 0\n"
    "v_mov_b32 v71, 0\n"
    "v_mov_b32 v72, 0\n"
    "v_mov_b32 v73, 0\n"
    "v_mov_b32 v74, 0\n"
    "v_mov_b32 v75, 0\n"
    "v_mov_b32 v76, 0\n"
    "v_mov_b32 v77, 0\n"
    "v_mov_b32 v78, 0\n"
    "v_mov_b32 v79, 0\n"
    "v_accvgpr_read_b32 v8, a14\n"
    "v_cndmask_b32 v8, v8, 0, s[78:79]\n"
    "v_accvgpr_write_b32 a14, v8\n"
    "v_mov_b32 v13, 0xfeffffff\n"
    "v_mov_b32 v80, 0\n"
    "v_mov_b32 v81, 0\n"
    "v_mov_b32 v82, 0\n"
    "v_mov_b32 v83, 0\n"
    "v_mov_b32 v84, 0\n"
    "v_mov_b32 v85, 0\n"
    "v_mov_b32 v86, 0\n"
    "v_mov_b32 v87, 0\n"
    "v_mov_b32 v88, 0\n"
    "v_mov_b32 v89, 0\n"
    "v_mov_b32 v90, 0\n"
    "v_mov_b32 v91, 0\n"
    "v_mov_b32 v92, 0\n"
    "v_mov_b32 v93, 0\n"
    "v_mov_b32 v94, 0\n"
    "v_mov_b32 v95, 0\n"
    "v_accvgpr_read_b32 v8, a22\n"
    "v_cndmask_b32 v8, v8, 0, s[78:79]\n"
    "v_accvgpr_write_b32 a22, v8\n"
    "v_mov_b32 v14, 0xfeffffff\n"
    "v_mov_b32 v96, 0\n"
    "v_mov_b32 v97, 0\n"
    "v_mov_b32 v98, 0\n"
    "v_mov_b32 v99, 0\n"
    "v_mov_b32 v100, 0\n"
    "v_mov_b32 v101, 0\n"
    "v_mov_b32 v102, 0\n"
    "v_mov_b32 v103, 0\n"
    "v_mov_b32 v104, 0\n"
    "v_mov_b32 v105, 0\n"
    "v_mov_b32 v106, 0\n"
    "v_mov_b32 v107, 0\n"
    "v_mov_b32 v108, 0\n"
    "v_mov_b32 v109, 0\n"
    "v_mov_b32 v110, 0\n"
    "v_mov_b32 v111, 0\n"
    "v_accvgpr_read_b32 v8, a30\n"
    "v_cndmask_b32 v8, v8, 0, s[78:79]\n"
    "v_accvgpr_write_b32 a30, v8\n"
    "v_mov_b32 v15, 0xfeffffff\n"
    "v_mov_b32 v112, 0\n"
    "v_mov_b32 v113, 0\n"
    "v_mov_b32 v114, 0\n"
    "v_mov_b32 v115, 0\n"
    "v_mov_b32 v116, 0\n"
    "v_mov_b32 v117, 0\n"
    "v_mov_b32 v118, 0\n"
    "v_mov_b32 v119, 0\n"
    "v_mov_b32 v120, 0\n"
    "v_mov_b32 v121, 0\n"
    "v_mov_b32 v122, 0\n"
    "v_mov_b32 v123, 0\n"
    "v_mov_b32 v124, 0\n"
    "v_mov_b32 v125, 0\n"
    "v_mov_b32 v126, 0\n"
    "v_mov_b32 v127, 0\n"
    "s_mov_b32 s51, 0\n"
    "s_nop 1\n"
    "Lfbloop_40%=:\n"
    "buffer_load_dwordx4 v[128:131], v0, s[12:15], s51 offen offset:0 sc1\n"
    "buffer_load_dwordx4 v[132:135], v0, s[12:15], s51 offen offset:1024 sc1\n"
    "buffer_load_dwordx4 v[136:139], v0, s[12:15], s51 offen offset:2048 sc1\n"
    "buffer_load_dwordx4 v[140:143], v0, s[12:15], s51 offen offset:3072 sc1\n"
    "s_waitcnt vmcnt(3)\n"
    "v_mfma_f32_32x32x16_bf16 v[16:31], v[128:131], a[0:3], 0\n"
    "s_waitcnt vmcnt(2)\n"
    "v_mfma_f32_32x32x16_bf16 v[16:31], v[132:135], a[4:7], v[16:31]\n"
    "s_nop 11\n"
    "v_max3_f32 v8, v16, v17, v18\n"
    "v_max3_f32 v8, v8, v19, v20\n"
    "v_max3_f32 v8, v8, v21, v22\n"
    "v_max3_f32 v8, v8, v23, v24\n"
    "v_max3_f32 v8, v8, v25, v26\n"
    "v_max3_f32 v8, v8, v27, v28\n"
    "v_max3_f32 v8, v8, v29, v30\n"
    "v_max3_f32 v8, v8, v31, v12\n"
    "v_mov_b32 v9, v8\n"
    "s_nop 1\n"
    "v_permlane32_swap_b32 v8, v9\n"
    "v_max_f32 v8, v8, v9\n"
    "v_sub_f32 v9, v12, v8\n"
    "v_exp_f32 v9, v9\n"
    "v_mov_b32 v12, v8\n"
    "v_sub_f32 v16, v16, v8\n"
    "v_sub_f32 v17, v17, v8\n"
    "v_sub_f32 v18, v18, v8\n"
    "v_sub_f32 v19, v19, v8\n"
    "v_sub_f32 v20, v20, v8\n"
    "v_sub_f32 v21, v21, v8\n"
    "v_sub_f32 v22, v22, v8\n"
    "v_sub_f32 v23, v23, v8\n"
    "v_sub_f32 v24, v24, v8\n"
    "v_sub_f32 v25, v25, v8\n"
    "v_sub_f32 v26, v26, v8\n"
    "v_sub_f32 v27, v27, v8\n"
    "v_sub_f32 v28, v28, v8\n"
    "v_sub_f32 v29, v29, v8\n"
    "v_sub_f32 v30, v30, v8\n"
    "v_sub_f32 v31, v31, v8\n"
    "v_exp_f32 v16, v16\n"
    "v_exp_f32 v17, v17\n"
    "v_exp_f32 v18, v18\n"
    "v_exp_f32 v19, v19\n"
    "v_exp_f32 v20, v20\n"
    "v_exp_f32 v21, v21\n"
    "v_exp_f32 v22, v22\n"
    "v_exp_f32 v23, v23\n"
    "v_exp_f32 v24, v24\n"
    "v_exp_f32 v25, v25\n"
    "v_exp_f32 v26, v26\n"
    "v_exp_f32 v27, v27\n"
    "v_exp_f32 v28, v28\n"
    "v_exp_f32 v29, v29\n"
    "v_exp_f32 v30, v30\n"
    "v_exp_f32 v31, v31\n"
    "v_mul_f32 v64, v64, v9\n"
    "v_mul_f32 v65, v65, v9\n"
    "v_mul_f32 v66, v66, v9\n"
    "v_mul_f32 v67, v67, v9\n"
    "v_mul_f32 v68, v68, v9\n"
    "v_mul_f32 v69, v69, v9\n"
    "v_mul_f32 v70, v70, v9\n"
    "v_mul_f32 v71, v71, v9\n"
    "v_mul_f32 v72, v72, v9\n"
    "v_mul_f32 v73, v73, v9\n"
    "v_mul_f32 v74, v74, v9\n"
    "v_mul_f32 v75, v75, v9\n"
    "v_mul_f32 v76, v76, v9\n"
    "v_mul_f32 v77, v77, v9\n"
    "v_mul_f32 v78, v78, v9\n"
    "v_mul_f32 v79, v79, v9\n"
    "v_cvt_pk_bf16_f32 v48, v16, v17\n"
    "v_cvt_pk_bf16_f32 v49, v18, v19\n"
    "v_cvt_pk_bf16_f32 v50, v20, v21\n"
    "v_cvt_pk_bf16_f32 v51, v22, v23\n"
    "v_cvt_pk_bf16_f32 v52, v24, v25\n"
    "v_cvt_pk_bf16_f32 v53, v26, v27\n"
    "v_cvt_pk_bf16_f32 v54, v28, v29\n"
    "v_cvt_pk_bf16_f32 v55, v30, v31\n"
    "s_waitcnt vmcnt(1)\n"
    "v_mfma_f32_32x32x16_bf16 v[64:79], v[136:139], v[48:51], v[64:79]\n"
    "s_waitcnt vmcnt(0)\n"
    "v_mfma_f32_32x32x16_bf16 v[64:79], v[140:143], v[52:55], v[64:79]\n"
    "v_mfma_f32_32x32x16_bf16 v[16:31], v[128:131], a[8:11], 0\n"
    "v_mfma_f32_32x32x16_bf16 v[16:31], v[132:135], a[12:15], v[16:31]\n"
    "s_nop 11\n"
    "v_max3_f32 v8, v16, v17, v18\n"
    "v_max3_f32 v8, v8, v19, v20\n"
    "v_max3_f32 v8, v8, v21, v22\n"
    "v_max3_f32 v8, v8, v23, v24\n"
    "v_max3_f32 v8, v8, v25, v26\n"
    "v_max3_f32 v8, v8, v27, v28\n"
    "v_max3_f32 v8, v8, v29, v30\n"
    "v_max3_f32 v8, v8, v31, v13\n"
    "v_mov_b32 v9, v8\n"
    "s_nop 1\n"
    "v_permlane32_swap_b32 v8, v9\n"
    "v_max_f32 v8, v8, v9\n"
    "v_sub_f32 v9, v13, v8\n"
    "v_exp_f32 v9, v9\n"
    "v_mov_b32 v13, v8\n"
    "v_sub_f32 v16, v16, v8\n"
    "v_sub_f32 v17, v17, v8\n"
    "v_sub_f32 v18, v18, v8\n"
    "v_sub_f32 v19, v19, v8\n"
    "v_sub_f32 v20, v20, v8\n"
    "v_sub_f32 v21, v21, v8\n"
    "v_sub_f32 v22, v22, v8\n"
    "v_sub_f32 v23, v23, v8\n"
    "v_sub_f32 v24, v24, v8\n"
    "v_sub_f32 v25, v25, v8\n"
    "v_sub_f32 v26, v26, v8\n"
    "v_sub_f32 v27, v27, v8\n"
    "v_sub_f32 v28, v28, v8\n"
    "v_sub_f32 v29, v29, v8\n"
    "v_sub_f32 v30, v30, v8\n"
    "v_sub_f32 v31, v31, v8\n"
    "v_exp_f32 v16, v16\n"
    "v_exp_f32 v17, v17\n"
    "v_exp_f32 v18, v18\n"
    "v_exp_f32 v19, v19\n"
    "v_exp_f32 v20, v20\n"
    "v_exp_f32 v21, v21\n"
    "v_exp_f32 v22, v22\n"
    "v_exp_f32 v23, v23\n"
    "v_exp_f32 v24, v24\n"
    "v_exp_f32 v25, v25\n"
    "v_exp_f32 v26, v26\n"
    "v_exp_f32 v27, v27\n"
    "v_exp_f32 v28, v28\n"
    "v_exp_f32 v29, v29\n"
    "v_exp_f32 v30, v30\n"
    "v_exp_f32 v31, v31\n"
    "v_mul_f32 v80, v80, v9\n"
    "v_mul_f32 v81, v81, v9\n"
    "v_mul_f32 v82, v82, v9\n"
    "v_mul_f32 v83, v83, v9\n"
    "v_mul_f32 v84, v84, v9\n"
    "v_mul_f32 v85, v85, v9\n"
    "v_mul_f32 v86, v86, v9\n"
    "v_mul_f32 v87, v87, v9\n"
    "v_mul_f32 v88, v88, v9\n"
    "v_mul_f32 v89, v89, v9\n"
    "v_mul_f32 v90, v90, v9\n"
    "v_mul_f32 v91, v91, v9\n"
    "v_mul_f32 v92, v92, v9\n"
    "v_mul_f32 v93, v93, v9\n"
    "v_mul_f32 v94, v94, v9\n"
    "v_mul_f32 v95, v95, v9\n"
    "v_cvt_pk_bf16_f32 v48, v16, v17\n"
    "v_cvt_pk_bf16_f32 v49, v18, v19\n"
    "v_cvt_pk_bf16_f32 v50, v20, v21\n"
    "v_cvt_pk_bf16_f32 v51, v22, v23\n"
    "v_cvt_pk_bf16_f32 v52, v24, v25\n"
    "v_cvt_pk_bf16_f32 v53, v26, v27\n"
    "v_cvt_pk_bf16_f32 v54, v28, v29\n"
    "v_cvt_pk_bf16_f32 v55, v30, v31\n"
    "v_mfma_f32_32x32x16_bf16 v[80:95], v[136:139], v[48:51], v[80:95]\n"
    "s_nop 0\n"
    "v_mfma_f32_32x32x16_bf16 v[80:95], v[140:143], v[52:55], v[80:95]\n"
    "v_mfma_f32_32x32x16_bf16 v[16:31], v[128:131], a[16:19], 0\n"
    "v_mfma_f32_32x32x16_bf16 v[16:31], v[132:135], a[20:23], v[16:31]\n"
    "s_nop 11\n"
    "v_max3_f32 v8, v16, v17, v18\n"
    "v_max3_f32 v8, v8, v19, v20\n"
    "v_max3_f32 v8, v8, v21, v22\n"
    "v_max3_f32 v8, v8, v23, v24\n"
    "v_max3_f32 v8, v8, v25, v26\n"
    "v_max3_f32 v8, v8, v27, v28\n"
    "v_max3_f32 v8, v8, v29, v30\n"
    "v_max3_f32 v8, v8, v31, v14\n"
    "v_mov_b32 v9, v8\n"
    "s_nop 1\n"
    "v_permlane32_swap_b32 v8, v9\n"
    "v_max_f32 v8, v8, v9\n"
    "v_sub_f32 v9, v14, v8\n"
    "v_exp_f32 v9, v9\n"
    "v_mov_b32 v14, v8\n"
    "v_sub_f32 v16, v16, v8\n"
    "v_sub_f32 v17, v17, v8\n"
    "v_sub_f32 v18, v18, v8\n"
    "v_sub_f32 v19, v19, v8\n"
    "v_sub_f32 v20, v20, v8\n"
    "v_sub_f32 v21, v21, v8\n"
    "v_sub_f32 v22, v22, v8\n"
    "v_sub_f32 v23, v23, v8\n"
    "v_sub_f32 v24, v24, v8\n"
    "v_sub_f32 v25, v25, v8\n"
    "v_sub_f32 v26, v26, v8\n"
    "v_sub_f32 v27, v27, v8\n"
    "v_sub_f32 v28, v28, v8\n"
    "v_sub_f32 v29, v29, v8\n"
    "v_sub_f32 v30, v30, v8\n"
    "v_sub_f32 v31, v31, v8\n"
    "v_exp_f32 v16, v16\n"
    "v_exp_f32 v17, v17\n"
    "v_exp_f32 v18, v18\n"
    "v_exp_f32 v19, v19\n"
    "v_exp_f32 v20, v20\n"
    "v_exp_f32 v21, v21\n"
    "v_exp_f32 v22, v22\n"
    "v_exp_f32 v23, v23\n"
    "v_exp_f32 v24, v24\n"
    "v_exp_f32 v25, v25\n"
    "v_exp_f32 v26, v26\n"
    "v_exp_f32 v27, v27\n"
    "v_exp_f32 v28, v28\n"
    "v_exp_f32 v29, v29\n"
    "v_exp_f32 v30, v30\n"
    "v_exp_f32 v31, v31\n"
    "v_mul_f32 v96, v96, v9\n"
    "v_mul_f32 v97, v97, v9\n"
    "v_mul_f32 v98, v98, v9\n"
    "v_mul_f32 v99, v99, v9\n"
    "v_mul_f32 v100, v100, v9\n"
    "v_mul_f32 v101, v101, v9\n"
    "v_mul_f32 v102, v102, v9\n"
    "v_mul_f32 v103, v103, v9\n"
    "v_mul_f32 v104, v104, v9\n"
    "v_mul_f32 v105, v105, v9\n"
    "v_mul_f32 v106, v106, v9\n"
    "v_mul_f32 v107, v107, v9\n"
    "v_mul_f32 v108, v108, v9\n"
    "v_mul_f32 v109, v109, v9\n"
    "v_mul_f32 v110, v110, v9\n"
    "v_mul_f32 v111, v111, v9\n"
    "v_cvt_pk_bf16_f32 v48, v16, v17\n"
    "v_cvt_pk_bf16_f32 v49, v18, v19\n"
    "v_cvt_pk_bf16_f32 v50, v20, v21\n"
    "v_cvt_pk_bf16_f32 v51, v22, v23\n"
    "v_cvt_pk_bf16_f32 v52, v24, v25\n"
    "v_cvt_pk_bf16_f32 v53, v26, v27\n"
    "v_cvt_pk_bf16_f32 v54, v28, v29\n"
    "v_cvt_pk_bf16_f32 v55, v30, v31\n"
    "v_mfma_f32_32x32x16_bf16 v[96:111], v[136:139], v[48:51], v[96:111]\n"
    "s_nop 0\n"
    "v_mfma_f32_32x32x16_bf16 v[96:111], v[140:143], v[52:55], v[96:111]\n"
    "v_mfma_f32_32x32x16_bf16 v[16:31], v[128:131], a[24:27], 0\n"
    "v_mfma_f32_32x32x16_bf16 v[16:31], v[132:135], a[28:31], v[16:31]\n"
    "s_nop 11\n"
    "v_max3_f32 v8, v16, v17, v18\n"
    "v_max3_f32 v8, v8, v19, v20\n"
    "v_max3_f32 v8, v8, v21, v22\n"
    "v_max3_f32 v8, v8, v23, v24\n"
    "v_max3_f32 v8, v8, v25, v26\n"
    "v_max3_f32 v8, v8, v27, v28\n"
    "v_max3_f32 v8, v8, v29, v30\n"
    "v_max3_f32 v8, v8, v31, v15\n"
    "v_mov_b32 v9, v8\n"
    "s_nop 1\n"
    "v_permlane32_swap_b32 v8, v9\n"
    "v_max_f32 v8, v8, v9\n"
    "v_sub_f32 v9, v15, v8\n"
    "v_exp_f32 v9, v9\n"
    "v_mov_b32 v15, v8\n"
    "v_sub_f32 v16, v16, v8\n"
    "v_sub_f32 v17, v17, v8\n"
    "v_sub_f32 v18, v18, v8\n"
    "v_sub_f32 v19, v19, v8\n"
    "v_sub_f32 v20, v20, v8\n"
    "v_sub_f32 v21, v21, v8\n"
    "v_sub_f32 v22, v22, v8\n"
    "v_sub_f32 v23, v23, v8\n"
    "v_sub_f32 v24, v24, v8\n"
    "v_sub_f32 v25, v25, v8\n"
    "v_sub_f32 v26, v26, v8\n"
    "v_sub_f32 v27, v27, v8\n"
    "v_sub_f32 v28, v28, v8\n"
    "v_sub_f32 v29, v29, v8\n"
    "v_sub_f32 v30, v30, v8\n"
    "v_sub_f32 v31, v31, v8\n"
    "v_exp_f32 v16, v16\n"
    "v_exp_f32 v17, v17\n"
    "v_exp_f32 v18, v18\n"
    "v_exp_f32 v19, v19\n"
    "v_exp_f32 v20, v20\n"
    "v_exp_f32 v21, v21\n"
    "v_exp_f32 v22, v22\n"
    "v_exp_f32 v23, v23\n"
    "v_exp_f32 v24, v24\n"
    "v_exp_f32 v25, v25\n"
    "v_exp_f32 v26, v26\n"
    "v_exp_f32 v27, v27\n"
    "v_exp_f32 v28, v28\n"
    "v_exp_f32 v29, v29\n"
    "v_exp_f32 v30, v30\n"
    "v_exp_f32 v31, v31\n"
    "v_mul_f32 v112, v112, v9\n"
    "v_mul_f32 v113, v113, v9\n"
    "v_mul_f32 v114, v114, v9\n"
    "v_mul_f32 v115, v115, v9\n"
    "v_mul_f32 v116, v116, v9\n"
    "v_mul_f32 v117, v117, v9\n"
    "v_mul_f32 v118, v118, v9\n"
    "v_mul_f32 v119, v119, v9\n"
    "v_mul_f32 v120, v120, v9\n"
    "v_mul_f32 v121, v121, v9\n"
    "v_mul_f32 v122, v122, v9\n"
    "v_mul_f32 v123, v123, v9\n"
    "v_mul_f32 v124, v124, v9\n"
    "v_mul_f32 v125, v125, v9\n"
    "v_mul_f32 v126, v126, v9\n"
    "v_mul_f32 v127, v127, v9\n"
    "v_cvt_pk_bf16_f32 v48, v16, v17\n"
    "v_cvt_pk_bf16_f32 v49, v18, v19\n"
    "v_cvt_pk_bf16_f32 v50, v20, v21\n"
    "v_cvt_pk_bf16_f32 v51, v22, v23\n"
    "v_cvt_pk_bf16_f32 v52, v24, v25\n"
    "v_cvt_pk_bf16_f32 v53, v26, v27\n"
    "v_cvt_pk_bf16_f32 v54, v28, v29\n"
    "v_cvt_pk_bf16_f32 v55, v30, v31\n"
    "v_mfma_f32_32x32x16_bf16 v[112:127], v[136:139], v[48:51], v[112:127]\n"
    "s_nop 0\n"
    "v_mfma_f32_32x32x16_bf16 v[112:127], v[140:143], v[52:55], v[112:127]\n"
    "s_add_u32 s51, s51, 0x4000\n"
    "s_cmp_lt_u32 s51, 0x40000\n"
    "s_nop 8\n"
    "s_cbranch_scc1 Lfbloop_40%=\n"
    "s_add_u32 s52, s18, 0\n"
    "s_and_b32 s52, s52, 3\n"
    "s_lshl_b32 s52, s52, 10\n"
    "s_add_u32 s52, s52, 143360\n"
    "v_lshlrev_b32 v8, 2, v1\n"
    "v_add_u32 v16, s52, v8\n"
    "s_lshl_b32 s44, s18, 8\n"
    "v_add_u32 v8, s44, v16\n"
    "ds_write_b32 v8, v12\n"
    "s_add_u32 s52, s18, 1\n"
    "s_and_b32 s52, s52, 3\n"
    "s_lshl_b32 s52, s52, 10\n"
    "s_add_u32 s52, s52, 143360\n"
    "v_lshlrev_b32 v8, 2, v1\n"
    "v_add_u32 v17, s52, v8\n"
    "s_lshl_b32 s44, s18, 8\n"
    "v_add_u32 v8, s44, v17\n"
    "ds_write_b32 v8, v13\n"
    "s_add_u32 s52, s18, 2\n"
    "s_and_b32 s52, s52, 3\n"
    "s_lshl_b32 s52, s52, 10\n"
    "s_add_u32 s52, s52, 143360\n"
    "v_lshlrev_b32 v8, 2, v1\n"
    "v_add_u32 v18, s52, v8\n"
    "s_lshl_b32 s44, s18, 8\n"
    "v_add_u32 v8, s44, v18\n"
    "ds_write_b32 v8, v14\n"
    "s_add_u32 s52, s18, 3\n"
    "s_and_b32 s52, s52, 3\n"
    "s_lshl_b32 s52, s52, 10\n"
    "s_add_u32 s52, s52, 143360\n"
    "v_lshlrev_b32 v8, 2, v1\n"
    "v_add_u32 v19, s52, v8\n"
    "s_lshl_b32 s44, s18, 8\n"
    "v_add_u32 v8, s44, v19\n"
    "ds_write_b32 v8, v15\n"
    "s_waitcnt lgkmcnt(0)\n"
    "s_barrier\n"
    "ds_read_b32 v20, v16 offset:0\n"
    "ds_read_b32 v21, v16 offset:256\n"
    "ds_read_b32 v22, v16 offset:512\n"
    "ds_read_b32 v23, v16 offset:768\n"
    "s_waitcnt lgkmcnt(1)\n"
    "v_max3_f32 v8, v20, v21, v22\n"
    "s_waitcnt lgkmcnt(0)\n"
    "v_max_f32 v8, v8, v23\n"
    "v_sub_f32 v8, v12, v8\n"
    "v_exp_f32 v8, v8\n"
    "s_nop 0\n"
    "v_mul_f32 v64, v64, v8\n"
    "v_mul_f32 v65, v65, v8\n"
    "v_mul_f32 v66, v66, v8\n"
    "v_mul_f32 v67, v67, v8\n"
    "v_mul_f32 v68, v68, v8\n"
    "v_mul_f32 v69, v69, v8\n"
    "v_mul_f32 v70, v70, v8\n"
    "v_mul_f32 v71, v71, v8\n"
    "v_mul_f32 v72, v72, v8\n"
    "v_mul_f32 v73, v73, v8\n"
    "v_mul_f32 v74, v74, v8\n"
    "v_mul_f32 v75, v75, v8\n"
    "v_mul_f32 v76, v76, v8\n"
    "v_mul_f32 v77, v77, v8\n"
    "v_mul_f32 v78, v78, v8\n"
    "v_mul_f32 v79, v79, v8\n"
    "ds_read_b32 v20, v17 offset:0\n"
    "ds_read_b32 v21, v17 offset:256\n"
    "ds_read_b32 v22, v17 offset:512\n"
    "ds_read_b32 v23, v17 offset:768\n"
    "s_waitcnt lgkmcnt(1)\n"
    "v_max3_f32 v8, v20, v21, v22\n"
    "s_waitcnt lgkmcnt(0)\n"
    "v_max_f32 v8, v8, v23\n"
    "v_sub_f32 v8, v13, v8\n"
    "v_exp_f32 v8, v8\n"
    "s_nop 0\n"
    "v_mul_f32 v80, v80, v8\n"
    "v_mul_f32 v81, v81, v8\n"
    "v_mul_f32 v82, v82, v8\n"
    "v_mul_f32 v83, v83, v8\n"
    "v_mul_f32 v84, v84, v8\n"
    "v_mul_f32 v85, v85, v8\n"
    "v_mul_f32 v86, v86, v8\n"
    "v_mul_f32 v87, v87, v8\n"
    "v_mul_f32 v88, v88, v8\n"
    "v_mul_f32 v89, v89, v8\n"
    "v_mul_f32 v90, v90, v8\n"
    "v_mul_f32 v91, v91, v8\n"
    "v_mul_f32 v92, v92, v8\n"
    "v_mul_f32 v93, v93, v8\n"
    "v_mul_f32 v94, v94, v8\n"
    "v_mul_f32 v95, v95, v8\n"
    "ds_read_b32 v20, v18 offset:0\n"
    "ds_read_b32 v21, v18 offset:256\n"
    "ds_read_b32 v22, v18 offset:512\n"
    "ds_read_b32 v23, v18 offset:768\n"
    "s_waitcnt lgkmcnt(1)\n"
    "v_max3_f32 v8, v20, v21, v22\n"
    "s_waitcnt lgkmcnt(0)\n"
    "v_max_f32 v8, v8, v23\n"
    "v_sub_f32 v8, v14, v8\n"
    "v_exp_f32 v8, v8\n"
    "s_nop 0\n"
    "v_mul_f32 v96, v96, v8\n"
    "v_mul_f32 v97, v97, v8\n"
    "v_mul_f32 v98, v98, v8\n"
    "v_mul_f32 v99, v99, v8\n"
    "v_mul_f32 v100, v100, v8\n"
    "v_mul_f32 v101, v101, v8\n"
    "v_mul_f32 v102, v102, v8\n"
    "v_mul_f32 v103, v103, v8\n"
    "v_mul_f32 v104, v104, v8\n"
    "v_mul_f32 v105, v105, v8\n"
    "v_mul_f32 v106, v106, v8\n"
    "v_mul_f32 v107, v107, v8\n"
    "v_mul_f32 v108, v108, v8\n"
    "v_mul_f32 v109, v109, v8\n"
    "v_mul_f32 v110, v110, v8\n"
    "v_mul_f32 v111, v111, v8\n"
    "ds_read_b32 v20, v19 offset:0\n"
    "ds_read_b32 v21, v19 offset:256\n"
    "ds_read_b32 v22, v19 offset:512\n"
    "ds_read_b32 v23, v19 offset:768\n"
    "s_waitcnt lgkmcnt(1)\n"
    "v_max3_f32 v8, v20, v21, v22\n"
    "s_waitcnt lgkmcnt(0)\n"
    "v_max_f32 v8, v8, v23\n"
    "v_sub_f32 v8, v15, v8\n"
    "v_exp_f32 v8, v8\n"
    "s_nop 0\n"
    "v_mul_f32 v112, v112, v8\n"
    "v_mul_f32 v113, v113, v8\n"
    "v_mul_f32 v114, v114, v8\n"
    "v_mul_f32 v115, v115, v8\n"
    "v_mul_f32 v116, v116, v8\n"
    "v_mul_f32 v117, v117, v8\n"
    "v_mul_f32 v118, v118, v8\n"
    "v_mul_f32 v119, v119, v8\n"
    "v_mul_f32 v120, v120, v8\n"
    "v_mul_f32 v121, v121, v8\n"
    "v_mul_f32 v122, v122, v8\n"
    "v_mul_f32 v123, v123, v8\n"
    "v_mul_f32 v124, v124, v8\n"
    "v_mul_f32 v125, v125, v8\n"
    "v_mul_f32 v126, v126, v8\n"
    "v_mul_f32 v127, v127, v8\n"
    "ds_write_b128 v251, v[80:83] offset:0\n"
    "ds_write_b128 v251, v[84:87] offset:1024\n"
    "ds_write_b128 v251, v[88:91] offset:2048\n"
    "ds_write_b128 v251, v[92:95] offset:3072\n"
    "ds_write_b128 v252, v[96:99] offset:0\n"
    "ds_write_b128 v252, v[100:103] offset:1024\n"
    "ds_write_b128 v252, v[104:107] offset:2048\n"
    "ds_write_b128 v252, v[108:111] offset:3072\n"
    "ds_write_b128 v253, v[112:115] offset:0\n"
    "ds_write_b128 v253, v[116:119] offset:1024\n"
    "ds_write_b128 v253, v[120:123] offset:2048\n"
    "ds_write_b128 v253, v[124:127] offset:3072\n"
    "v_mov_b32 v8, 0\n"
    "v_mov_b32 v9, 0\n"
    "v_mov_b32 v10, 0\n"
    "v_mov_b32 v11, 0\n"
    "s_waitcnt lgkmcnt(0)\n"
    "s_barrier\n"
    "s_branch Ljoin_19%=\n"
    "Lend_28%=:\n"
    "Lend%=:\n"
    : "+{v0}"(tid), "+{s[0:1]}"(ka), "+{s2}"(bid)
    :
    : "v1", "v2", "v3", "v4", "v5", "v6", "v7", "v8", "v9", "v10", "v11", "v12", "v13", "v14", "v15", "v16", "v17", "v18", "v19", "v20", "v21", "v22", "v23", "v24", "v25", "v26", "v27", "v28", "v29", "v30", "v31", "v32", "v33", "v34", "v35", "v36", "v37", "v38", "v39", "v40", "v41", "v42", "v43", "v44", "v45", "v46", "v47", "v48", "v49", "v50", "v51", "v52", "v53", "v54", "v55", "v56", "v57", "v58", "v59", "v60", "v61", "v62", "v63", "v64", "v65", "v66", "v67", "v68", "v69", "v70", "v71", "v72", "v73", "v74", "v75", "v76", "v77", "v78", "v79", "v80", "v81", "v82", "v83", "v84", "v85", "v86", "v87", "v88", "v89", "v90", "v91", "v92", "v93", "v94", "v95", "v96", "v97", "v98", "v99", "v100", "v101", "v102", "v103", "v104", "v105", "v106", "v107", "v108", "v109", "v110", "v111", "v112", "v113", "v114", "v115", "v116", "v117", "v118", "v119", "v120", "v121", "v122", "v123", "v124", "v125", "v126", "v127", "v128", "v129", "v130", "v131", "v132", "v133", "v134", "v135", "v136", "v137", "v138", "v139", "v140", "v141", "v142", "v143", "v144", "v145", "v146", "v147", "v148", "v149", "v150", "v151", "v152", "v153", "v154", "v155", "v156", "v157", "v158", "v159", "v160", "v161", "v162", "v163", "v164", "v165", "v166", "v167", "v168", "v169", "v170", "v171", "v172", "v173", "v174", "v175", "v176", "v177", "v178", "v179", "v180", "v181", "v182", "v183", "v184", "v185", "v186", "v187", "v188", "v189", "v190", "v191", "v192", "v193", "v194", "v195", "v196", "v197", "v198", "v199", "v200", "v201", "v202", "v203", "v204", "v205", "v206", "v207", "v208", "v209", "v210", "v211", "v212", "v213", "v214", "v215", "v216", "v217", "v218", "v219", "v220", "v221", "v222", "v223", "v224", "v225", "v226", "v227", "v228", "v229", "v230", "v231", "v232", "v233", "v234", "v235", "v236", "v237", "v238", "v239", "v240", "v241", "v242", "v243", "v244", "v245", "v246", "v247", "v248", "v249", "v250", "v251", "v252", "v253", "v254", "v255", "a0", "a1", "a2", "a3", "a4", "a5", "a6", "a7", "a8", "a9", "a10", "a11", "a12", "a13", "a14", "a15", "a16", "a17", "a18", "a19", "a20", "a21", "a22", "a23", "a24", "a25", "a26", "a27", "a28", "a29", "a30", "a31", "a32", "a33", "a34", "a35", "a36", "a37", "a38", "a39", "a40", "a41", "a42", "a43", "a44", "a45", "a46", "a47", "a48", "a49", "a50", "a51", "a52", "a53", "a54", "a55", "a56", "a57", "a58", "a59", "a60", "a61", "a62", "a63", "a64", "a65", "a66", "a67", "a68", "a69", "a70", "a71", "a72", "a73", "a74", "a75", "a76", "a77", "a78", "a79", "a80", "a81", "a82", "a83", "a84", "a85", "a86", "a87", "a88", "a89", "a90", "a91", "a92", "a93", "a94", "a95", "a96", "a97", "a98", "a99", "a100", "a101", "a102", "a103", "a104", "a105", "a106", "a107", "a108", "a109", "a110", "a111", "a112", "a113", "a114", "a115", "a116", "a117", "a118", "a119", "a120", "a121", "a122", "a123", "a124", "a125", "a126", "a127", "a128", "a129", "a130", "a131", "a132", "a133", "a134", "a135", "a136", "a137", "a138", "a139", "a140", "a141", "a142", "a143", "a144", "a145", "a146", "a147", "a148", "a149", "a150", "a151", "a152", "a153", "a154", "a155", "a156", "a157", "a158", "a159", "a160", "a161", "a162", "a163", "a164", "a165", "a166", "a167", "a168", "a169", "a170", "a171", "a172", "a173", "a174", "a175", "a176", "a177", "a178", "a179", "a180", "a181", "a182", "a183", "a184", "a185", "a186", "a187", "a188", "a189", "a190", "a191", "a192", "a193", "a194", "a195", "a196", "a197", "a198", "a199", "a200", "a201", "a202", "a203", "a204", "a205", "a206", "a207", "a208", "a209", "a210", "a211", "a212", "a213", "a214", "a215", "a216", "a217", "a218", "a219", "a220", "a221", "a222", "a223", "a224", "a225", "a226", "a227", "a228", "a229", "a230", "a231", "a232", "a233", "a234", "a235", "a236", "a237", "a238", "a239", "a240", "a241", "a242", "a243", "a244", "a245", "a246", "a247", "a248", "a249", "a250", "a251", "a252", "a253", "a254", "a255", "s3", "s4", "s5", "s6", "s7", "s8", "s9", "s10", "s11", "s12", "s13", "s14", "s15", "s16", "s17", "s18", "s19", "s20", "s21", "s22", "s23", "s24", "s25", "s26", "s27", "s28", "s29", "s30", "s31", "s33", "s34", "s35", "s36", "s37", "s38", "s39", "s40", "s41", "s42", "s43", "s44", "s45", "s46", "s47", "s48", "s49", "s50", "s51", "s52", "s53", "s54", "s55", "s56", "s57", "s58", "s59", "s60", "s61", "s62", "s63", "s64", "s65", "s66", "s67", "s68", "s69", "s70", "s71", "s72", "s73", "s74", "s75", "s76", "s77", "s78", "s79", "s80", "s81", "s82", "s83", "s84", "s85", "s86", "s87", "s88", "s89", "s90", "s91", "s92", "s93", "s94", "s95", "s96", "s97", "s98", "s99", "vcc", "scc", "memory");
}


extern "C" void kernel_launch(void* const* d_in, const int* in_sizes, int n_in, void* d_out, int out_size, void* d_ws, size_t ws_size, hipStream_t stream) {
  (void)hipFuncSetAttribute((const void*)adaln_block, hipFuncAttributeMaxDynamicSharedMemorySize, 163840);

  adaln_block<<<256, 256, 163840, stream>>>(d_in[0], d_in[1], d_in[2], d_in[4], d_in[5], d_in[6], d_in[7], d_in[8], d_ws, d_out);

}
```
